```python
import torch

def _softmax(x, axis=-1):
    return torch.softmax(x, dim=axis)
def _gelu(x):
    return torch.nn.functional.gelu(x, approximate="tanh")
def _silu(x):
    return torch.nn.functional.silu(x)
def _sigmoid(x):
    return torch.sigmoid(x)
def _layernorm(x, axis=-1, eps=1e-5):
    m = x.mean(dim=axis, keepdim=True)
    v = x.var(dim=axis, keepdim=True, unbiased=False)
    return (x - m) / torch.sqrt(v + eps)
def _scatter_add(tgt, idx, src, axis=0):
    return tgt.clone().index_add_(axis, idx, src)
def _segment_sum(src, seg, n):
    out = torch.zeros((n,) + src.shape[1:], dtype=src.dtype)
    return out.index_add_(0, seg, src)
def _pad(x, pw):
    flat = tuple(v for pair in reversed(pw) for v in pair)
    return torch.nn.functional.pad(x, flat)

kernel_name = 'sequential_preLN__sliding_block_cached__moe_arch_mod__decode'

def setup_inputs():

    _w = (lambda a: (lambda t: t.to(torch.bfloat16) if t.is_floating_point() else t)(torch.as_tensor(a)))
    def _A_setup():
        B, Sq, D = 8, 1, 576
        Hq, Hkv, Sk, Dh = 32, 16, 4096, 128
        assert Hq % Hkv == 0, (Hq, Hkv)
        return {
            "x": _w((lambda *s: torch.randn(*s, dtype=torch.bfloat16) * 0.05)(B, Sq, D)),
            "k_cache": _w((lambda *s: torch.randn(*s, dtype=torch.bfloat16) * 3.2)(B, Hkv, Sk, Dh)),
            "v_cache": _w((lambda *s: torch.randn(*s, dtype=torch.bfloat16) * 0.4)(B, Hkv, Sk, Dh)),
            "wq": _w((lambda *s: torch.randn(*s, dtype=torch.bfloat16) * 3.2)(D, Hq * Dh) * (D ** -0.5)),
            "wo": _w((lambda *s: torch.randn(*s, dtype=torch.bfloat16) * 0.4)(Hq * Dh, D) * ((Hq * Dh) ** -0.5)),
        }
    _A_in = _A_setup()

    def _B_setup():
        B, S, D, F, C = 8, 1, 576, 4608, 1
        return {
            "x": _w((lambda *s: torch.randn(*s, dtype=torch.bfloat16) * 0.05)(B, S, D)),
            "w_r": _w((lambda *s: torch.randn(*s, dtype=torch.bfloat16) * 0.4)(D)),
            "w_gate": _w((lambda *s: torch.randn(*s, dtype=torch.bfloat16) * 0.05)(D, F)),
            "w_up": _w((lambda *s: torch.randn(*s, dtype=torch.bfloat16) * 0.05)(D, F)),
            "w_down": _w((lambda *s: torch.randn(*s, dtype=torch.bfloat16) * 0.05)(F, D)),
        }
    _B_in = _B_setup()

    return {
        "A_x": _A_in["x"],
        "A_k_cache": _A_in["k_cache"],
        "A_v_cache": _A_in["v_cache"],
        "A_wq": _A_in["wq"],
        "A_wo": _A_in["wo"],
        "B_x": _B_in["x"],
        "B_w_r": _B_in["w_r"],
        "B_w_gate": _B_in["w_gate"],
        "B_w_up": _B_in["w_up"],
        "B_w_down": _B_in["w_down"],
    }

def reference(A_x, A_k_cache, A_v_cache, A_wq, A_wo, B_x, B_w_r, B_w_gate, B_w_up, B_w_down):
    def _A_ref(x, k_cache, v_cache, wq, wo):
        B, Sq, D = x.shape
        Hq, Dh = 32, 128
        Hkv, Sk = k_cache.shape[1], k_cache.shape[2]
        W = min(4096, max(256, Sk // 4))
        rep = Hq // Hkv
        q = (lambda x, *d: x.permute(*d))((x @ wq).reshape(B, Sq, Hq, Dh), 0, 2, 1, 3)
        k = (lambda x, n, axis: torch.repeat_interleave(x, n, dim=axis))(k_cache[:, :, Sk - W:, :], rep, axis=1)
        v = (lambda x, n, axis: torch.repeat_interleave(x, n, dim=axis))(v_cache[:, :, Sk - W:, :], rep, axis=1)
        scores = torch.einsum("bhqd,bhkd->bhqk", q, k) * (Dh ** -0.5)
        p = _softmax(scores, axis=-1)
        o = torch.einsum("bhqk,bhkd->bhqd", p, v)
        return (lambda x, *d: x.permute(*d))(o, 0, 2, 1, 3).reshape(B, Sq, Hq * Dh) @ wo

    def _B_ref(x, w_r, w_gate, w_up, w_down):
        B, S, D = x.shape
        C = 1
        r = x @ w_r
        idx = torch.argsort(-r, axis=-1)[:, :C]
        idx_e = (lambda x, n, axis: torch.repeat_interleave(x, n, dim=axis))(idx.reshape(B, C, 1), D, axis=2)
        x_sel = (lambda x, i, axis: torch.take_along_dim(x, i, dim=axis))(x, idx_e, axis=1)
        g = _silu(x_sel @ w_gate)
        u = x_sel @ w_up
        y = (g * u) @ w_down
        r_sel = _sigmoid((lambda x, i, axis: torch.take_along_dim(x, i, dim=axis))(r, idx, axis=1))
        y = y * r_sel.reshape(B, C, 1)
        b_off = (torch.arange(B) * S).reshape(B, 1)
        flat_idx = (idx + b_off).reshape(B * C)
        zero = (x * 0.0).reshape(B * S, D)
        upd = _scatter_add(zero, flat_idx, y.reshape(B * C, D), axis=0)
        return x + upd.reshape(B, S, D)

    x = A_x
    h = x + _A_ref(_layernorm(x, axis=-1), A_k_cache, A_v_cache, A_wq, A_wo)
    h2 = h + _B_ref(_layernorm(h, axis=-1), B_w_r, B_w_gate, B_w_up, B_w_down)
    return h2
```

```cpp
#include <hip/hip_runtime.h>
#include <hip/hip_bf16.h>
#include <stdint.h>

typedef unsigned short u16;
__device__ __forceinline__ float bf2f(u16 b) { return __uint_as_float(((unsigned)b) << 16); }
__device__ __forceinline__ float rbf(float f) {
  unsigned u = __float_as_uint(f); u += 0x7FFFu + ((u >> 16) & 1u); return __uint_as_float(u & 0xFFFF0000u); }
__device__ __forceinline__ u16 f2bf(float f) { unsigned u = __float_as_uint(f); u += 0x7FFFu + ((u >> 16) & 1u); return (u16)(u >> 16); }

constexpr int B = 8, D = 576, HQ = 32, HKV = 16, SK = 4096, DH = 128, W = 1024, F = 4608;

__device__ void ln_row(const float* src, float* dst, int tid, int nthr) {
  float s = 0.f; for (int i = 0; i < D; ++i) s += src[i];
  float mean = s / D; float vs = 0.f; for (int i = 0; i < D; ++i) { float d = src[i] - mean; vs += d * d; }
  float var = rbf(vs / D); float m = rbf(mean);
  float sd = rbf(sqrtf(rbf(var + 1e-5f)));
  for (int i = tid; i < D; i += nthr) dst[i] = rbf(rbf(src[i] - m) / sd);
}

__global__ void k_q(const u16* x, const u16* wq, float* q) {
  __shared__ float xs[B][D], xn[B][D];
  int tid = threadIdx.x;
  for (int i = tid; i < B * D; i += 256) xs[i / D][i % D] = bf2f(x[i]);
  __syncthreads();
  for (int b = 0; b < B; ++b) ln_row(xs[b], xn[b], tid, 256);
  __syncthreads();
  int n = blockIdx.x * 256 + tid;
  float acc[B]; for (int b = 0; b < B; ++b) acc[b] = 0.f;
  for (int k = 0; k < D; ++k) { float w = bf2f(wq[(size_t)k * (HQ * DH) + n]); for (int b = 0; b < B; ++b) acc[b] += xn[b][k] * w; }
  for (int b = 0; b < B; ++b) q[b * HQ * DH + n] = rbf(acc[b]);
}

__global__ void k_attn(const float* q, const u16* kc, const u16* vc, float* o) {
  __shared__ float qs[2][DH], sc[2][W], red[2][256];
  int kvh = blockIdx.x, b = blockIdx.y, tid = threadIdx.x;
  qs[tid >> 7][tid & 127] = q[b * HQ * DH + kvh * 256 + tid];
  __syncthreads();
  const u16* kp = kc + ((size_t)(b * HKV + kvh) * SK + (SK - W)) * DH;
  const u16* vp = vc + ((size_t)(b * HKV + kvh) * SK + (SK - W)) * DH;
  for (int j = tid; j < W; j += 256) {
    float a0 = 0.f, a1 = 0.f;
    for (int d = 0; d < DH; ++d) { float kk = bf2f(kp[(size_t)j * DH + d]); a0 += qs[0][d] * kk; a1 += qs[1][d] * kk; }
    sc[0][j] = rbf(rbf(a0) * 0.08838834764831845f); sc[1][j] = rbf(rbf(a1) * 0.08838834764831845f);
  }
  __syncthreads();
  for (int h = 0; h < 2; ++h) {
    float m = -INFINITY; for (int j = 0; j < W; ++j) m = fmaxf(m, sc[h][j]);
    float l = 0.f; for (int j = 0; j < W; ++j) l += expf(sc[h][j] - m);
    __syncthreads();
    for (int j = tid; j < W; j += 256) sc[h][j] = rbf(expf(sc[h][j] - m) / l);
    __syncthreads();
  }
  int h = tid >> 7, d = tid & 127; float acc = 0.f;
  for (int j = 0; j < W; ++j) acc += sc[h][j] * bf2f(vp[(size_t)j * DH + d]);
  o[b * HQ * DH + kvh * 256 + tid] = rbf(acc);
}

__global__ void k_wo(const float* o, const u16* wo, const u16* x, float* h) {
  __shared__ float red[8][B][64];
  int c = threadIdx.x & 63, ks = threadIdx.x >> 6; int n = blockIdx.x * 64 + c;
  float acc[B]; for (int b = 0; b < B; ++b) acc[b] = 0.f;
  for (int k = ks * 512; k < ks * 512 + 512; ++k) { float w = bf2f(wo[(size_t)k * D + n]); for (int b = 0; b < B; ++b) acc[b] += o[b * 4096 + k] * w; }
  for (int b = 0; b < B; ++b) red[ks][b][c] = acc[b];
  __syncthreads();
  int b = ks; float s = 0.f; for (int i = 0; i < 8; ++i) s += red[i][b][c];
  h[b * D + n] = rbf(bf2f(x[b * D + n]) + rbf(s));
}

__global__ void k_gu(const float* h, const u16* wg, const u16* wu, const u16* wr, float* act, float* hn_out, float* rs) {
  __shared__ float hs[B][D], hn[B][D];
  int tid = threadIdx.x;
  for (int i = tid; i < B * D; i += 256) hs[i / D][i % D] = h[i];
  __syncthreads();
  for (int b = 0; b < B; ++b) ln_row(hs[b], hn[b], tid, 256);
  __syncthreads();
  int n = blockIdx.x * 256 + tid;
  float ag[B], au[B]; for (int b = 0; b < B; ++b) ag[b] = au[b] = 0.f;
  for (int k = 0; k < D; ++k) { float g = bf2f(wg[(size_t)k * F + n]), u = bf2f(wu[(size_t)k * F + n]); for (int b = 0; b < B; ++b) { ag[b] += hn[b][k] * g; au[b] += hn[b][k] * u; } }
  for (int b = 0; b < B; ++b) { float g = rbf(ag[b]); g = rbf(g / (1.f + expf(-g))); act[b * F + n] = rbf(g * rbf(au[b])); }
  if (blockIdx.x == 0) {
    for (int i = tid; i < B * D; i += 256) hn_out[i] = hn[i / D][i % D];
    if (tid < B) { float r = 0.f; for (int k = 0; k < D; ++k) r += hn[tid][k] * bf2f(wr[k]); r = rbf(r); rs[tid] = rbf(1.f / (1.f + expf(-r))); }
  }
}

__global__ void k_dn(const float* act, const u16* wd, const float* h, const float* hn, const float* rs, u16* out) {
  __shared__ float red[8][B][64];
  int c = threadIdx.x & 63, ks = threadIdx.x >> 6; int n = blockIdx.x * 64 + c;
  float acc[B]; for (int b = 0; b < B; ++b) acc[b] = 0.f;
  for (int k = ks * 576; k < ks * 576 + 576; ++k) { float w = bf2f(wd[(size_t)k * D + n]); for (int b = 0; b < B; ++b) acc[b] += act[b * F + k] * w; }
  for (int b = 0; b < B; ++b) red[ks][b][c] = acc[b];
  __syncthreads();
  int b = ks; float s = 0.f; for (int i = 0; i < 8; ++i) s += red[i][b][c];
  float y = rbf(rbf(s) * rs[b]);
  float bo = rbf(hn[b * D + n] + y);
  out[b * D + n] = f2bf(h[b * D + n] + bo);
}

extern "C" void kernel_launch(void* const* d_in, const int* in_sizes, int n_in, void* d_out, int out_size, void* d_ws, size_t ws_size, hipStream_t stream) {
  const u16* x = (const u16*)d_in[0]; const u16* kc = (const u16*)d_in[1]; const u16* vc = (const u16*)d_in[2];
  const u16* wq = (const u16*)d_in[3]; const u16* wo = (const u16*)d_in[4];
  const u16* wr = (const u16*)d_in[6]; const u16* wg = (const u16*)d_in[7]; const u16* wu = (const u16*)d_in[8]; const u16* wd = (const u16*)d_in[9];
  float* ws = (float*)d_ws;
  float* q = ws; float* o = q + 8 * 4096; float* h = o + 8 * 4096; float* act = h + 8 * 576; float* hn = act + 8 * F; float* rs = hn + 8 * 576;
  k_q<<<16, 256, 0, stream>>>(x, wq, q);
  k_attn<<<dim3(HKV, B), 256, 0, stream>>>(q, kc, vc, o);
  k_wo<<<9, 512, 0, stream>>>(o, wo, x, h);
  k_gu<<<18, 256, 0, stream>>>(h, wg, wu, wr, act, hn, rs);
  k_dn<<<9, 512, 0, stream>>>(act, wd, h, hn, rs, (u16*)d_out);
}
```

```cpp
#include <hip/hip_runtime.h>
#include <stdint.h>

typedef unsigned u32;
typedef unsigned short u16;
typedef u32 u32x4 __attribute__((ext_vector_type(4)));
typedef u32 u32x2 __attribute__((ext_vector_type(2)));
typedef float f32x4 __attribute__((ext_vector_type(4)));
typedef float f32x2 __attribute__((ext_vector_type(2)));
typedef short s16x4 __attribute__((ext_vector_type(4)));
typedef __bf16 bf16x8 __attribute__((ext_vector_type(8)));
typedef __bf16 bf16x2 __attribute__((ext_vector_type(2)));
#define GAS __attribute__((address_space(1)))
#define LAS __attribute__((address_space(3)))

#define STAMPS 0
#define LWIN 20
#define GWIN 40
#define QDELAY 8
#define PSLEEP 1
#define QS_T4 1
#define OS_B 1

namespace {

constexpr int DM = 576, HKV = 16, SK = 4096, DH = 128, WIN = 1024, NQ = 4096, FF = 4608;
constexpr int NWG = 256, NTHR = 576;

constexpr int WS_EPOCH = 0;
constexpr int WS_QS = 4096;
constexpr int WS_SC = WS_QS + 16 * 8 * 4 * 256 * 4;
constexpr int WS_OS = WS_SC + 256 * 128;
constexpr int WS_PW = WS_OS + 16 * 8 * 2 * 256 * 4;
constexpr int WS_HS = WS_PW + 8 * 9 * 16 * 64 * 4;
constexpr int WS_AS = WS_HS + 8 * 576 * 4;
constexpr int WS_PD = WS_AS + 8 * 4608 * 4;
constexpr int WS_END = WS_PD + 8 * 9 * 16 * 64 * 4;
constexpr int WS_STAMP = (WS_END + 4095) / 4096 * 4096;
constexpr int AUX_SC1 = 16;
constexpr unsigned SPIN_CAP = 1u << 16, LSPIN_CAP = 1u << 22;

constexpr int L_EXT = 128 * 1024;
constexpr int L_FL = 144 * 1024;
constexpr int L_ST = L_FL + 128;
constexpr int L_P = L_ST + 256;
constexpr int L_QSUM = L_P + 2048;
constexpr int L_Q = L_QSUM + 8192;
constexpr int L_HN = L_P;
constexpr int HN_STRIDE = 292;
constexpr int LDS_BYTES = 160 * 1024;
static_assert(L_Q + 512 <= LDS_BYTES && L_HN + 8 * HN_STRIDE * 4 <= LDS_BYTES, "LDS");
enum { F_LANDED = 0, F_QPUB = 1, F_BAR = 2, F_QRDY = 3, F_SRDY = 4, F_KDONE = 8, F_VDONE = 16 };

struct Params {
  const u16 *x, *kc, *vc, *wq, *wo, *wr, *wg, *wu, *wd;
  u16* out;
  unsigned char* ws;
};

__device__ __forceinline__ float bflo(u32 d) { return __uint_as_float(d << 16); }
__device__ __forceinline__ float bfhi(u32 d) { return __uint_as_float(d & 0xFFFF0000u); }
__device__ __forceinline__ float rbf(float f) { return (float)(__bf16)f; }
__device__ __forceinline__ u32 bfbits_hi(float f) { return __float_as_uint(rbf(f)); }
__device__ __forceinline__ u32 pack2(float lo, float hi) {
  bf16x2 v = {(__bf16)lo, (__bf16)hi};
  return __builtin_bit_cast(u32, v);
}
template <int CTRL> __device__ __forceinline__ float dpp(float x) {
  return __uint_as_float((u32)__builtin_amdgcn_mov_dpp((int)__float_as_uint(x), CTRL, 0xf, 0xf, true));
}
constexpr int XOR1 = 0xB1, XOR2 = 0x4E, XOR7 = 0x141, XOR8 = 0x128;
__device__ __forceinline__ float swap32_add(float a, float b) {
  auto r = __builtin_amdgcn_permlane32_swap(__float_as_uint(a), __float_as_uint(b), false, false);
  return __uint_as_float(r[0]) + __uint_as_float(r[1]);
}
__device__ __forceinline__ float swap16_add(float a, float b) {
  auto r = __builtin_amdgcn_permlane16_swap(__float_as_uint(a), __float_as_uint(b), false, false);
  return __uint_as_float(r[0]) + __uint_as_float(r[1]);
}
__device__ __forceinline__ float wave_sum(float x) {
  x += dpp<XOR1>(x); x += dpp<XOR2>(x); x += dpp<XOR7>(x); x += dpp<XOR8>(x);
  x = swap16_add(x, x); x = swap32_add(x, x);
  return x;
}
__device__ __forceinline__ float fmax_(float a, float b) { return __builtin_amdgcn_fmed3f(a, b, INFINITY); }
__device__ __forceinline__ float wave_max(float x) {
  x = fmax_(x, dpp<XOR1>(x)); x = fmax_(x, dpp<XOR2>(x)); x = fmax_(x, dpp<XOR7>(x)); x = fmax_(x, dpp<XOR8>(x));
  auto r = __builtin_amdgcn_permlane16_swap(__float_as_uint(x), __float_as_uint(x), false, false);
  x = fmax_(__uint_as_float(r[0]), __uint_as_float(r[1]));
  auto t = __builtin_amdgcn_permlane32_swap(__float_as_uint(x), __float_as_uint(x), false, false);
  return fmax_(__uint_as_float(t[0]), __uint_as_float(t[1]));
}
__device__ __forceinline__ f32x4 mfma(u32x4 a, u32x4 b, f32x4 c) {
  return __builtin_amdgcn_mfma_f32_16x16x32_bf16(__builtin_bit_cast(bf16x8, a), __builtin_bit_cast(bf16x8, b), c, 0, 0, 0);
}
#define SB() __builtin_amdgcn_sched_barrier(0)
template <int N> __device__ __forceinline__ void waitvm() { asm volatile("s_waitcnt vmcnt(%0)" ::"n"(N) : "memory"); }
typedef __amdgpu_buffer_rsrc_t rsrc_t;
__device__ __forceinline__ u32x4 ld16c(rsrc_t rs, int off) { return __builtin_amdgcn_raw_buffer_load_b128(rs, off, 0, AUX_SC1); }
__device__ __forceinline__ u32 ld4c(rsrc_t rs, int off) { return __builtin_amdgcn_raw_buffer_load_b32(rs, off, 0, AUX_SC1); }
__device__ __forceinline__ void st16c(rsrc_t rs, int off, u32x4 v) { __builtin_amdgcn_raw_buffer_store_b128(v, rs, off, 0, AUX_SC1); }
template <u32 M> __device__ __forceinline__ bool tag_ok(u32x4 v, u32 t) { return ((((v.x ^ t) | (v.y ^ t)) | ((v.z ^ t) | (v.w ^ t))) & M) == 0u; }
template <u32 M> __device__ __forceinline__ u32 put_tag(float f, u32 t) { return ((__float_as_uint(f) + (M + 1) / 2) & ~M) | t; }
template <u32 M> __device__ __forceinline__ float get_tag(u32 d) { return __uint_as_float(d & ~M); }
#define POLL_COUNT(n)
#define POLL(cond_load) do { unsigned spins_ = 0; _Pragma("clang loop unroll(disable)") while (!(cond_load) && ++spins_ < SPIN_CAP) __builtin_amdgcn_s_sleep(PSLEEP); POLL_COUNT(spins_); } while (0)

__device__ __forceinline__ u32 lds_ld(u32 addr) {
  u32 v;
  asm volatile("ds_read_b32 %0, %1\n\ts_waitcnt lgkmcnt(0)" : "=v"(v) : "v"(addr) : "memory");
  return v;
}
__device__ __forceinline__ void lds_st(u32 addr, u32 v) { asm volatile("ds_write_b32 %0, %1" ::"v"(addr), "v"(v) : "memory"); }
__device__ __forceinline__ void wait_word(int word, u32 want) {
  unsigned spins = 0;
#pragma clang loop unroll(disable)
  while ((u32)__builtin_amdgcn_readfirstlane(lds_ld(L_FL + 4 * word)) < want)
    if (++spins > LSPIN_CAP) break;
}
__device__ __forceinline__ void wait_word_sleep(int word, u32 want) {
  unsigned spins = 0;
#pragma clang loop unroll(disable)
  while ((u32)__builtin_amdgcn_readfirstlane(lds_ld(L_FL + 4 * word)) < want) {
    __builtin_amdgcn_s_sleep(2);
    if (++spins > LSPIN_CAP) break;
  }
}
__device__ __forceinline__ void wait_all8(int word0, u32 want, int lane) {
  unsigned spins = 0;
#pragma clang loop unroll(disable)
  while (!__all(lds_ld(L_FL + 4 * (word0 + (lane & 7))) >= want)) {
    __builtin_amdgcn_s_sleep(1);
    if (++spins > LSPIN_CAP) break;
  }
}
__device__ __forceinline__ void cbar(u32 n, int lane) {
  asm volatile("s_waitcnt lgkmcnt(0)" ::: "memory");
  if (lane == 0) asm volatile("ds_add_u32 %0, %1" ::"v"((u32)(L_FL + 4 * F_BAR)), "v"(1u) : "memory");
  wait_word(F_BAR, 8u * n);
}
__device__ __forceinline__ void hw_barrier() { __builtin_amdgcn_sched_barrier(0); __builtin_amdgcn_s_barrier(); __builtin_amdgcn_sched_barrier(0); }
__device__ __forceinline__ void wg_barrier() {
  asm volatile("s_waitcnt lgkmcnt(0)" ::: "memory");
  hw_barrier();
  asm volatile("" ::: "memory");
}
__device__ __forceinline__ void glds16(const void* sbase, u32 voff, u32 lds_dst) {
  asm volatile("s_mov_b32 m0, %2\n\ts_nop 0\n\tglobal_load_lds_dwordx4 %0, %1 nt" ::"v"(voff), "s"(sbase), "s"(lds_dst) : "memory");
}
__device__ __forceinline__ int vswz(int row) { return ((row & 3) << 2) | ((row >> 2) & 3); }
__device__ __forceinline__ int swz(int row) { return ((row >> 1) & 1) | ((row >> 2) & 2); }
__device__ __forceinline__ u32 rowoff(int q, int stride, int lane) { const int row = 8 * q + (lane >> 3); return row * stride + (((lane & 7) ^ (swz(row) << 1)) & 7) * 16; }
template <int W> __device__ __forceinline__ void tile_dma(const unsigned char* a, int stride, u32 o0, u32 o1, u32 dst) {
  glds16(a, o0, dst); if (W >= 0) waitvm<W>();
  glds16(a, o1, dst + 1024); if (W >= 0) waitvm<W>();
  glds16(a + (size_t)16 * stride, o0, dst + 2048); if (W >= 0) waitvm<W>();
  glds16(a + (size_t)16 * stride, o1, dst + 3072); if (W >= 0) waitvm<W>();
}
__device__ __forceinline__ void tile_frags_lds(const unsigned char* lds, u32 base, int lane, u32x4 (&f)[4]) {
  const int g = lane >> 4, q = (lane & 15) >> 2, p = lane & 3;
#pragma unroll
  for (int e = 0; e < 4; ++e) {
    u32x2 h[2];
#pragma unroll
    for (int t = 0; t < 2; ++t) {
      const int row = 8 * g + 4 * t + q;
      const u32 a = base + row * 128 + (((2 * e + (p >> 1)) ^ (swz(row) << 1)) << 4) + 8 * (p & 1);
      h[t] = __builtin_bit_cast(u32x2, __builtin_amdgcn_ds_read_tr16_b64_v4i16((LAS s16x4*)(lds + a)));
    }
    f[e] = u32x4{h[0].x, h[0].y, h[1].x, h[1].y};
  }
}

#define ESTAMP(k) do { } while (0)
#define STAMP(k) do { } while (0)
#define STAMP_FLUSH() do { } while (0)

__device__ __forceinline__ u32x4 afrag(u32x4 a, u32x4 b) {
  u32x4 f;
  f.x = __builtin_amdgcn_perm(a.y, a.x, 0x07060302u);
  f.y = __builtin_amdgcn_perm(a.w, a.z, 0x07060302u);
  f.z = __builtin_amdgcn_perm(b.y, b.x, 0x07060302u);
  f.w = __builtin_amdgcn_perm(b.w, b.z, 0x07060302u);
  return f;
}

struct RowStats { float m, rs; };
template <int N> __device__ __forceinline__ RowStats row_stats(const float (&v)[N], const bool (&valid)[N], float pivot) {
  float s1[2] = {0.f, 0.f}, s2[2] = {0.f, 0.f};
#pragma unroll
  for (int e = 0; e < N; ++e) { const float d = valid[e] ? v[e] - pivot : 0.f; s1[e & 1] += d; s2[e & 1] = fmaf(d, d, s2[e & 1]); }
  const float m1 = wave_sum(s1[0] + s1[1]) * (1.0f / DM), m2 = wave_sum(s2[0] + s2[1]) * (1.0f / DM);
  const float var = rbf(fmax_(fmaf(-m1, m1, m2), 0.f));
  RowStats r;
  r.m = rbf(pivot + m1);
  r.rs = __builtin_amdgcn_rcpf(rbf(__builtin_amdgcn_sqrtf(rbf(var + 1e-5f))));
  return r;
}
__device__ __forceinline__ float ln_apply(float v, RowStats s) { return rbf(v - s.m) * s.rs; }

enum { ROLE_W = 0, ROLE_G = 1, ROLE_F = 2 };

__global__ void __launch_bounds__(NTHR, 3) fused_kernel(Params p) {
  extern __shared__ __attribute__((aligned(16))) unsigned char lds[];
  const int tid = threadIdx.x, lane = tid & 63, w = __builtin_amdgcn_readfirstlane(tid >> 6);
  const int wg = blockIdx.x, xcd = wg & 7, s = wg >> 3, j = xcd + 8 * (s >> 4), i = s & 15, b = i >> 1, half = i & 1;
  const int cidx = (s >> 4) * 7 + (i - 9);
  const int role = i < 9 ? ROLE_W : (cidx < 9 ? ROLE_G : ROLE_F);
  const size_t ubase = ((size_t)(b * HKV + j) * SK + (SK - WIN)) * DH;
  const int cq = i & 3, kp = i >> 2;
  const int ks0 = kp < 3 ? 4 * kp : 13, nt = kp < 2 ? 4 : 5;
  const u32 PK = 4 * nt, PV = PK + 128, PW = PK + 256;
  const int g4 = lane >> 4, c16 = lane & 15;
#define RSTAMP(k)
  STAMP(0);
  const int cw = (w + 7) & 7;
  u32 xr[5];
  u32x4 xa;
  {
    const GAS u32* xrow = (const GAS u32*)(p.x + cw * DM);
#pragma unroll
    for (int m = 0; m < 4; ++m) xr[m] = __builtin_nontemporal_load(xrow + lane + 64 * m);
    xr[4] = __builtin_nontemporal_load(xrow + (lane & 31) + 256);
    xa = __builtin_nontemporal_load((const GAS u32x4*)(p.x + (lane & 7) * DM + 32 * (ks0 + (cw < nt ? cw : 0)) + 8 * g4));
  }
  if (w == 0) {
    if (lane < 32) lds_st(L_FL + 4 * lane, 0u);
    asm volatile("s_waitcnt lgkmcnt(0)" ::: "memory");
  }
  hw_barrier();

  if (w == 0) {
    __builtin_amdgcn_s_setprio(3);
    u32 pub = 0;
#define PUBLISH(W) lds_st(L_FL + 4 * F_LANDED, pub - (W))
    {
      const unsigned char* a = (const unsigned char*)(p.wq + (size_t)32 * ks0 * NQ + 256 * j + 64 * cq);
      const u32 o0 = rowoff(0, NQ * 2, lane), o1 = rowoff(1, NQ * 2, lane);
#pragma unroll
      for (int t = 0; t < 4; ++t) tile_dma<-1>(a + (size_t)t * 32 * NQ * 2, NQ * 2, o0, o1, L_EXT + t * 4096);
      if (nt == 5) tile_dma<-1>(a + (size_t)4 * 32 * NQ * 2, NQ * 2, o0, o1, L_QSUM);
      pub = PK;
    }
    ESTAMP(8);
    {
      const unsigned char* kb = (const unsigned char*)(p.kc + ubase + (size_t)half * 512 * DH);
      u32 koff[4];
#pragma unroll
      for (int q = 0; q < 4; ++q) { const int key = 4 * q + g4; koff[q] = key * 256 + ((c16 ^ key) & 15) * 16; }
#pragma clang loop unroll(disable)
      for (int t = 0; t < 32; ++t) {
#pragma unroll
        for (int q = 0; q < 4; ++q) { glds16(kb + t * 4096, koff[q], (t * 4 + q) * 1024); waitvm<LWIN>(); }
        pub += 4;
        if (pub > LWIN) PUBLISH(LWIN);
      }
    }
    STAMP(1);
    {
      const unsigned char* vb = (const unsigned char*)(p.vc + ubase + (size_t)half * 512 * DH);
      u32 voff[4];
#pragma unroll
      for (int q = 0; q < 4; ++q) { const int key = 4 * q + g4; voff[q] = key * 256 + ((c16 ^ vswz(key)) & 15) * 16; }
      wait_word(F_QPUB, 1u);
#pragma clang loop unroll(disable)
      for (int t = 0; t < 32; ++t) {
        if ((t & 7) == 4) wait_all8(F_KDONE, (t >> 3) + 1, lane);
        const u32 dst = t < 4 ? L_EXT + t * 4096 : (t - 4) * 4096;
#pragma unroll
        for (int q = 0; q < 4; ++q) { glds16(vb + t * 4096, voff[q], dst + q * 1024); waitvm<LWIN>(); }
        pub += 4; PUBLISH(LWIN);
      }
    }
    STAMP(2);
    if (role == ROLE_W) {
      const unsigned char* a = (const unsigned char*)(p.wo + (size_t)256 * j * DM + 64 * i);
      const u32 o0 = rowoff(0, DM * 2, lane), o1 = rowoff(1, DM * 2, lane);
      wait_all8(F_VDONE, 1, lane);
#pragma clang loop unroll(disable)
      for (int t = 0; t < 8; ++t) { tile_dma<LWIN>(a + (size_t)t * 32 * DM * 2, DM * 2, o0, o1, t * 4096); pub += 4; PUBLISH(LWIN); }
    } else if (role == ROLE_G) {
      const int gx = xcd * 9 + cidx;
      const u32 o0 = rowoff(0, FF * 2, lane), o1 = rowoff(1, FF * 2, lane);
#pragma clang loop unroll(disable)
      for (int t = 0; t < 36; ++t) {
        if (t == 0 || t == 16) wait_all8(F_VDONE, (t >> 4) + 1, lane);
        const unsigned char* a = (const unsigned char*)(((t & 1) ? p.wu : p.wg) + (size_t)(t >> 1) * 32 * FF + 64 * gx);
        tile_dma<GWIN>(a, FF * 2, o0, o1, t < 4 ? L_EXT + t * 4096 : (t - 4) * 4096);
        pub += 4;
        if (4 * t + 4 > GWIN - LWIN) PUBLISH(GWIN);
      }
    }
    STAMP(3);
#define DR(N) if (GWIN > N && (role == ROLE_G || LWIN > N)) { waitvm<N>(); PUBLISH(N); }
    DR(44) DR(40) DR(36) DR(32) DR(28) DR(24) DR(20) DR(16) DR(12) DR(8) DR(4) DR(0)
#undef DR
#undef PUBLISH
    STAMP(14);
    STAMP_FLUSH();
    return;
  }

  const int ct = tid - 64;
  const rsrc_t rs = __builtin_amdgcn_make_buffer_rsrc((void*)p.ws, 0, WS_END, 0x00020000);
  const u32 T = *(const __attribute__((address_space(4))) u32*)(p.ws + WS_EPOCH) + 1u;
  const u32 T16 = T & 0xFFFFu, T8 = T & 0xFFu, T4 = T & 0xFu;
  float* const lf = (float*)lds;
  u32* const lu = (u32*)lds;
  {
    ESTAMP(8);
    asm volatile("" :: "v"(xr[0]), "v"(xr[4]));
    ESTAMP(9);
    float v[10]; bool valid[10];
#pragma unroll
    for (int m = 0; m < 5; ++m) { valid[2 * m] = valid[2 * m + 1] = (m < 4) || (lane < 32); v[2 * m] = bflo(xr[m]); v[2 * m + 1] = bfhi(xr[m]); }
    const float pivot = __uint_as_float(__builtin_amdgcn_readfirstlane(xr[0]) << 16);
    const RowStats st = row_stats<10>(v, valid, pivot);
    if (lane == 0) *(f32x2*)(lf + L_ST / 4 + 2 * cw) = f32x2{st.m, st.rs};
    ESTAMP(10);
    cbar(1, lane);
    STAMP(1);
  }
  if (cw < nt) {
    const f32x2 sv = *(const f32x2*)(lf + L_ST / 4 + 2 * (lane & 7));
    const RowStats st = {sv.x, sv.y};
    u32x4 a;
#pragma unroll
    for (int q = 0; q < 4; ++q) a[q] = pack2(ln_apply(bflo(xa[q]), st), ln_apply(bfhi(xa[q]), st));
    const u32 base = cw < 4 ? L_EXT + cw * 4096 : L_QSUM;
    wait_word(F_LANDED, 4 * (cw + 1));
    u32x4 f[4]; tile_frags_lds(lds, base, lane, f);
    f32x4 d[4];
#pragma unroll
    for (int e = 0; e < 4; ++e) d[e] = mfma(f[e], a, f32x4{0.f, 0.f, 0.f, 0.f});
    if (c16 < 8) {
#pragma unroll
      for (int e = 0; e < 4; ++e) *(f32x4*)(lf + base / 4 + c16 * 64 + 16 * e + 4 * g4) = d[e];
    }
  }
  ESTAMP(11);
  cbar(2, lane);
  ESTAMP(12);
  {
    float sum = (lf[L_EXT / 4 + cw * 64 + lane] + lf[L_EXT / 4 + 1024 + cw * 64 + lane]) + (lf[L_EXT / 4 + 2048 + cw * 64 + lane] + lf[L_EXT / 4 + 3072 + cw * 64 + lane]);
    if (nt == 5) sum += lf[L_QSUM / 4 + cw * 64 + lane];
    const u32 v = put_tag<0xFu>(sum, T4);
    const u32x4 o = {v, (u32)__builtin_amdgcn_mov_dpp((int)v, 0x55, 0xf, 0xf, true), (u32)__builtin_amdgcn_mov_dpp((int)v, 0xAA, 0xf, 0xf, true),
                     (u32)__builtin_amdgcn_mov_dpp((int)v, 0xFF, 0xf, 0xf, true)};
    if ((lane & 3) == 0) st16c(rs, WS_QS + ((((j * 8 + cw) * 4 + kp) * 256) + 64 * cq + lane) * 4, o);
  }
  cbar(3, lane);
  if (ct == 0) lds_st(L_FL + 4 * F_QPUB, 1u);
  STAMP(2);
  if (cw >= 2 && cw < 6) {
    u32x4 qg;
    __builtin_amdgcn_s_sleep(QDELAY);
    {
      unsigned spins = 0;
#pragma clang loop unroll(disable)
      for (;;) {
        qg = ld16c(rs, WS_QS + (((j * 8 + b) * 4 + (cw - 2)) * 256 + 4 * lane) * 4);
        if (tag_ok<0xFu>(qg, T4) || ++spins > SPIN_CAP) break;
        __builtin_amdgcn_s_sleep(4);
      }
    }
    *(f32x4*)(lf + L_QSUM / 4 + (cw - 2) * 256 + 4 * lane) = f32x4{get_tag<0xFu>(qg.x), get_tag<0xFu>(qg.y), get_tag<0xFu>(qg.z), get_tag<0xFu>(qg.w)};
  }
  cbar(4, lane);
  if (cw == 0) {
    f32x4 sv[4];
#pragma unroll
    for (int q = 0; q < 4; ++q) sv[q] = *(const f32x4*)(lf + L_QSUM / 4 + q * 256 + 4 * lane);
    const f32x4 sum = (sv[0] + sv[1]) + (sv[2] + sv[3]);
    *(u32x2*)(lu + L_Q / 4 + 2 * lane) = u32x2{pack2(sum.x, sum.y), pack2(sum.z, sum.w)};
    asm volatile("s_waitcnt lgkmcnt(0)" ::: "memory");
    if (lane == 0) lds_st(L_FL + 4 * F_QRDY, 1u);
  } else {
    wait_word(F_QRDY, 1u);
  }
  STAMP(3);
  float s0, s1;
  {
    u32x4 qa[4];
#pragma unroll
    for (int ks = 0; ks < 4; ++ks) qa[ks] = *(const u32x4*)(lu + L_Q / 4 + (lane & 1) * 64 + 16 * ks + 4 * g4);
    f32x4 d[4];
#pragma unroll
    for (int m = 0; m < 4; ++m) {
      const int tile = cw + 8 * m;
      wait_word_sleep(F_LANDED, PK + 4 * (tile + 1));
      u32x4 kf[4];
#pragma unroll
      for (int ks = 0; ks < 4; ++ks) kf[ks] = *(const u32x4*)(lds + tile * 4096 + c16 * 256 + (((4 * ks + g4) ^ c16) << 4));
      f32x4 acc = mfma(qa[0], kf[0], f32x4{0.f, 0.f, 0.f, 0.f});
      acc = mfma(qa[1], kf[1], acc); acc = mfma(qa[2], kf[2], acc); acc = mfma(qa[3], kf[3], acc);
      d[m] = acc;
      asm volatile("s_waitcnt lgkmcnt(0)" ::: "memory");
      if (lane == 0) lds_st(L_FL + 4 * (F_KDONE + cw), m + 1);
    }
    const float x0 = g4 == 0 ? d[0][0] : g4 == 1 ? d[1][0] : g4 == 2 ? d[2][0] : d[3][0];
    const float x1 = g4 == 0 ? d[0][1] : g4 == 1 ? d[1][1] : g4 == 2 ? d[2][1] : d[3][1];
    constexpr float SCALE = 0.08838834764831845f;
    s0 = rbf(rbf(x0) * SCALE); s1 = rbf(rbf(x1) * SCALE);
  }
  STAMP(4);
  {
    constexpr float L2E = 1.4426950408889634f;
    const float m0 = wave_max(s0), m1 = wave_max(s1);
    const float l0 = wave_sum(__builtin_amdgcn_exp2f((s0 - m0) * L2E)), l1 = wave_sum(__builtin_amdgcn_exp2f((s1 - m1) * L2E));
    if (lane == 0) *(f32x4*)(lf + L_ST / 4 + 16 + 4 * cw) = f32x4{m0, m1, l0, l1};
    cbar(5, lane);
    float M0 = -INFINITY, M1 = -INFINITY;
    f32x4 st[8];
#pragma unroll
    for (int ww = 0; ww < 8; ++ww) { st[ww] = *(const f32x4*)(lf + L_ST / 4 + 16 + 4 * ww); M0 = fmax_(M0, st[ww].x); M1 = fmax_(M1, st[ww].y); }
    float L0 = 0.f, L1 = 0.f;
#pragma unroll
    for (int ww = 0; ww < 8; ++ww) {
      L0 = fmaf(st[ww].z, __builtin_amdgcn_exp2f((st[ww].x - M0) * L2E), L0);
      L1 = fmaf(st[ww].w, __builtin_amdgcn_exp2f((st[ww].y - M1) * L2E), L1);
    }
    const u32x4 mine = {__float_as_uint(M0) | T16, __float_as_uint(M1) | T16, put_tag<0xFFu>(L0, T8), put_tag<0xFFu>(L1, T8)};
    u32x4 oth;
    if (cw == 1) {
      if (lane == 0) st16c(rs, WS_SC + wg * 128, mine);
      unsigned spins = 0;
#pragma clang loop unroll(disable)
      for (;;) {
        oth = ld16c(rs, WS_SC + (wg ^ 8) * 128);
        if (__all(((((oth.x ^ T16) | (oth.y ^ T16)) & 0xFFFFu) | (((oth.z ^ T8) | (oth.w ^ T8)) & 0xFFu)) == 0u) || ++spins > SPIN_CAP) break;
        __builtin_amdgcn_s_sleep(4);
      }
      if (lane == 0) { *(u32x4*)(lu + L_ST / 4 + 48) = oth; asm volatile("s_waitcnt lgkmcnt(0)" ::: "memory"); lds_st(L_FL + 4 * F_SRDY, 1u); }
    } else {
      wait_word_sleep(F_SRDY, 1u);
      oth = *(const u32x4*)(lu + L_ST / 4 + 48);
    }
    STAMP(5);
    const float om0 = bfhi(oth.x), om1 = bfhi(oth.y);
    const float G0 = fmax_(M0, om0), G1 = fmax_(M1, om1);
    const float S0 = get_tag<0xFFu>(mine.z) * __builtin_amdgcn_exp2f((M0 - G0) * L2E) + get_tag<0xFFu>(oth.z) * __builtin_amdgcn_exp2f((om0 - G0) * L2E);
    const float S1 = get_tag<0xFFu>(mine.w) * __builtin_amdgcn_exp2f((M1 - G1) * L2E) + get_tag<0xFFu>(oth.w) * __builtin_amdgcn_exp2f((om1 - G1) * L2E);
    const int kl = 16 * (cw + 8 * g4) + c16;
    __bf16* P = (__bf16*)(lds + L_P);
    P[kl] = (__bf16)(__builtin_amdgcn_exp2f((s0 - G0) * L2E) * (1.0f / S0));
    P[512 + kl] = (__bf16)(__builtin_amdgcn_exp2f((s1 - G1) * L2E) * (1.0f / S1));
  }
  cbar(6, lane);
  STAMP(6);
  {
    f32x4 d[8];
#pragma unroll
    for (int e = 0; e < 8; ++e) d[e] = f32x4{0.f, 0.f, 0.f, 0.f};
    const int q4 = (lane & 15) >> 2, p4 = lane & 3;
#pragma unroll
    for (int m = 0; m < 2; ++m) {
      const int ks = cw + 8 * m;
      const u32x4 pf = *(const u32x4*)(lds + L_P + (lane & 1) * 1024 + (32 * ks + 8 * g4) * 2);
      const u32 base = ks < 2 ? L_EXT + ks * 8192 : (ks - 2) * 8192;
      wait_word(F_LANDED, PV + 8 * (ks + 1));
      u32x4 f[8];
#pragma unroll
      for (int e = 0; e < 8; ++e) {
        u32x2 h[2];
#pragma unroll
        for (int t = 0; t < 2; ++t) {
          const int row = 8 * g4 + 4 * t + q4;
          const u32 a = base + row * 256 + (((2 * e + (p4 >> 1)) ^ vswz(row)) << 4) + 8 * (p4 & 1);
          h[t] = __builtin_bit_cast(u32x2, __builtin_amdgcn_ds_read_tr16_b64_v4i16((LAS s16x4*)(lds + a)));
        }
        f[e] = u32x4{h[0].x, h[0].y, h[1].x, h[1].y};
      }
      SB();
#pragma unroll
      for (int e = 0; e < 8; ++e) d[e] = mfma(f[e], pf, d[e]);
      asm volatile("s_waitcnt lgkmcnt(0)" ::: "memory");
      if (lane == 0) lds_st(L_FL + 4 * (F_VDONE + cw), m + 1);
    }
    if (c16 < 2) {
#pragma unroll
      for (int e = 0; e < 8; ++e) *(f32x4*)(lf + L_QSUM / 4 + cw * 256 + c16 * 128 + 16 * e + 4 * g4) = d[e];
    }
  }
  cbar(7, lane);
  auto gather4 = [&](u32 v) -> u32x4 {
    return u32x4{v, (u32)__builtin_amdgcn_mov_dpp((int)v, 0x55, 0xf, 0xf, true), (u32)__builtin_amdgcn_mov_dpp((int)v, 0xAA, 0xf, 0xf, true),
                 (u32)__builtin_amdgcn_mov_dpp((int)v, 0xFF, 0xf, 0xf, true)};
  };
  if (cw < 4) {
    float sv[8];
#pragma unroll
    for (int ww = 0; ww < 8; ++ww) sv[ww] = lf[L_QSUM / 4 + ww * 256 + ct];
    const float sum = ((sv[0] + sv[1]) + (sv[2] + sv[3])) + ((sv[4] + sv[5]) + (sv[6] + sv[7]));
    const u32x4 o = gather4(put_tag<0xFFu>(sum, T8));
    if ((lane & 3) == 0) st16c(rs, WS_OS + (((j * 8 + b) * 2 + half) * 256 + ct) * 4, o);
  }
  STAMP(7);

  auto acc_to_lds = [&](int base, const f32x4 (&d)[4]) {
    if (c16 < 8) {
#pragma unroll
      for (int e = 0; e < 4; ++e) *(f32x4*)(lf + base / 4 + (cw * 8 + c16) * 64 + 16 * e + 4 * g4) = d[e];
    }
  };
  auto publish_partial = [&](int base, int cg) {
    float sv[8];
#pragma unroll
    for (int ww = 0; ww < 8; ++ww) sv[ww] = lf[L_EXT / 4 + ww * 512 + cw * 64 + lane];
    const float sum = ((sv[0] + sv[1]) + (sv[2] + sv[3])) + ((sv[4] + sv[5]) + (sv[6] + sv[7]));
    const u32x4 o = gather4(put_tag<0xFFu>(sum, T8));
    if ((lane & 3) == 0) st16c(rs, base + (((cw * 9 + cg) * 16 + j) * 64 + lane) * 4, o);
  };

  if (role == ROLE_W) {
    const int cg = i;
    u32x4 f[4];
    wait_word(F_LANDED, PW + 32);
    tile_frags_lds(lds, cw * 4096, lane, f);
    {
      const int off = WS_OS + ((j * 8 + (lane & 7)) * 512 + 32 * cw + 8 * g4) * 4;
      u32x4 a0, a1, b0, b1;
      POLL((a0 = ld16c(rs, off), a1 = ld16c(rs, off + 16), b0 = ld16c(rs, off + 1024), b1 = ld16c(rs, off + 1040),
            tag_ok<0xFFu>(a0, T8) & tag_ok<0xFFu>(a1, T8) & tag_ok<0xFFu>(b0, T8) & tag_ok<0xFFu>(b1, T8)));
      STAMP(8);
      RSTAMP(0);
#define S2(x, y) (get_tag<0xFFu>(x) + get_tag<0xFFu>(y))
      const u32x4 a = {pack2(S2(a0.x, b0.x), S2(a0.y, b0.y)), pack2(S2(a0.z, b0.z), S2(a0.w, b0.w)), pack2(S2(a1.x, b1.x), S2(a1.y, b1.y)), pack2(S2(a1.z, b1.z), S2(a1.w, b1.w))};
#undef S2
      f32x4 d[4];
#pragma unroll
      for (int e = 0; e < 4; ++e) d[e] = mfma(f[e], a, f32x4{0.f, 0.f, 0.f, 0.f});
      acc_to_lds(L_EXT, d);
    }
    wg_barrier();
    publish_partial(WS_PW, cg);
    STAMP(9);
    u32x4 f2[4];
    {
      const unsigned char* a = (const unsigned char*)(p.wd + (size_t)(288 * j + 32 * cw) * DM + 64 * cg);
      const u32 o0 = rowoff(0, DM * 2, lane), o1 = rowoff(1, DM * 2, lane);
      tile_dma<-1>(a, DM * 2, o0, o1, (8 + cw) * 4096);
      if (cw == 0) tile_dma<-1>(a + 256 * DM * 2, DM * 2, o0, o1, 16 * 4096);
      waitvm<0>();
      tile_frags_lds(lds, (8 + cw) * 4096, lane, f);
      if (cw == 0) tile_frags_lds(lds, 16 * 4096, lane, f2);
    }
    {
      const int off = WS_AS + ((lane & 7) * FF + 288 * j + 32 * cw + 8 * g4) * 4;
      u32x4 a0, a1, a2, a3;
      if (cw == 0) POLL((a0 = ld16c(rs, off), a1 = ld16c(rs, off + 16), a2 = ld16c(rs, off + 1024), a3 = ld16c(rs, off + 1040),
                         tag_ok<0xFFFFu>(a0, T16) & tag_ok<0xFFFFu>(a1, T16) & tag_ok<0xFFFFu>(a2, T16) & tag_ok<0xFFFFu>(a3, T16)));
      else POLL((a0 = ld16c(rs, off), a1 = ld16c(rs, off + 16), tag_ok<0xFFFFu>(a0, T16) & tag_ok<0xFFFFu>(a1, T16)));
      STAMP(12);
      RSTAMP(1);
      const u32x4 a = afrag(a0, a1);
      f32x4 d[4];
#pragma unroll
      for (int e = 0; e < 4; ++e) d[e] = mfma(f[e], a, f32x4{0.f, 0.f, 0.f, 0.f});
      if (cw == 0) {
        const u32x4 ab = afrag(a2, a3);
#pragma unroll
        for (int e = 0; e < 4; ++e) d[e] = mfma(f2[e], ab, d[e]);
      }
      acc_to_lds(L_EXT, d);
    }
    wg_barrier();
    publish_partial(WS_PD, cg);
    STAMP(13);
  } else if (role == ROLE_G) {
    const int gx = xcd * 9 + cidx;
    u32x4 f[5][4];
#pragma unroll
    for (int tl = 0; tl < 5; ++tl) {
      if (tl < 4 || cw < 4) {
        const int tau = cw + 8 * tl;
        wait_word(F_LANDED, PW + 4 * (tau + 1));
        tile_frags_lds(lds, tau < 4 ? L_EXT + tau * 4096 : (tau - 4) * 4096, lane, f[tl]);
      }
    }
    wait_word(F_LANDED, PW + 144);
    STAMP(8);
    {
      const int off = WS_HS + cw * DM * 4;
      u32x4 h0, h1; u32 h2;
      POLL((h0 = ld16c(rs, off + lane * 32), h1 = ld16c(rs, off + lane * 32 + 16), h2 = ld4c(rs, off + 2048 + lane * 4),
            tag_ok<0xFFFFu>(h0, T16) & tag_ok<0xFFFFu>(h1, T16) & ((h2 & 0xFFFFu) == T16)));
      STAMP(10);
      RSTAMP(0);
      float v[9]; bool valid[9];
#pragma unroll
      for (int e = 0; e < 4; ++e) { v[e] = bfhi(h0[e]); v[4 + e] = bfhi(h1[e]); }
      v[8] = bfhi(h2);
#pragma unroll
      for (int e = 0; e < 9; ++e) valid[e] = true;
      const float pivot = __uint_as_float(__builtin_amdgcn_readfirstlane(h0.x) & 0xFFFF0000u);
      const RowStats st = row_stats<9>(v, valid, pivot);
      u32* hn = lu + L_HN / 4 + cw * HN_STRIDE;
      *(u32x4*)(hn + 4 * lane) = u32x4{pack2(ln_apply(v[0], st), ln_apply(v[1], st)), pack2(ln_apply(v[2], st), ln_apply(v[3], st)),
                                       pack2(ln_apply(v[4], st), ln_apply(v[5], st)), pack2(ln_apply(v[6], st), ln_apply(v[7], st))};
      ((__bf16*)(hn + 256))[lane] = (__bf16)ln_apply(v[8], st);
    }
    wg_barrier();
    {
      f32x4 d[4];
      const u32* hn = lu + L_HN / 4 + (lane & 7) * HN_STRIDE + 16 * (cw >> 1) + 4 * g4;
      u32x4 a[5];
#pragma unroll
      for (int tl = 0; tl < 5; ++tl) a[tl] = *(const u32x4*)(hn + (tl < 4 || cw < 4 ? 64 * tl : 0));
      SB();
#pragma unroll
      for (int e = 0; e < 4; ++e) d[e] = mfma(f[0][e], a[0], f32x4{0.f, 0.f, 0.f, 0.f});
#pragma unroll
      for (int tl = 1; tl < 5; ++tl) {
        if (tl < 4 || cw < 4) {
#pragma unroll
          for (int e = 0; e < 4; ++e) d[e] = mfma(f[tl][e], a[tl], d[e]);
        }
      }
      acc_to_lds(0, d);
    }
    wg_barrier();
    {
      float gv[4], uv[4];
#pragma unroll
      for (int ww = 0; ww < 4; ++ww) { gv[ww] = lf[(2 * ww) * 512 + cw * 64 + lane]; uv[ww] = lf[(2 * ww + 1) * 512 + cw * 64 + lane]; }
      const float gg = rbf((gv[0] + gv[1]) + (gv[2] + gv[3])), uu = rbf((uv[0] + uv[1]) + (uv[2] + uv[3]));
      const float sl = rbf(gg * __builtin_amdgcn_rcpf(1.0f + __builtin_amdgcn_exp2f(gg * -1.4426950408889634f)));
      const u32x4 o = gather4(bfbits_hi(sl * uu) | T16);
      if ((lane & 3) == 0) st16c(rs, WS_AS + (cw * FF + 64 * gx + lane) * 4, o);
    }
    STAMP(11);
  } else {
    const int f = xcd * 5 + (cidx - 9);
    const int job = 2 * f + (cw >> 2);
    if ((cw & 3) == 0 && job < 72) {
      const int jb = job / 9, cg = job % 9;
      const u32x2 xq = *(const GAS u32x2*)(p.x + jb * DM + 64 * cg + 4 * c16);
      const u32x4 wr0 = *(const GAS u32x4*)(p.wr + 8 * lane);
      const u16 wr1 = ((const GAS u16*)p.wr)[512 + lane];
      auto gather16 = [&](int base) -> f32x4 {
        const int off = base + (((jb * 9 + cg) * 16 + g4) * 64 + 4 * c16) * 4;
        u32x4 q0, q1, q2, q3;
        POLL((q0 = ld16c(rs, off), q1 = ld16c(rs, off + 1024), q2 = ld16c(rs, off + 2048), q3 = ld16c(rs, off + 3072),
              tag_ok<0xFFu>(q0, T8) & tag_ok<0xFFu>(q1, T8) & tag_ok<0xFFu>(q2, T8) & tag_ok<0xFFu>(q3, T8)));
        f32x4 sum;
#pragma unroll
        for (int e = 0; e < 4; ++e) {
          float t = (get_tag<0xFFu>(q0[e]) + get_tag<0xFFu>(q1[e])) + (get_tag<0xFFu>(q2[e]) + get_tag<0xFFu>(q3[e]));
          t = swap16_add(t, t); t = swap32_add(t, t);
          sum[e] = t;
        }
        return sum;
      };
      STAMP(3);
      const f32x4 at = gather16(WS_PW);
      STAMP(8);
      RSTAMP(0);
      f32x4 hv;
      hv[0] = rbf(bflo(xq.x) + rbf(at[0])); hv[1] = rbf(bfhi(xq.x) + rbf(at[1]));
      hv[2] = rbf(bflo(xq.y) + rbf(at[2])); hv[3] = rbf(bfhi(xq.y) + rbf(at[3]));
      if (lane < 16)
        st16c(rs, WS_HS + (jb * DM + 64 * cg + 4 * lane) * 4,
              u32x4{__float_as_uint(hv[0]) | T16, __float_as_uint(hv[1]) | T16, __float_as_uint(hv[2]) | T16, __float_as_uint(hv[3]) | T16});
      STAMP(9);
      RowStats st; float sig;
      {
        const int off = WS_HS + jb * DM * 4;
        u32x4 h0, h1; u32 h2;
        POLL((h0 = ld16c(rs, off + lane * 32), h1 = ld16c(rs, off + lane * 32 + 16), h2 = ld4c(rs, off + 2048 + lane * 4),
              tag_ok<0xFFFFu>(h0, T16) & tag_ok<0xFFFFu>(h1, T16) & ((h2 & 0xFFFFu) == T16)));
        float v[9]; bool valid[9];
#pragma unroll
        for (int e = 0; e < 4; ++e) { v[e] = bfhi(h0[e]); v[4 + e] = bfhi(h1[e]); }
        v[8] = bfhi(h2);
#pragma unroll
        for (int e = 0; e < 9; ++e) valid[e] = true;
        const float pivot = __uint_as_float(__builtin_amdgcn_readfirstlane(h0.x) & 0xFFFF0000u);
        st = row_stats<9>(v, valid, pivot);
        float r = rbf(ln_apply(v[8], st)) * bflo((u32)wr1);
#pragma unroll
        for (int e = 0; e < 8; ++e) r = fmaf(rbf(ln_apply(v[e], st)), (e & 1) ? bfhi(wr0[e >> 1]) : bflo(wr0[e >> 1]), r);
        r = rbf(wave_sum(r));
        sig = rbf(__builtin_amdgcn_rcpf(1.0f + __builtin_amdgcn_exp2f(r * -1.4426950408889634f)));
      }
      float hn[4];
#pragma unroll
      for (int e = 0; e < 4; ++e) hn[e] = rbf(ln_apply(hv[e], st));
      STAMP(10);
      const f32x4 y = gather16(WS_PD);
      STAMP(12);
      RSTAMP(1);
      float o[4];
#pragma unroll
      for (int e = 0; e < 4; ++e) o[e] = hv[e] + rbf(hn[e] + rbf(rbf(y[e]) * sig));
      if (lane < 16) *(GAS u32x2*)(p.out + jb * DM + 64 * cg + 4 * lane) = u32x2{pack2(o[0], o[1]), pack2(o[2], o[3])};
      if (job == 0 && lane == 0) __hip_atomic_store((GAS u32*)(p.ws + WS_EPOCH), T, __ATOMIC_RELAXED, __HIP_MEMORY_SCOPE_AGENT);
      STAMP(13);
    }
  }
  STAMP(14);
  STAMP_FLUSH();
}

}

extern "C" void kernel_launch(void* const* d_in, const int* in_sizes, int n_in, void* d_out, int out_size, void* d_ws, size_t ws_size,
                              hipStream_t stream) {
  Params p;
  p.x = (const u16*)d_in[0];
  p.kc = (const u16*)d_in[1];
  p.vc = (const u16*)d_in[2];
  p.wq = (const u16*)d_in[3];
  p.wo = (const u16*)d_in[4];
  p.wr = (const u16*)d_in[6];
  p.wg = (const u16*)d_in[7];
  p.wu = (const u16*)d_in[8];
  p.wd = (const u16*)d_in[9];
  p.out = (u16*)d_out;
  p.ws = (unsigned char*)d_ws;
  (void)hipFuncSetAttribute((const void*)fused_kernel, hipFuncAttributeMaxDynamicSharedMemorySize, LDS_BYTES);
  hipLaunchKernelGGL(fused_kernel, dim3(NWG), dim3(NTHR), LDS_BYTES, stream, p);
}
```

```cpp
#include <hip/hip_runtime.h>
#include <stdint.h>

typedef unsigned u32;
typedef unsigned short u16;
typedef u32 u32x4 __attribute__((ext_vector_type(4)));
typedef u32 u32x2 __attribute__((ext_vector_type(2)));
typedef float f32x4 __attribute__((ext_vector_type(4)));
typedef float f32x2 __attribute__((ext_vector_type(2)));
typedef short s16x4 __attribute__((ext_vector_type(4)));
typedef __bf16 bf16x8 __attribute__((ext_vector_type(8)));
typedef __bf16 bf16x2 __attribute__((ext_vector_type(2)));
#define GAS __attribute__((address_space(1)))
#define LAS __attribute__((address_space(3)))

#define STAMPS 0
#define LWIN 20
#define GWIN 40
#define QDELAY 8
#define PSLEEP 1
#define QS_T4 1
#define PROBE 0
#define OS_B 1

namespace {

constexpr int DM = 576, HKV = 16, SK = 4096, DH = 128, WIN = 1024, NQ = 4096, FF = 4608;
constexpr int NWG = 256, NTHR = 576;

constexpr int WS_EPOCH = 0;
constexpr int WS_QS = 4096;
constexpr int WS_SC = WS_QS + 16 * 8 * 4 * 256 * 4;
constexpr int WS_OS = WS_SC + 256 * 128;
constexpr int WS_PW = WS_OS + 16 * 8 * 2 * 256 * 4;
constexpr int WS_HS = WS_PW + 8 * 9 * 16 * 64 * 4;
constexpr int WS_AS = WS_HS + 8 * 576 * 4;
constexpr int WS_PD = WS_AS + 8 * 4608 * 4;
constexpr int WS_END = WS_PD + 8 * 9 * 16 * 64 * 4;
constexpr int WS_STAMP = (WS_END + 4095) / 4096 * 4096;
constexpr int AUX_SC1 = 16;
constexpr unsigned SPIN_CAP = 1u << 16, LSPIN_CAP = 1u << 22;

constexpr int L_EXT = 128 * 1024;
constexpr int L_FL = 144 * 1024;
constexpr int L_ST = L_FL + 128;
constexpr int L_P = L_ST + 256;
constexpr int L_QSUM = L_P + 2048;
constexpr int L_Q = L_QSUM + 8192;
constexpr int L_HN = L_P;
constexpr int HN_STRIDE = 292;
constexpr int LDS_BYTES = 160 * 1024;
static_assert(L_Q + 512 <= LDS_BYTES && L_HN + 8 * HN_STRIDE * 4 <= LDS_BYTES, "LDS");
enum { F_LANDED = 0, F_QPUB = 1, F_BAR = 2, F_QRDY = 3, F_SRDY = 4, F_KDONE = 8, F_VDONE = 16 };

struct Params {
  const u16 *x, *kc, *vc, *wq, *wo, *wr, *wg, *wu, *wd;
  u16* out;
  unsigned char* ws;
};

__device__ __forceinline__ float bflo(u32 d) { return __uint_as_float(d << 16); }
__device__ __forceinline__ float bfhi(u32 d) { return __uint_as_float(d & 0xFFFF0000u); }
__device__ __forceinline__ float rbf(float f) { return (float)(__bf16)f; }
__device__ __forceinline__ u32 bfbits_hi(float f) { return __float_as_uint(rbf(f)); }
__device__ __forceinline__ u32 pack2(float lo, float hi) {
  bf16x2 v = {(__bf16)lo, (__bf16)hi};
  return __builtin_bit_cast(u32, v);
}
template <int CTRL> __device__ __forceinline__ float dpp(float x) {
  return __uint_as_float((u32)__builtin_amdgcn_mov_dpp((int)__float_as_uint(x), CTRL, 0xf, 0xf, true));
}
constexpr int XOR1 = 0xB1, XOR2 = 0x4E, XOR7 = 0x141, XOR8 = 0x128;
__device__ __forceinline__ float swap32_add(float a, float b) {
  auto r = __builtin_amdgcn_permlane32_swap(__float_as_uint(a), __float_as_uint(b), false, false);
  return __uint_as_float(r[0]) + __uint_as_float(r[1]);
}
__device__ __forceinline__ float swap16_add(float a, float b) {
  auto r = __builtin_amdgcn_permlane16_swap(__float_as_uint(a), __float_as_uint(b), false, false);
  return __uint_as_float(r[0]) + __uint_as_float(r[1]);
}
__device__ __forceinline__ float wave_sum(float x) {
  x += dpp<XOR1>(x); x += dpp<XOR2>(x); x += dpp<XOR7>(x); x += dpp<XOR8>(x);
  x = swap16_add(x, x); x = swap32_add(x, x);
  return x;
}
__device__ __forceinline__ float fmax_(float a, float b) { return __builtin_amdgcn_fmed3f(a, b, INFINITY); }
__device__ __forceinline__ float wave_max(float x) {
  x = fmax_(x, dpp<XOR1>(x)); x = fmax_(x, dpp<XOR2>(x)); x = fmax_(x, dpp<XOR7>(x)); x = fmax_(x, dpp<XOR8>(x));
  auto r = __builtin_amdgcn_permlane16_swap(__float_as_uint(x), __float_as_uint(x), false, false);
  x = fmax_(__uint_as_float(r[0]), __uint_as_float(r[1]));
  auto t = __builtin_amdgcn_permlane32_swap(__float_as_uint(x), __float_as_uint(x), false, false);
  return fmax_(__uint_as_float(t[0]), __uint_as_float(t[1]));
}
__device__ __forceinline__ f32x4 mfma(u32x4 a, u32x4 b, f32x4 c) {
  return __builtin_amdgcn_mfma_f32_16x16x32_bf16(__builtin_bit_cast(bf16x8, a), __builtin_bit_cast(bf16x8, b), c, 0, 0, 0);
}
#define SB() __builtin_amdgcn_sched_barrier(0)
template <int N> __device__ __forceinline__ void waitvm() { asm volatile("s_waitcnt vmcnt(%0)" ::"n"(N) : "memory"); }
typedef __amdgpu_buffer_rsrc_t rsrc_t;
__device__ __forceinline__ u32x4 ld16c(rsrc_t rs, int off) { return __builtin_amdgcn_raw_buffer_load_b128(rs, off, 0, AUX_SC1); }
__device__ __forceinline__ u32 ld4c(rsrc_t rs, int off) { return __builtin_amdgcn_raw_buffer_load_b32(rs, off, 0, AUX_SC1); }
__device__ __forceinline__ void st16c(rsrc_t rs, int off, u32x4 v) { __builtin_amdgcn_raw_buffer_store_b128(v, rs, off, 0, AUX_SC1); }
template <u32 M> __device__ __forceinline__ bool tag_ok(u32x4 v, u32 t) { return ((((v.x ^ t) | (v.y ^ t)) | ((v.z ^ t) | (v.w ^ t))) & M) == 0u; }
template <u32 M> __device__ __forceinline__ u32 put_tag(float f, u32 t) { return ((__float_as_uint(f) + (M + 1) / 2) & ~M) | t; }
template <u32 M> __device__ __forceinline__ float get_tag(u32 d) { return __uint_as_float(d & ~M); }
#define POLL_COUNT(n)
#define POLL(cond_load) do { unsigned spins_ = 0; _Pragma("clang loop unroll(disable)") while (!(cond_load) && ++spins_ < SPIN_CAP) __builtin_amdgcn_s_sleep(PSLEEP); POLL_COUNT(spins_); } while (0)

__device__ __forceinline__ u32 lds_ld(u32 addr) {
  u32 v;
  asm volatile("ds_read_b32 %0, %1\n\ts_waitcnt lgkmcnt(0)" : "=v"(v) : "v"(addr) : "memory");
  return v;
}
__device__ __forceinline__ void lds_st(u32 addr, u32 v) { asm volatile("ds_write_b32 %0, %1" ::"v"(addr), "v"(v) : "memory"); }
__device__ __forceinline__ void wait_word(int word, u32 want) {
  unsigned spins = 0;
#pragma clang loop unroll(disable)
  while ((u32)__builtin_amdgcn_readfirstlane(lds_ld(L_FL + 4 * word)) < want)
    if (++spins > LSPIN_CAP) break;
}
__device__ __forceinline__ void wait_word_sleep(int word, u32 want) {
  unsigned spins = 0;
#pragma clang loop unroll(disable)
  while ((u32)__builtin_amdgcn_readfirstlane(lds_ld(L_FL + 4 * word)) < want) {
    __builtin_amdgcn_s_sleep(2);
    if (++spins > LSPIN_CAP) break;
  }
}
__device__ __forceinline__ void wait_all8(int word0, u32 want, int lane) {
  unsigned spins = 0;
#pragma clang loop unroll(disable)
  while (!__all(lds_ld(L_FL + 4 * (word0 + (lane & 7))) >= want)) {
    __builtin_amdgcn_s_sleep(1);
    if (++spins > LSPIN_CAP) break;
  }
}
__device__ __forceinline__ void cbar(u32 n, int lane) {
  asm volatile("s_waitcnt lgkmcnt(0)" ::: "memory");
  if (lane == 0) asm volatile("ds_add_u32 %0, %1" ::"v"((u32)(L_FL + 4 * F_BAR)), "v"(1u) : "memory");
  wait_word(F_BAR, 8u * n);
}
__device__ __forceinline__ void hw_barrier() { __builtin_amdgcn_sched_barrier(0); __builtin_amdgcn_s_barrier(); __builtin_amdgcn_sched_barrier(0); }
__device__ __forceinline__ void wg_barrier() {
  asm volatile("s_waitcnt lgkmcnt(0)" ::: "memory");
  hw_barrier();
  asm volatile("" ::: "memory");
}
__device__ __forceinline__ void glds16(const void* sbase, u32 voff, u32 lds_dst) {
  asm volatile("s_mov_b32 m0, %2\n\ts_nop 0\n\tglobal_load_lds_dwordx4 %0, %1 nt" ::"v"(voff), "s"(sbase), "s"(lds_dst) : "memory");
}
__device__ __forceinline__ int vswz(int row) { return ((row & 3) << 2) | ((row >> 2) & 3); }
__device__ __forceinline__ int swz(int row) { return ((row >> 1) & 1) | ((row >> 2) & 2); }
__device__ __forceinline__ u32 rowoff(int q, int stride, int lane) { const int row = 8 * q + (lane >> 3); return row * stride + (((lane & 7) ^ (swz(row) << 1)) & 7) * 16; }
template <int W> __device__ __forceinline__ void tile_dma(const unsigned char* a, int stride, u32 o0, u32 o1, u32 dst) {
  glds16(a, o0, dst); if (W >= 0) waitvm<W>();
  glds16(a, o1, dst + 1024); if (W >= 0) waitvm<W>();
  glds16(a + (size_t)16 * stride, o0, dst + 2048); if (W >= 0) waitvm<W>();
  glds16(a + (size_t)16 * stride, o1, dst + 3072); if (W >= 0) waitvm<W>();
}
__device__ __forceinline__ void tile_frags_lds(const unsigned char* lds, u32 base, int lane, u32x4 (&f)[4]) {
  const int g = lane >> 4, q = (lane & 15) >> 2, p = lane & 3;
#pragma unroll
  for (int e = 0; e < 4; ++e) {
    u32x2 h[2];
#pragma unroll
    for (int t = 0; t < 2; ++t) {
      const int row = 8 * g + 4 * t + q;
      const u32 a = base + row * 128 + (((2 * e + (p >> 1)) ^ (swz(row) << 1)) << 4) + 8 * (p & 1);
      h[t] = __builtin_bit_cast(u32x2, __builtin_amdgcn_ds_read_tr16_b64_v4i16((LAS s16x4*)(lds + a)));
    }
    f[e] = u32x4{h[0].x, h[0].y, h[1].x, h[1].y};
  }
}

#define ESTAMP(k) do { } while (0)
#define STAMP(k) do { } while (0)
#define STAMP_FLUSH() do { } while (0)

__device__ __forceinline__ u32x4 afrag(u32x4 a, u32x4 b) {
  u32x4 f;
  f.x = __builtin_amdgcn_perm(a.y, a.x, 0x07060302u);
  f.y = __builtin_amdgcn_perm(a.w, a.z, 0x07060302u);
  f.z = __builtin_amdgcn_perm(b.y, b.x, 0x07060302u);
  f.w = __builtin_amdgcn_perm(b.w, b.z, 0x07060302u);
  return f;
}

struct RowStats { float m, rs; };
template <int N> __device__ __forceinline__ RowStats row_stats(const float (&v)[N], const bool (&valid)[N], float pivot) {
  float s1[2] = {0.f, 0.f}, s2[2] = {0.f, 0.f};
#pragma unroll
  for (int e = 0; e < N; ++e) { const float d = valid[e] ? v[e] - pivot : 0.f; s1[e & 1] += d; s2[e & 1] = fmaf(d, d, s2[e & 1]); }
  const float m1 = wave_sum(s1[0] + s1[1]) * (1.0f / DM), m2 = wave_sum(s2[0] + s2[1]) * (1.0f / DM);
  const float var = rbf(fmax_(fmaf(-m1, m1, m2), 0.f));
  RowStats r;
  r.m = rbf(pivot + m1);
  r.rs = __builtin_amdgcn_rcpf(rbf(__builtin_amdgcn_sqrtf(rbf(var + 1e-5f))));
  return r;
}
__device__ __forceinline__ float ln_apply(float v, RowStats s) { return rbf(v - s.m) * s.rs; }

enum { ROLE_W = 0, ROLE_G = 1, ROLE_F = 2 };

__global__ void __launch_bounds__(NTHR, 3) fused_kernel(Params p) {
  extern __shared__ __attribute__((aligned(16))) unsigned char lds[];
  const int tid = threadIdx.x, lane = tid & 63, w = __builtin_amdgcn_readfirstlane(tid >> 6);
  const int wg = blockIdx.x, xcd = wg & 7, s = wg >> 3, j = xcd + 8 * (s >> 4), i = s & 15, b = i >> 1, half = i & 1;
  const int cidx = (s >> 4) * 7 + (i - 9);
  const int role = i < 9 ? ROLE_W : (cidx < 9 ? ROLE_G : ROLE_F);
  const size_t ubase = ((size_t)(b * HKV + j) * SK + (SK - WIN)) * DH;
  const int cq = i & 3, kp = i >> 2;
  const int ks0 = kp < 3 ? 4 * kp : 13, nt = kp < 2 ? 4 : 5;
  const u32 PK = 4 * nt, PV = PK + 128, PW = PK + 256;
  const int g4 = lane >> 4, c16 = lane & 15;
#define RSTAMP(k)
  const unsigned long long pt0 = __builtin_amdgcn_s_memrealtime();
  unsigned long long px = 0;
#define PMARK(n) do { if (PROBE == (n)) px = __builtin_amdgcn_s_memrealtime() - pt0; } while (0)
  STAMP(0);
  const int cw = (w + 7) & 7;
  u32 xr[5];
  u32x4 xa;
  {
    const GAS u32* xrow = (const GAS u32*)(p.x + cw * DM);
#pragma unroll
    for (int m = 0; m < 4; ++m) xr[m] = __builtin_nontemporal_load(xrow + lane + 64 * m);
    xr[4] = __builtin_nontemporal_load(xrow + (lane & 31) + 256);
    xa = __builtin_nontemporal_load((const GAS u32x4*)(p.x + (lane & 7) * DM + 32 * (ks0 + (cw < nt ? cw : 0)) + 8 * g4));
  }
  if (w == 0) {
    if (lane < 32) lds_st(L_FL + 4 * lane, 0u);
    asm volatile("s_waitcnt lgkmcnt(0)" ::: "memory");
  }
  hw_barrier();

  if (w == 0) {
    __builtin_amdgcn_s_setprio(3);
    u32 pub = 0;
#define PUBLISH(W) lds_st(L_FL + 4 * F_LANDED, pub - (W))
    {
      const unsigned char* a = (const unsigned char*)(p.wq + (size_t)32 * ks0 * NQ + 256 * j + 64 * cq);
      const u32 o0 = rowoff(0, NQ * 2, lane), o1 = rowoff(1, NQ * 2, lane);
#pragma unroll
      for (int t = 0; t < 4; ++t) tile_dma<-1>(a + (size_t)t * 32 * NQ * 2, NQ * 2, o0, o1, L_EXT + t * 4096);
      if (nt == 5) tile_dma<-1>(a + (size_t)4 * 32 * NQ * 2, NQ * 2, o0, o1, L_QSUM);
      pub = PK;
    }
    ESTAMP(8);
    {
      const unsigned char* kb = (const unsigned char*)(p.kc + ubase + (size_t)half * 512 * DH);
      u32 koff[4];
#pragma unroll
      for (int q = 0; q < 4; ++q) { const int key = 4 * q + g4; koff[q] = key * 256 + ((c16 ^ key) & 15) * 16; }
#pragma clang loop unroll(disable)
      for (int t = 0; t < 32; ++t) {
#pragma unroll
        for (int q = 0; q < 4; ++q) { glds16(kb + t * 4096, koff[q], (t * 4 + q) * 1024); waitvm<LWIN>(); }
        pub += 4;
        if (pub > LWIN) PUBLISH(LWIN);
      }
    }
    STAMP(1);
    {
      const unsigned char* vb = (const unsigned char*)(p.vc + ubase + (size_t)half * 512 * DH);
      u32 voff[4];
#pragma unroll
      for (int q = 0; q < 4; ++q) { const int key = 4 * q + g4; voff[q] = key * 256 + ((c16 ^ vswz(key)) & 15) * 16; }
      wait_word(F_QPUB, 1u);
#pragma clang loop unroll(disable)
      for (int t = 0; t < 32; ++t) {
        if ((t & 7) == 4) wait_all8(F_KDONE, (t >> 3) + 1, lane);
        const u32 dst = t < 4 ? L_EXT + t * 4096 : (t - 4) * 4096;
#pragma unroll
        for (int q = 0; q < 4; ++q) { glds16(vb + t * 4096, voff[q], dst + q * 1024); waitvm<LWIN>(); }
        pub += 4; PUBLISH(LWIN);
      }
    }
    STAMP(2);
    if (role == ROLE_W) {
      const unsigned char* a = (const unsigned char*)(p.wo + (size_t)256 * j * DM + 64 * i);
      const u32 o0 = rowoff(0, DM * 2, lane), o1 = rowoff(1, DM * 2, lane);
      wait_all8(F_VDONE, 1, lane);
#pragma clang loop unroll(disable)
      for (int t = 0; t < 8; ++t) { tile_dma<LWIN>(a + (size_t)t * 32 * DM * 2, DM * 2, o0, o1, t * 4096); pub += 4; PUBLISH(LWIN); }
    } else if (role == ROLE_G) {
      const int gx = xcd * 9 + cidx;
      const u32 o0 = rowoff(0, FF * 2, lane), o1 = rowoff(1, FF * 2, lane);
#pragma clang loop unroll(disable)
      for (int t = 0; t < 36; ++t) {
        if (t == 0 || t == 16) wait_all8(F_VDONE, (t >> 4) + 1, lane);
        const unsigned char* a = (const unsigned char*)(((t & 1) ? p.wu : p.wg) + (size_t)(t >> 1) * 32 * FF + 64 * gx);
        tile_dma<GWIN>(a, FF * 2, o0, o1, t < 4 ? L_EXT + t * 4096 : (t - 4) * 4096);
        pub += 4;
        if (4 * t + 4 > GWIN - LWIN) PUBLISH(GWIN);
      }
    }
    STAMP(3);
#define DR(N) if (GWIN > N && (role == ROLE_G || LWIN > N)) { waitvm<N>(); PUBLISH(N); }
    DR(44) DR(40) DR(36) DR(32) DR(28) DR(24) DR(20) DR(16) DR(12) DR(8) DR(4) DR(0)
#undef DR
#undef PUBLISH
    STAMP(14);
    STAMP_FLUSH();
    return;
  }

  const int ct = tid - 64;
  const rsrc_t rs = __builtin_amdgcn_make_buffer_rsrc((void*)p.ws, 0, WS_END, 0x00020000);
  const u32 T = *(const __attribute__((address_space(4))) u32*)(p.ws + WS_EPOCH) + 1u;
  const u32 T16 = T & 0xFFFFu, T8 = T & 0xFFu, T4 = T & 0xFu;
  float* const lf = (float*)lds;
  u32* const lu = (u32*)lds;
  {
    ESTAMP(8);
    asm volatile("" :: "v"(xr[0]), "v"(xr[4]));
    ESTAMP(9);
    float v[10]; bool valid[10];
#pragma unroll
    for (int m = 0; m < 5; ++m) { valid[2 * m] = valid[2 * m + 1] = (m < 4) || (lane < 32); v[2 * m] = bflo(xr[m]); v[2 * m + 1] = bfhi(xr[m]); }
    const float pivot = __uint_as_float(__builtin_amdgcn_readfirstlane(xr[0]) << 16);
    const RowStats st = row_stats<10>(v, valid, pivot);
    if (lane == 0) *(f32x2*)(lf + L_ST / 4 + 2 * cw) = f32x2{st.m, st.rs};
    ESTAMP(10);
    cbar(1, lane);
    STAMP(1);
    PMARK(1);
  }
  if (cw < nt) {
    const f32x2 sv = *(const f32x2*)(lf + L_ST / 4 + 2 * (lane & 7));
    const RowStats st = {sv.x, sv.y};
    u32x4 a;
#pragma unroll
    for (int q = 0; q < 4; ++q) a[q] = pack2(ln_apply(bflo(xa[q]), st), ln_apply(bfhi(xa[q]), st));
    const u32 base = cw < 4 ? L_EXT + cw * 4096 : L_QSUM;
    wait_word(F_LANDED, 4 * (cw + 1));
    u32x4 f[4]; tile_frags_lds(lds, base, lane, f);
    f32x4 d[4];
#pragma unroll
    for (int e = 0; e < 4; ++e) d[e] = mfma(f[e], a, f32x4{0.f, 0.f, 0.f, 0.f});
    if (c16 < 8) {
#pragma unroll
      for (int e = 0; e < 4; ++e) *(f32x4*)(lf + base / 4 + c16 * 64 + 16 * e + 4 * g4) = d[e];
    }
  }
  ESTAMP(11);
  cbar(2, lane);
  ESTAMP(12);
  {
    float sum = (lf[L_EXT / 4 + cw * 64 + lane] + lf[L_EXT / 4 + 1024 + cw * 64 + lane]) + (lf[L_EXT / 4 + 2048 + cw * 64 + lane] + lf[L_EXT / 4 + 3072 + cw * 64 + lane]);
    if (nt == 5) sum += lf[L_QSUM / 4 + cw * 64 + lane];
    const u32 v = put_tag<0xFu>(sum, T4);
    const u32x4 o = {v, (u32)__builtin_amdgcn_mov_dpp((int)v, 0x55, 0xf, 0xf, true), (u32)__builtin_amdgcn_mov_dpp((int)v, 0xAA, 0xf, 0xf, true),
                     (u32)__builtin_amdgcn_mov_dpp((int)v, 0xFF, 0xf, 0xf, true)};
    if ((lane & 3) == 0) st16c(rs, WS_QS + ((((j * 8 + cw) * 4 + kp) * 256) + 64 * cq + lane) * 4, o);
  }
  cbar(3, lane);
  if (ct == 0) lds_st(L_FL + 4 * F_QPUB, 1u);
  STAMP(2);
  PMARK(2);
  if (cw >= 2 && cw < 6) {
    u32x4 qg;
    __builtin_amdgcn_s_sleep(QDELAY);
    {
      unsigned spins = 0;
#pragma clang loop unroll(disable)
      for (;;) {
        qg = ld16c(rs, WS_QS + (((j * 8 + b) * 4 + (cw - 2)) * 256 + 4 * lane) * 4);
        if (tag_ok<0xFu>(qg, T4) || ++spins > SPIN_CAP) break;
        __builtin_amdgcn_s_sleep(4);
      }
    }
    *(f32x4*)(lf + L_QSUM / 4 + (cw - 2) * 256 + 4 * lane) = f32x4{get_tag<0xFu>(qg.x), get_tag<0xFu>(qg.y), get_tag<0xFu>(qg.z), get_tag<0xFu>(qg.w)};
  }
  cbar(4, lane);
  if (cw == 0) {
    f32x4 sv[4];
#pragma unroll
    for (int q = 0; q < 4; ++q) sv[q] = *(const f32x4*)(lf + L_QSUM / 4 + q * 256 + 4 * lane);
    const f32x4 sum = (sv[0] + sv[1]) + (sv[2] + sv[3]);
    *(u32x2*)(lu + L_Q / 4 + 2 * lane) = u32x2{pack2(sum.x, sum.y), pack2(sum.z, sum.w)};
    asm volatile("s_waitcnt lgkmcnt(0)" ::: "memory");
    if (lane == 0) lds_st(L_FL + 4 * F_QRDY, 1u);
  } else {
    wait_word(F_QRDY, 1u);
  }
  STAMP(3);
  PMARK(3);
  float s0, s1;
  {
    u32x4 qa[4];
#pragma unroll
    for (int ks = 0; ks < 4; ++ks) qa[ks] = *(const u32x4*)(lu + L_Q / 4 + (lane & 1) * 64 + 16 * ks + 4 * g4);
    f32x4 d[4];
#pragma unroll
    for (int m = 0; m < 4; ++m) {
      const int tile = cw + 8 * m;
      wait_word_sleep(F_LANDED, PK + 4 * (tile + 1));
      u32x4 kf[4];
#pragma unroll
      for (int ks = 0; ks < 4; ++ks) kf[ks] = *(const u32x4*)(lds + tile * 4096 + c16 * 256 + (((4 * ks + g4) ^ c16) << 4));
      f32x4 acc = mfma(qa[0], kf[0], f32x4{0.f, 0.f, 0.f, 0.f});
      acc = mfma(qa[1], kf[1], acc); acc = mfma(qa[2], kf[2], acc); acc = mfma(qa[3], kf[3], acc);
      d[m] = acc;
      asm volatile("s_waitcnt lgkmcnt(0)" ::: "memory");
      if (lane == 0) lds_st(L_FL + 4 * (F_KDONE + cw), m + 1);
    }
    const float x0 = g4 == 0 ? d[0][0] : g4 == 1 ? d[1][0] : g4 == 2 ? d[2][0] : d[3][0];
    const float x1 = g4 == 0 ? d[0][1] : g4 == 1 ? d[1][1] : g4 == 2 ? d[2][1] : d[3][1];
    constexpr float SCALE = 0.08838834764831845f;
    s0 = rbf(rbf(x0) * SCALE); s1 = rbf(rbf(x1) * SCALE);
  }
  STAMP(4);
  PMARK(4);
  {
    constexpr float L2E = 1.4426950408889634f;
    const float m0 = wave_max(s0), m1 = wave_max(s1);
    const float l0 = wave_sum(__builtin_amdgcn_exp2f((s0 - m0) * L2E)), l1 = wave_sum(__builtin_amdgcn_exp2f((s1 - m1) * L2E));
    if (lane == 0) *(f32x4*)(lf + L_ST / 4 + 16 + 4 * cw) = f32x4{m0, m1, l0, l1};
    cbar(5, lane);
    float M0 = -INFINITY, M1 = -INFINITY;
    f32x4 st[8];
#pragma unroll
    for (int ww = 0; ww < 8; ++ww) { st[ww] = *(const f32x4*)(lf + L_ST / 4 + 16 + 4 * ww); M0 = fmax_(M0, st[ww].x); M1 = fmax_(M1, st[ww].y); }
    float L0 = 0.f, L1 = 0.f;
#pragma unroll
    for (int ww = 0; ww < 8; ++ww) {
      L0 = fmaf(st[ww].z, __builtin_amdgcn_exp2f((st[ww].x - M0) * L2E), L0);
      L1 = fmaf(st[ww].w, __builtin_amdgcn_exp2f((st[ww].y - M1) * L2E), L1);
    }
    const u32x4 mine = {__float_as_uint(M0) | T16, __float_as_uint(M1) | T16, put_tag<0xFFu>(L0, T8), put_tag<0xFFu>(L1, T8)};
    u32x4 oth;
    if (cw == 1) {
      if (lane == 0) st16c(rs, WS_SC + wg * 128, mine);
      unsigned spins = 0;
#pragma clang loop unroll(disable)
      for (;;) {
        oth = ld16c(rs, WS_SC + (wg ^ 8) * 128);
        if (__all(((((oth.x ^ T16) | (oth.y ^ T16)) & 0xFFFFu) | (((oth.z ^ T8) | (oth.w ^ T8)) & 0xFFu)) == 0u) || ++spins > SPIN_CAP) break;
        __builtin_amdgcn_s_sleep(4);
      }
      if (lane == 0) { *(u32x4*)(lu + L_ST / 4 + 48) = oth; asm volatile("s_waitcnt lgkmcnt(0)" ::: "memory"); lds_st(L_FL + 4 * F_SRDY, 1u); }
    } else {
      wait_word_sleep(F_SRDY, 1u);
      oth = *(const u32x4*)(lu + L_ST / 4 + 48);
    }
    STAMP(5);
    const float om0 = bfhi(oth.x), om1 = bfhi(oth.y);
    const float G0 = fmax_(M0, om0), G1 = fmax_(M1, om1);
    const float S0 = get_tag<0xFFu>(mine.z) * __builtin_amdgcn_exp2f((M0 - G0) * L2E) + get_tag<0xFFu>(oth.z) * __builtin_amdgcn_exp2f((om0 - G0) * L2E);
    const float S1 = get_tag<0xFFu>(mine.w) * __builtin_amdgcn_exp2f((M1 - G1) * L2E) + get_tag<0xFFu>(oth.w) * __builtin_amdgcn_exp2f((om1 - G1) * L2E);
    const int kl = 16 * (cw + 8 * g4) + c16;
    __bf16* P = (__bf16*)(lds + L_P);
    P[kl] = (__bf16)(__builtin_amdgcn_exp2f((s0 - G0) * L2E) * (1.0f / S0));
    P[512 + kl] = (__bf16)(__builtin_amdgcn_exp2f((s1 - G1) * L2E) * (1.0f / S1));
  }
  cbar(6, lane);
  STAMP(6);
  {
    f32x4 d[8];
#pragma unroll
    for (int e = 0; e < 8; ++e) d[e] = f32x4{0.f, 0.f, 0.f, 0.f};
    const int q4 = (lane & 15) >> 2, p4 = lane & 3;
#pragma unroll
    for (int m = 0; m < 2; ++m) {
      const int ks = cw + 8 * m;
      const u32x4 pf = *(const u32x4*)(lds + L_P + (lane & 1) * 1024 + (32 * ks + 8 * g4) * 2);
      const u32 base = ks < 2 ? L_EXT + ks * 8192 : (ks - 2) * 8192;
      wait_word(F_LANDED, PV + 8 * (ks + 1));
      u32x4 f[8];
#pragma unroll
      for (int e = 0; e < 8; ++e) {
        u32x2 h[2];
#pragma unroll
        for (int t = 0; t < 2; ++t) {
          const int row = 8 * g4 + 4 * t + q4;
          const u32 a = base + row * 256 + (((2 * e + (p4 >> 1)) ^ vswz(row)) << 4) + 8 * (p4 & 1);
          h[t] = __builtin_bit_cast(u32x2, __builtin_amdgcn_ds_read_tr16_b64_v4i16((LAS s16x4*)(lds + a)));
        }
        f[e] = u32x4{h[0].x, h[0].y, h[1].x, h[1].y};
      }
      SB();
#pragma unroll
      for (int e = 0; e < 8; ++e) d[e] = mfma(f[e], pf, d[e]);
      asm volatile("s_waitcnt lgkmcnt(0)" ::: "memory");
      if (lane == 0) lds_st(L_FL + 4 * (F_VDONE + cw), m + 1);
    }
    if (c16 < 2) {
#pragma unroll
      for (int e = 0; e < 8; ++e) *(f32x4*)(lf + L_QSUM / 4 + cw * 256 + c16 * 128 + 16 * e + 4 * g4) = d[e];
    }
  }
  cbar(7, lane);
  auto gather4 = [&](u32 v) -> u32x4 {
    return u32x4{v, (u32)__builtin_amdgcn_mov_dpp((int)v, 0x55, 0xf, 0xf, true), (u32)__builtin_amdgcn_mov_dpp((int)v, 0xAA, 0xf, 0xf, true),
                 (u32)__builtin_amdgcn_mov_dpp((int)v, 0xFF, 0xf, 0xf, true)};
  };
  if (cw < 4) {
    float sv[8];
#pragma unroll
    for (int ww = 0; ww < 8; ++ww) sv[ww] = lf[L_QSUM / 4 + ww * 256 + ct];
    const float sum = ((sv[0] + sv[1]) + (sv[2] + sv[3])) + ((sv[4] + sv[5]) + (sv[6] + sv[7]));
    const u32x4 o = gather4(put_tag<0xFFu>(sum, T8));
    if ((lane & 3) == 0) st16c(rs, WS_OS + (((j * 8 + b) * 2 + half) * 256 + ct) * 4, o);
  }
  STAMP(7);
  PMARK(7);

  auto acc_to_lds = [&](int base, const f32x4 (&d)[4]) {
    if (c16 < 8) {
#pragma unroll
      for (int e = 0; e < 4; ++e) *(f32x4*)(lf + base / 4 + (cw * 8 + c16) * 64 + 16 * e + 4 * g4) = d[e];
    }
  };
  auto publish_partial = [&](int base, int cg) {
    float sv[8];
#pragma unroll
    for (int ww = 0; ww < 8; ++ww) sv[ww] = lf[L_EXT / 4 + ww * 512 + cw * 64 + lane];
    const float sum = ((sv[0] + sv[1]) + (sv[2] + sv[3])) + ((sv[4] + sv[5]) + (sv[6] + sv[7]));
    const u32x4 o = gather4(put_tag<0xFFu>(sum, T8));
    if ((lane & 3) == 0) st16c(rs, base + (((cw * 9 + cg) * 16 + j) * 64 + lane) * 4, o);
  };

  if (role == ROLE_W) {
    const int cg = i;
    u32x4 f[4];
    wait_word(F_LANDED, PW + 32);
    tile_frags_lds(lds, cw * 4096, lane, f);
    {
      const int off = WS_OS + ((j * 8 + (lane & 7)) * 512 + 32 * cw + 8 * g4) * 4;
      u32x4 a0, a1, b0, b1;
      POLL((a0 = ld16c(rs, off), a1 = ld16c(rs, off + 16), b0 = ld16c(rs, off + 1024), b1 = ld16c(rs, off + 1040),
            tag_ok<0xFFu>(a0, T8) & tag_ok<0xFFu>(a1, T8) & tag_ok<0xFFu>(b0, T8) & tag_ok<0xFFu>(b1, T8)));
      STAMP(8);
      RSTAMP(0);
#define S2(x, y) (get_tag<0xFFu>(x) + get_tag<0xFFu>(y))
      const u32x4 a = {pack2(S2(a0.x, b0.x), S2(a0.y, b0.y)), pack2(S2(a0.z, b0.z), S2(a0.w, b0.w)), pack2(S2(a1.x, b1.x), S2(a1.y, b1.y)), pack2(S2(a1.z, b1.z), S2(a1.w, b1.w))};
#undef S2
      f32x4 d[4];
#pragma unroll
      for (int e = 0; e < 4; ++e) d[e] = mfma(f[e], a, f32x4{0.f, 0.f, 0.f, 0.f});
      acc_to_lds(L_EXT, d);
    }
    wg_barrier();
    publish_partial(WS_PW, cg);
    STAMP(9);
    u32x4 f2[4];
    {
      const unsigned char* a = (const unsigned char*)(p.wd + (size_t)(288 * j + 32 * cw) * DM + 64 * cg);
      const u32 o0 = rowoff(0, DM * 2, lane), o1 = rowoff(1, DM * 2, lane);
      tile_dma<-1>(a, DM * 2, o0, o1, (8 + cw) * 4096);
      if (cw == 0) tile_dma<-1>(a + 256 * DM * 2, DM * 2, o0, o1, 16 * 4096);
      waitvm<0>();
      tile_frags_lds(lds, (8 + cw) * 4096, lane, f);
      if (cw == 0) tile_frags_lds(lds, 16 * 4096, lane, f2);
    }
    {
      const int off = WS_AS + ((lane & 7) * FF + 288 * j + 32 * cw + 8 * g4) * 4;
      u32x4 a0, a1, a2, a3;
      if (cw == 0) POLL((a0 = ld16c(rs, off), a1 = ld16c(rs, off + 16), a2 = ld16c(rs, off + 1024), a3 = ld16c(rs, off + 1040),
                         tag_ok<0xFFFFu>(a0, T16) & tag_ok<0xFFFFu>(a1, T16) & tag_ok<0xFFFFu>(a2, T16) & tag_ok<0xFFFFu>(a3, T16)));
      else POLL((a0 = ld16c(rs, off), a1 = ld16c(rs, off + 16), tag_ok<0xFFFFu>(a0, T16) & tag_ok<0xFFFFu>(a1, T16)));
      STAMP(12);
      RSTAMP(1);
      const u32x4 a = afrag(a0, a1);
      f32x4 d[4];
#pragma unroll
      for (int e = 0; e < 4; ++e) d[e] = mfma(f[e], a, f32x4{0.f, 0.f, 0.f, 0.f});
      if (cw == 0) {
        const u32x4 ab = afrag(a2, a3);
#pragma unroll
        for (int e = 0; e < 4; ++e) d[e] = mfma(f2[e], ab, d[e]);
      }
      acc_to_lds(L_EXT, d);
    }
    wg_barrier();
    publish_partial(WS_PD, cg);
    STAMP(13);
  } else if (role == ROLE_G) {
    const int gx = xcd * 9 + cidx;
    u32x4 f[5][4];
#pragma unroll
    for (int tl = 0; tl < 5; ++tl) {
      if (tl < 4 || cw < 4) {
        const int tau = cw + 8 * tl;
        wait_word(F_LANDED, PW + 4 * (tau + 1));
        tile_frags_lds(lds, tau < 4 ? L_EXT + tau * 4096 : (tau - 4) * 4096, lane, f[tl]);
      }
    }
    wait_word(F_LANDED, PW + 144);
    STAMP(8);
    {
      const int off = WS_HS + cw * DM * 4;
      u32x4 h0, h1; u32 h2;
      POLL((h0 = ld16c(rs, off + lane * 32), h1 = ld16c(rs, off + lane * 32 + 16), h2 = ld4c(rs, off + 2048 + lane * 4),
            tag_ok<0xFFFFu>(h0, T16) & tag_ok<0xFFFFu>(h1, T16) & ((h2 & 0xFFFFu) == T16)));
      STAMP(10);
      RSTAMP(0);
      float v[9]; bool valid[9];
#pragma unroll
      for (int e = 0; e < 4; ++e) { v[e] = bfhi(h0[e]); v[4 + e] = bfhi(h1[e]); }
      v[8] = bfhi(h2);
#pragma unroll
      for (int e = 0; e < 9; ++e) valid[e] = true;
      const float pivot = __uint_as_float(__builtin_amdgcn_readfirstlane(h0.x) & 0xFFFF0000u);
      const RowStats st = row_stats<9>(v, valid, pivot);
      u32* hn = lu + L_HN / 4 + cw * HN_STRIDE;
      *(u32x4*)(hn + 4 * lane) = u32x4{pack2(ln_apply(v[0], st), ln_apply(v[1], st)), pack2(ln_apply(v[2], st), ln_apply(v[3], st)),
                                       pack2(ln_apply(v[4], st), ln_apply(v[5], st)), pack2(ln_apply(v[6], st), ln_apply(v[7], st))};
      ((__bf16*)(hn + 256))[lane] = (__bf16)ln_apply(v[8], st);
    }
    wg_barrier();
    {
      f32x4 d[4];
      const u32* hn = lu + L_HN / 4 + (lane & 7) * HN_STRIDE + 16 * (cw >> 1) + 4 * g4;
      u32x4 a[5];
#pragma unroll
      for (int tl = 0; tl < 5; ++tl) a[tl] = *(const u32x4*)(hn + (tl < 4 || cw < 4 ? 64 * tl : 0));
      SB();
#pragma unroll
      for (int e = 0; e < 4; ++e) d[e] = mfma(f[0][e], a[0], f32x4{0.f, 0.f, 0.f, 0.f});
#pragma unroll
      for (int tl = 1; tl < 5; ++tl) {
        if (tl < 4 || cw < 4) {
#pragma unroll
          for (int e = 0; e < 4; ++e) d[e] = mfma(f[tl][e], a[tl], d[e]);
        }
      }
      acc_to_lds(0, d);
    }
    wg_barrier();
    {
      float gv[4], uv[4];
#pragma unroll
      for (int ww = 0; ww < 4; ++ww) { gv[ww] = lf[(2 * ww) * 512 + cw * 64 + lane]; uv[ww] = lf[(2 * ww + 1) * 512 + cw * 64 + lane]; }
      const float gg = rbf((gv[0] + gv[1]) + (gv[2] + gv[3])), uu = rbf((uv[0] + uv[1]) + (uv[2] + uv[3]));
      const float sl = rbf(gg * __builtin_amdgcn_rcpf(1.0f + __builtin_amdgcn_exp2f(gg * -1.4426950408889634f)));
      const u32x4 o = gather4(bfbits_hi(sl * uu) | T16);
      if ((lane & 3) == 0) st16c(rs, WS_AS + (cw * FF + 64 * gx + lane) * 4, o);
    }
    STAMP(11);
  } else {
    const int f = xcd * 5 + (cidx - 9);
    const int job = 2 * f + (cw >> 2);
    if ((cw & 3) == 0 && job < 72) {
      const int jb = job / 9, cg = job % 9;
      const u32x2 xq = *(const GAS u32x2*)(p.x + jb * DM + 64 * cg + 4 * c16);
      const u32x4 wr0 = *(const GAS u32x4*)(p.wr + 8 * lane);
      const u16 wr1 = ((const GAS u16*)p.wr)[512 + lane];
      auto gather16 = [&](int base) -> f32x4 {
        const int off = base + (((jb * 9 + cg) * 16 + g4) * 64 + 4 * c16) * 4;
        u32x4 q0, q1, q2, q3;
        POLL((q0 = ld16c(rs, off), q1 = ld16c(rs, off + 1024), q2 = ld16c(rs, off + 2048), q3 = ld16c(rs, off + 3072),
              tag_ok<0xFFu>(q0, T8) & tag_ok<0xFFu>(q1, T8) & tag_ok<0xFFu>(q2, T8) & tag_ok<0xFFu>(q3, T8)));
        f32x4 sum;
#pragma unroll
        for (int e = 0; e < 4; ++e) {
          float t = (get_tag<0xFFu>(q0[e]) + get_tag<0xFFu>(q1[e])) + (get_tag<0xFFu>(q2[e]) + get_tag<0xFFu>(q3[e]));
          t = swap16_add(t, t); t = swap32_add(t, t);
          sum[e] = t;
        }
        return sum;
      };
      STAMP(3);
      const f32x4 at = gather16(WS_PW);
      STAMP(8);
      PMARK(8);
      RSTAMP(0);
      f32x4 hv;
      hv[0] = rbf(bflo(xq.x) + rbf(at[0])); hv[1] = rbf(bfhi(xq.x) + rbf(at[1]));
      hv[2] = rbf(bflo(xq.y) + rbf(at[2])); hv[3] = rbf(bfhi(xq.y) + rbf(at[3]));
      if (lane < 16)
        st16c(rs, WS_HS + (jb * DM + 64 * cg + 4 * lane) * 4,
              u32x4{__float_as_uint(hv[0]) | T16, __float_as_uint(hv[1]) | T16, __float_as_uint(hv[2]) | T16, __float_as_uint(hv[3]) | T16});
      STAMP(9);
      RowStats st; float sig;
      {
        const int off = WS_HS + jb * DM * 4;
        u32x4 h0, h1; u32 h2;
        POLL((h0 = ld16c(rs, off + lane * 32), h1 = ld16c(rs, off + lane * 32 + 16), h2 = ld4c(rs, off + 2048 + lane * 4),
              tag_ok<0xFFFFu>(h0, T16) & tag_ok<0xFFFFu>(h1, T16) & ((h2 & 0xFFFFu) == T16)));
        float v[9]; bool valid[9];
#pragma unroll
        for (int e = 0; e < 4; ++e) { v[e] = bfhi(h0[e]); v[4 + e] = bfhi(h1[e]); }
        v[8] = bfhi(h2);
#pragma unroll
        for (int e = 0; e < 9; ++e) valid[e] = true;
        const float pivot = __uint_as_float(__builtin_amdgcn_readfirstlane(h0.x) & 0xFFFF0000u);
        st = row_stats<9>(v, valid, pivot);
        float r = rbf(ln_apply(v[8], st)) * bflo((u32)wr1);
#pragma unroll
        for (int e = 0; e < 8; ++e) r = fmaf(rbf(ln_apply(v[e], st)), (e & 1) ? bfhi(wr0[e >> 1]) : bflo(wr0[e >> 1]), r);
        r = rbf(wave_sum(r));
        sig = rbf(__builtin_amdgcn_rcpf(1.0f + __builtin_amdgcn_exp2f(r * -1.4426950408889634f)));
      }
      float hn[4];
#pragma unroll
      for (int e = 0; e < 4; ++e) hn[e] = rbf(ln_apply(hv[e], st));
      STAMP(10);
      const f32x4 y = gather16(WS_PD);
      STAMP(12);
      RSTAMP(1);
      float o[4];
#pragma unroll
      for (int e = 0; e < 4; ++e) o[e] = hv[e] + rbf(hn[e] + rbf(rbf(y[e]) * sig));
      if (lane < 16) *(GAS u32x2*)(p.out + jb * DM + 64 * cg + 4 * lane) = u32x2{pack2(o[0], o[1]), pack2(o[2], o[3])};
      PMARK(9);
      if (job == 0) { const unsigned long long until = 6000ull + (PROBE ? 10ull * px : 0ull); while (__builtin_amdgcn_s_memrealtime() - pt0 < until) __builtin_amdgcn_s_sleep(1); }
      if (job == 0 && lane == 0) __hip_atomic_store((GAS u32*)(p.ws + WS_EPOCH), T, __ATOMIC_RELAXED, __HIP_MEMORY_SCOPE_AGENT);
      STAMP(13);
    }
  }
  STAMP(14);
  STAMP_FLUSH();
}

}

extern "C" void kernel_launch(void* const* d_in, const int* in_sizes, int n_in, void* d_out, int out_size, void* d_ws, size_t ws_size,
                              hipStream_t stream) {
  Params p;
  p.x = (const u16*)d_in[0];
  p.kc = (const u16*)d_in[1];
  p.vc = (const u16*)d_in[2];
  p.wq = (const u16*)d_in[3];
  p.wo = (const u16*)d_in[4];
  p.wr = (const u16*)d_in[6];
  p.wg = (const u16*)d_in[7];
  p.wu = (const u16*)d_in[8];
  p.wd = (const u16*)d_in[9];
  p.out = (u16*)d_out;
  p.ws = (unsigned char*)d_ws;
  (void)hipFuncSetAttribute((const void*)fused_kernel, hipFuncAttributeMaxDynamicSharedMemorySize, LDS_BYTES);
  hipLaunchKernelGGL(fused_kernel, dim3(NWG), dim3(NTHR), LDS_BYTES, stream, p);
}
```

```cpp
#include <hip/hip_runtime.h>
#include <stdint.h>

typedef unsigned u32;
typedef unsigned short u16;
typedef u32 u32x4 __attribute__((ext_vector_type(4)));
typedef u32 u32x2 __attribute__((ext_vector_type(2)));
typedef float f32x4 __attribute__((ext_vector_type(4)));
typedef float f32x2 __attribute__((ext_vector_type(2)));
typedef short s16x4 __attribute__((ext_vector_type(4)));
typedef __bf16 bf16x8 __attribute__((ext_vector_type(8)));
typedef __bf16 bf16x2 __attribute__((ext_vector_type(2)));
#define GAS __attribute__((address_space(1)))
#define LAS __attribute__((address_space(3)))

#define STAMPS 0
#define LWIN 20
#define GWIN 40
#define QDELAY 8
#define GATE_PW 90
#define GATE_H 150
#define GATE_ACT 220
#define GATE_PD 270
#define PSTAG 6
#define PSLEEP 1
#define QS_T4 1
#define DYNROLE 1
#define OS_B 1

namespace {

constexpr int DM = 576, HKV = 16, SK = 4096, DH = 128, WIN = 1024, NQ = 4096, FF = 4608;
constexpr int NWG = 256, NTHR = 576;

constexpr int WS_EPOCH = 0;
constexpr int WS_RANK = 2048;
constexpr int WS_QS = 4096;
constexpr int WS_SC = WS_QS + 16 * 8 * 4 * 256 * 4;
constexpr int WS_OS = WS_SC + 256 * 128;
constexpr int WS_PW = WS_OS + 16 * 8 * 2 * 256 * 4;
constexpr int HREP = 8;
constexpr int WS_HS = WS_PW + 8 * 9 * 16 * 64 * 4;
constexpr int WS_AS = WS_HS + HREP * 8 * 576 * 4;
constexpr int WS_PD = WS_AS + 8 * 4608 * 4;
constexpr int WS_END = WS_PD + 8 * 9 * 16 * 64 * 4;
constexpr int WS_STAMP = (WS_END + 4095) / 4096 * 4096;
constexpr int AUX_SC1 = 16;
constexpr unsigned SPIN_CAP = 1u << 16, LSPIN_CAP = 1u << 22;

constexpr int L_EXT = 128 * 1024;
constexpr int L_FL = 144 * 1024;
constexpr int L_ST = L_FL + 128;
constexpr int L_P = L_ST + 256;
constexpr int L_QSUM = L_P + 2048;
constexpr int L_Q = L_QSUM + 8192;
constexpr int L_HN = L_P;
constexpr int HN_STRIDE = 292;
constexpr int LDS_BYTES = 160 * 1024;
static_assert(L_Q + 512 <= LDS_BYTES && L_HN + 8 * HN_STRIDE * 4 <= LDS_BYTES, "LDS");
enum { F_LANDED = 0, F_QPUB = 1, F_BAR = 2, F_QRDY = 3, F_RANK = 4, F_KDONE = 8, F_VDONE = 16 };

struct Params {
  const u16 *x, *kc, *vc, *wq, *wo, *wr, *wg, *wu, *wd;
  u16* out;
  unsigned char* ws;
};

__device__ __forceinline__ float bflo(u32 d) { return __uint_as_float(d << 16); }
__device__ __forceinline__ float bfhi(u32 d) { return __uint_as_float(d & 0xFFFF0000u); }
__device__ __forceinline__ float rbf(float f) { return (float)(__bf16)f; }
__device__ __forceinline__ u32 bfbits_hi(float f) { return __float_as_uint(rbf(f)); }
__device__ __forceinline__ u32 pack2(float lo, float hi) {
  bf16x2 v = {(__bf16)lo, (__bf16)hi};
  return __builtin_bit_cast(u32, v);
}
template <int CTRL> __device__ __forceinline__ float dpp(float x) {
  return __uint_as_float((u32)__builtin_amdgcn_mov_dpp((int)__float_as_uint(x), CTRL, 0xf, 0xf, true));
}
constexpr int XOR1 = 0xB1, XOR2 = 0x4E, XOR7 = 0x141, XOR8 = 0x128;
__device__ __forceinline__ float swap32_add(float a, float b) {
  auto r = __builtin_amdgcn_permlane32_swap(__float_as_uint(a), __float_as_uint(b), false, false);
  return __uint_as_float(r[0]) + __uint_as_float(r[1]);
}
__device__ __forceinline__ float swap16_add(float a, float b) {
  auto r = __builtin_amdgcn_permlane16_swap(__float_as_uint(a), __float_as_uint(b), false, false);
  return __uint_as_float(r[0]) + __uint_as_float(r[1]);
}
__device__ __forceinline__ float wave_sum(float x) {
  x += dpp<XOR1>(x); x += dpp<XOR2>(x); x += dpp<XOR7>(x); x += dpp<XOR8>(x);
  x = swap16_add(x, x); x = swap32_add(x, x);
  return x;
}
__device__ __forceinline__ float fmax_(float a, float b) { return __builtin_amdgcn_fmed3f(a, b, INFINITY); }
__device__ __forceinline__ float wave_max(float x) {
  x = fmax_(x, dpp<XOR1>(x)); x = fmax_(x, dpp<XOR2>(x)); x = fmax_(x, dpp<XOR7>(x)); x = fmax_(x, dpp<XOR8>(x));
  auto r = __builtin_amdgcn_permlane16_swap(__float_as_uint(x), __float_as_uint(x), false, false);
  x = fmax_(__uint_as_float(r[0]), __uint_as_float(r[1]));
  auto t = __builtin_amdgcn_permlane32_swap(__float_as_uint(x), __float_as_uint(x), false, false);
  return fmax_(__uint_as_float(t[0]), __uint_as_float(t[1]));
}
__device__ __forceinline__ f32x4 mfma(u32x4 a, u32x4 b, f32x4 c) {
  return __builtin_amdgcn_mfma_f32_16x16x32_bf16(__builtin_bit_cast(bf16x8, a), __builtin_bit_cast(bf16x8, b), c, 0, 0, 0);
}
#define SB() __builtin_amdgcn_sched_barrier(0)
template <int N> __device__ __forceinline__ void waitvm() { asm volatile("s_waitcnt vmcnt(%0)" ::"n"(N) : "memory"); }
typedef __amdgpu_buffer_rsrc_t rsrc_t;
__device__ __forceinline__ u32x4 ld16c(rsrc_t rs, int off) { return __builtin_amdgcn_raw_buffer_load_b128(rs, off, 0, AUX_SC1); }
__device__ __forceinline__ u32 ld4c(rsrc_t rs, int off) { return __builtin_amdgcn_raw_buffer_load_b32(rs, off, 0, AUX_SC1); }
__device__ __forceinline__ void st16c(rsrc_t rs, int off, u32x4 v) { __builtin_amdgcn_raw_buffer_store_b128(v, rs, off, 0, AUX_SC1); }
template <u32 M> __device__ __forceinline__ bool tag_ok(u32x4 v, u32 t) { return ((((v.x ^ t) | (v.y ^ t)) | ((v.z ^ t) | (v.w ^ t))) & M) == 0u; }
template <u32 M> __device__ __forceinline__ u32 put_tag(float f, u32 t) { return ((__float_as_uint(f) + (M + 1) / 2) & ~M) | t; }
template <u32 M> __device__ __forceinline__ float get_tag(u32 d) { return __uint_as_float(d & ~M); }
#define POLL_COUNT(n)
#define POLL(cond_load) do { unsigned spins_ = 0; _Pragma("clang loop unroll(disable)") while (!(cond_load) && ++spins_ < SPIN_CAP) __builtin_amdgcn_s_sleep(PSLEEP); POLL_COUNT(spins_); } while (0)

#define PV_CLOB "v128","v129","v130","v131","v132","v133","v134","v135","v136","v137","v138","v139","v140","v141","v142","v143", \
                "v144","v145","v146","v147","v148","v149","v150","v151","v152","v153","v154","v155","v156","v157","v158","v159","v160","v161","v162","v163", "vcc", "scc", "memory"
#define PV_LD4(r0, r1, r2, r3, r4, r5, r6, r7) \
  "buffer_load_dwordx4 v[" r0 ":" r1 "], %[o0], %[rs], 0 offen sc1\n\t" "buffer_load_dwordx4 v[" r2 ":" r3 "], %[o1], %[rs], 0 offen sc1\n\t" \
  "buffer_load_dwordx4 v[" r4 ":" r5 "], %[o2], %[rs], 0 offen sc1\n\t" "buffer_load_dwordx4 v[" r6 ":" r7 "], %[o3], %[rs], 0 offen sc1\n\t"
#define PV_X4(t, a, b, c, d) "v_xor_b32 " t ", %[T], v" a "\n\t" "v_bitop3_b32 " t ", " t ", v" b ", %[T] bitop3:0xf6\n\t" "v_bitop3_b32 " t ", " t ", v" c ", %[T] bitop3:0xf6\n\t" "v_bitop3_b32 " t ", " t ", v" d ", %[T] bitop3:0xf6\n\t"
#define PV_TEST "v_and_b32 v160, %[M], v160\n\tv_cmp_ne_u32 vcc, 0, v160\n\ts_cmp_eq_u64 vcc, 0\n\t"
#define PV_MOV2(o, a, b) "v_mov_b64 " o ", v[" a ":" b "]\n\t"
__device__ __forceinline__ void poll4(rsrc_t rs, int o0, int o1, int o2, int o3, u32 T, u32 M, u32x4 (&R)[4]) {
  u32x2 r0, r1, r2, r3, r4, r5, r6, r7; u32 cnt = SPIN_CAP;
  asm volatile(
      PV_LD4("128", "131", "132", "135", "136", "139", "140", "143")
      "s_sleep %[stag]\n"
      "1:\n\t"
      PV_LD4("144", "147", "148", "151", "152", "155", "156", "159")
      "s_waitcnt vmcnt(4)\n\t"
      PV_X4("v160", "128", "129", "130", "131") PV_X4("v161", "132", "133", "134", "135") PV_X4("v162", "136", "137", "138", "139") PV_X4("v163", "140", "141", "142", "143")
      "v_or3_b32 v160, v160, v161, v162\n\tv_or_b32 v160, v160, v163\n\t" PV_TEST
      "s_cbranch_scc1 7f\n\t"
      PV_LD4("128", "131", "132", "135", "136", "139", "140", "143")
      "s_waitcnt vmcnt(4)\n\t"
      PV_X4("v160", "144", "145", "146", "147") PV_X4("v161", "148", "149", "150", "151") PV_X4("v162", "152", "153", "154", "155") PV_X4("v163", "156", "157", "158", "159")
      "v_or3_b32 v160, v160, v161, v162\n\tv_or_b32 v160, v160, v163\n\t" PV_TEST
      "s_cbranch_scc1 8f\n\t"
      "s_sub_u32 %[cnt], %[cnt], 1\n\ts_cbranch_scc0 1b\n\t"
      "s_waitcnt vmcnt(0)\n"
      "7:\n\t"
      PV_MOV2("%[r0]", "128", "129") PV_MOV2("%[r1]", "130", "131") PV_MOV2("%[r2]", "132", "133") PV_MOV2("%[r3]", "134", "135")
      PV_MOV2("%[r4]", "136", "137") PV_MOV2("%[r5]", "138", "139") PV_MOV2("%[r6]", "140", "141") PV_MOV2("%[r7]", "142", "143")
      "s_branch 9f\n"
      "8:\n\t"
      PV_MOV2("%[r0]", "144", "145") PV_MOV2("%[r1]", "146", "147") PV_MOV2("%[r2]", "148", "149") PV_MOV2("%[r3]", "150", "151")
      PV_MOV2("%[r4]", "152", "153") PV_MOV2("%[r5]", "154", "155") PV_MOV2("%[r6]", "156", "157") PV_MOV2("%[r7]", "158", "159")
      "9:\n\t"
      : [r0] "=&v"(r0), [r1] "=&v"(r1), [r2] "=&v"(r2), [r3] "=&v"(r3), [r4] "=&v"(r4), [r5] "=&v"(r5), [r6] "=&v"(r6), [r7] "=&v"(r7), [cnt] "+s"(cnt)
      : [o0] "v"(o0), [o1] "v"(o1), [o2] "v"(o2), [o3] "v"(o3), [rs] "s"(rs), [T] "s"(T), [M] "s"(M), [stag] "n"(PSTAG)
      : PV_CLOB);
  R[0] = u32x4{r0.x, r0.y, r1.x, r1.y}; R[1] = u32x4{r2.x, r2.y, r3.x, r3.y}; R[2] = u32x4{r4.x, r4.y, r5.x, r5.y}; R[3] = u32x4{r6.x, r6.y, r7.x, r7.y};
}
#define PV_LD2(r0, r1, r2, r3) \
  "buffer_load_dwordx4 v[" r0 ":" r1 "], %[o0], %[rs], 0 offen sc1\n\t" "buffer_load_dwordx4 v[" r2 ":" r3 "], %[o0], %[rs], 0 offen offset:16 sc1\n\t"
__device__ __forceinline__ void poll2(rsrc_t rs, int o0, u32 T, u32 M, u32x4& a, u32x4& b) {
  u32x2 r0, r1, r2, r3; u32 cnt = SPIN_CAP;
  asm volatile(
      PV_LD2("128", "131", "132", "135")
      "s_sleep %[stag]\n"
      "1:\n\t"
      PV_LD2("144", "147", "148", "151")
      "s_waitcnt vmcnt(2)\n\t"
      PV_X4("v160", "128", "129", "130", "131") PV_X4("v161", "132", "133", "134", "135") "v_or_b32 v160, v160, v161\n\t" PV_TEST
      "s_cbranch_scc1 7f\n\t"
      PV_LD2("128", "131", "132", "135")
      "s_waitcnt vmcnt(2)\n\t"
      PV_X4("v160", "144", "145", "146", "147") PV_X4("v161", "148", "149", "150", "151") "v_or_b32 v160, v160, v161\n\t" PV_TEST
      "s_cbranch_scc1 8f\n\t"
      "s_sub_u32 %[cnt], %[cnt], 1\n\ts_cbranch_scc0 1b\n\t"
      "s_waitcnt vmcnt(0)\n"
      "7:\n\t"
      PV_MOV2("%[r0]", "128", "129") PV_MOV2("%[r1]", "130", "131") PV_MOV2("%[r2]", "132", "133") PV_MOV2("%[r3]", "134", "135")
      "s_branch 9f\n"
      "8:\n\t"
      PV_MOV2("%[r0]", "144", "145") PV_MOV2("%[r1]", "146", "147") PV_MOV2("%[r2]", "148", "149") PV_MOV2("%[r3]", "150", "151")
      "9:\n\t"
      : [r0] "=&v"(r0), [r1] "=&v"(r1), [r2] "=&v"(r2), [r3] "=&v"(r3), [cnt] "+s"(cnt)
      : [o0] "v"(o0), [rs] "s"(rs), [T] "s"(T), [M] "s"(M), [stag] "n"(PSTAG)
      : PV_CLOB);
  a = u32x4{r0.x, r0.y, r1.x, r1.y}; b = u32x4{r2.x, r2.y, r3.x, r3.y};
}
#define PV_LD3(r0, r1, r2, r3, r4) \
  "buffer_load_dwordx4 v[" r0 ":" r1 "], %[o0], %[rs], 0 offen sc1\n\t" "buffer_load_dwordx4 v[" r2 ":" r3 "], %[o0], %[rs], 0 offen offset:16 sc1\n\t" \
  "buffer_load_dword v" r4 ", %[o1], %[rs], 0 offen sc1\n\t"
__device__ __forceinline__ void poll3(rsrc_t rs, int o0, int o1, u32 T, u32 M, u32x4& a, u32x4& b, u32& c) {
  u32x2 r0, r1, r2, r3; u32 r4, cnt = SPIN_CAP;
  asm volatile(
      PV_LD3("128", "131", "132", "135", "136")
      "s_sleep %[stag]\n"
      "1:\n\t"
      PV_LD3("144", "147", "148", "151", "152")
      "s_waitcnt vmcnt(3)\n\t"
      PV_X4("v160", "128", "129", "130", "131") PV_X4("v161", "132", "133", "134", "135") "v_xor_b32 v162, %[T], v136\n\t"
      "v_or3_b32 v160, v160, v161, v162\n\t" PV_TEST
      "s_cbranch_scc1 7f\n\t"
      PV_LD3("128", "131", "132", "135", "136")
      "s_waitcnt vmcnt(3)\n\t"
      PV_X4("v160", "144", "145", "146", "147") PV_X4("v161", "148", "149", "150", "151") "v_xor_b32 v162, %[T], v152\n\t"
      "v_or3_b32 v160, v160, v161, v162\n\t" PV_TEST
      "s_cbranch_scc1 8f\n\t"
      "s_sub_u32 %[cnt], %[cnt], 1\n\ts_cbranch_scc0 1b\n\t"
      "s_waitcnt vmcnt(0)\n"
      "7:\n\t"
      PV_MOV2("%[r0]", "128", "129") PV_MOV2("%[r1]", "130", "131") PV_MOV2("%[r2]", "132", "133") PV_MOV2("%[r3]", "134", "135") "v_mov_b32 %[r4], v136\n\t"
      "s_branch 9f\n"
      "8:\n\t"
      PV_MOV2("%[r0]", "144", "145") PV_MOV2("%[r1]", "146", "147") PV_MOV2("%[r2]", "148", "149") PV_MOV2("%[r3]", "150", "151") "v_mov_b32 %[r4], v152\n\t"
      "9:\n\t"
      : [r0] "=&v"(r0), [r1] "=&v"(r1), [r2] "=&v"(r2), [r3] "=&v"(r3), [r4] "=&v"(r4), [cnt] "+s"(cnt)
      : [o0] "v"(o0), [o1] "v"(o1), [rs] "s"(rs), [T] "s"(T), [M] "s"(M), [stag] "n"(PSTAG)
      : PV_CLOB);
  a = u32x4{r0.x, r0.y, r1.x, r1.y}; b = u32x4{r2.x, r2.y, r3.x, r3.y}; c = r4;
}

__device__ __forceinline__ unsigned long long now() { return __builtin_amdgcn_s_memrealtime(); }
__device__ __forceinline__ void not_before(unsigned long long t0, unsigned ticks) {
  while (now() - t0 < ticks) __builtin_amdgcn_s_sleep(2);
}
__device__ __forceinline__ u32 lds_ld(u32 addr) {
  u32 v;
  asm volatile("ds_read_b32 %0, %1\n\ts_waitcnt lgkmcnt(0)" : "=v"(v) : "v"(addr) : "memory");
  return v;
}
__device__ __forceinline__ void lds_st(u32 addr, u32 v) { asm volatile("ds_write_b32 %0, %1" ::"v"(addr), "v"(v) : "memory"); }
__device__ __forceinline__ void wait_word(int word, u32 want) {
  unsigned spins = 0;
#pragma clang loop unroll(disable)
  while ((u32)__builtin_amdgcn_readfirstlane(lds_ld(L_FL + 4 * word)) < want)
    if (++spins > LSPIN_CAP) break;
}
__device__ __forceinline__ void wait_word_sleep(int word, u32 want) {
  unsigned spins = 0;
#pragma clang loop unroll(disable)
  while ((u32)__builtin_amdgcn_readfirstlane(lds_ld(L_FL + 4 * word)) < want) {
    __builtin_amdgcn_s_sleep(2);
    if (++spins > LSPIN_CAP) break;
  }
}
__device__ __forceinline__ void wait_all8(int word0, u32 want, int lane) {
  unsigned spins = 0;
#pragma clang loop unroll(disable)
  while (!__all(lds_ld(L_FL + 4 * (word0 + (lane & 7))) >= want)) {
    __builtin_amdgcn_s_sleep(1);
    if (++spins > LSPIN_CAP) break;
  }
}
__device__ __forceinline__ void cbar(u32 n, int lane) {
  asm volatile("s_waitcnt lgkmcnt(0)" ::: "memory");
  if (lane == 0) asm volatile("ds_add_u32 %0, %1" ::"v"((u32)(L_FL + 4 * F_BAR)), "v"(1u) : "memory");
  wait_word(F_BAR, 8u * n);
}
__device__ __forceinline__ void hw_barrier() { __builtin_amdgcn_sched_barrier(0); __builtin_amdgcn_s_barrier(); __builtin_amdgcn_sched_barrier(0); }
__device__ __forceinline__ void wg_barrier() {
  asm volatile("s_waitcnt lgkmcnt(0)" ::: "memory");
  hw_barrier();
  asm volatile("" ::: "memory");
}
__device__ __forceinline__ void glds16(const void* sbase, u32 voff, u32 lds_dst) {
  asm volatile("s_mov_b32 m0, %2\n\ts_nop 0\n\tglobal_load_lds_dwordx4 %0, %1 nt" ::"v"(voff), "s"(sbase), "s"(lds_dst) : "memory");
}
__device__ __forceinline__ int vswz(int row) { return ((row & 3) << 2) | ((row >> 2) & 3); }
__device__ __forceinline__ int swz(int row) { return ((row >> 1) & 1) | ((row >> 2) & 2); }
__device__ __forceinline__ u32 rowoff(int q, int stride, int lane) { const int row = 8 * q + (lane >> 3); return row * stride + (((lane & 7) ^ (swz(row) << 1)) & 7) * 16; }
template <int W> __device__ __forceinline__ void tile_dma(const unsigned char* a, int stride, u32 o0, u32 o1, u32 dst) {
  glds16(a, o0, dst); if (W >= 0) waitvm<W>();
  glds16(a, o1, dst + 1024); if (W >= 0) waitvm<W>();
  glds16(a + (size_t)16 * stride, o0, dst + 2048); if (W >= 0) waitvm<W>();
  glds16(a + (size_t)16 * stride, o1, dst + 3072); if (W >= 0) waitvm<W>();
}
__device__ __forceinline__ void tile_frags_lds(const unsigned char* lds, u32 base, int lane, u32x4 (&f)[4]) {
  const int g = lane >> 4, q = (lane & 15) >> 2, p = lane & 3;
#pragma unroll
  for (int e = 0; e < 4; ++e) {
    u32x2 h[2];
#pragma unroll
    for (int t = 0; t < 2; ++t) {
      const int row = 8 * g + 4 * t + q;
      const u32 a = base + row * 128 + (((2 * e + (p >> 1)) ^ (swz(row) << 1)) << 4) + 8 * (p & 1);
      h[t] = __builtin_bit_cast(u32x2, __builtin_amdgcn_ds_read_tr16_b64_v4i16((LAS s16x4*)(lds + a)));
    }
    f[e] = u32x4{h[0].x, h[0].y, h[1].x, h[1].y};
  }
}

#define ESTAMP(k) do { } while (0)
#define STAMP(k) do { } while (0)
#define STAMP_FLUSH() do { } while (0)

__device__ __forceinline__ u32x4 afrag(u32x4 a, u32x4 b) {
  u32x4 f;
  f.x = __builtin_amdgcn_perm(a.y, a.x, 0x07060302u);
  f.y = __builtin_amdgcn_perm(a.w, a.z, 0x07060302u);
  f.z = __builtin_amdgcn_perm(b.y, b.x, 0x07060302u);
  f.w = __builtin_amdgcn_perm(b.w, b.z, 0x07060302u);
  return f;
}

struct RowStats { float m, rs; };
template <int N> __device__ __forceinline__ RowStats row_stats(const float (&v)[N], const bool (&valid)[N], float pivot) {
  float s1[2] = {0.f, 0.f}, s2[2] = {0.f, 0.f};
#pragma unroll
  for (int e = 0; e < N; ++e) { const float d = valid[e] ? v[e] - pivot : 0.f; s1[e & 1] += d; s2[e & 1] = fmaf(d, d, s2[e & 1]); }
  const float m1 = wave_sum(s1[0] + s1[1]) * (1.0f / DM), m2 = wave_sum(s2[0] + s2[1]) * (1.0f / DM);
  const float var = rbf(fmax_(fmaf(-m1, m1, m2), 0.f));
  RowStats r;
  r.m = rbf(pivot + m1);
  r.rs = __builtin_amdgcn_rcpf(rbf(__builtin_amdgcn_sqrtf(rbf(var + 1e-5f))));
  return r;
}
__device__ __forceinline__ float ln_apply(float v, RowStats s) { return rbf(v - s.m) * s.rs; }

enum { ROLE_W = 0, ROLE_G = 1, ROLE_F = 2 };

__global__ void __launch_bounds__(NTHR, 3) fused_kernel(Params p) {
  extern __shared__ __attribute__((aligned(16))) unsigned char lds[];
  const int tid = threadIdx.x, lane = tid & 63, w = __builtin_amdgcn_readfirstlane(tid >> 6);
  const int wg = blockIdx.x, xcd = wg & 7, s = wg >> 3, j = xcd + 8 * (s >> 4), i = s & 15, b = i >> 1, half = i & 1;
  const size_t ubase = ((size_t)(b * HKV + j) * SK + (SK - WIN)) * DH;
  const int cq = i & 3, kp = i >> 2;
  const int ks0 = kp < 3 ? 4 * kp : 13, nt = kp < 2 ? 4 : 5;
  const u32 PK = 4 * nt, PV = PK + 128, PW = PK + 256;
  const int g4 = lane >> 4, c16 = lane & 15;
#define RSTAMP(k)
#define PMARK(n)
  STAMP(0);
  const int cw = (w + 7) & 7;
  u32 xr[5];
  u32x4 xa;
  {
    const GAS u32* xrow = (const GAS u32*)(p.x + cw * DM);
#pragma unroll
    for (int m = 0; m < 4; ++m) xr[m] = __builtin_nontemporal_load(xrow + lane + 64 * m);
    xr[4] = __builtin_nontemporal_load(xrow + (lane & 31) + 256);
    xa = __builtin_nontemporal_load((const GAS u32x4*)(p.x + (lane & 7) * DM + 32 * (ks0 + (cw < nt ? cw : 0)) + 8 * g4));
  }
  if (w == 0) {
    if (lane < 32) lds_st(L_FL + 4 * lane, 0u);
    asm volatile("s_waitcnt lgkmcnt(0)" ::: "memory");
  }
  hw_barrier();

  if (w == 0) {
    __builtin_amdgcn_s_setprio(3);
    u32 pub = 0;
#define PUBLISH(W) lds_st(L_FL + 4 * F_LANDED, pub - (W))
    {
      const unsigned char* a = (const unsigned char*)(p.wq + (size_t)32 * ks0 * NQ + 256 * j + 64 * cq);
      const u32 o0 = rowoff(0, NQ * 2, lane), o1 = rowoff(1, NQ * 2, lane);
#pragma unroll
      for (int t = 0; t < 4; ++t) tile_dma<-1>(a + (size_t)t * 32 * NQ * 2, NQ * 2, o0, o1, L_EXT + t * 4096);
      if (nt == 5) tile_dma<-1>(a + (size_t)4 * 32 * NQ * 2, NQ * 2, o0, o1, L_QSUM);
      pub = PK;
    }
    ESTAMP(8);
    {
      const unsigned char* kb = (const unsigned char*)(p.kc + ubase + (size_t)half * 512 * DH);
      u32 koff[4];
#pragma unroll
      for (int q = 0; q < 4; ++q) { const int key = 4 * q + g4; koff[q] = key * 256 + ((c16 ^ key) & 15) * 16; }
#pragma clang loop unroll(disable)
      for (int t = 0; t < 32; ++t) {
#pragma unroll
        for (int q = 0; q < 4; ++q) { glds16(kb + t * 4096, koff[q], (t * 4 + q) * 1024); waitvm<LWIN>(); }
        pub += 4;
        if (pub > LWIN) PUBLISH(LWIN);
      }
    }
    STAMP(1);
    {
      const unsigned char* vb = (const unsigned char*)(p.vc + ubase + (size_t)half * 512 * DH);
      u32 voff[4];
#pragma unroll
      for (int q = 0; q < 4; ++q) { const int key = 4 * q + g4; voff[q] = key * 256 + ((c16 ^ vswz(key)) & 15) * 16; }
      wait_word(F_QPUB, 1u);
#pragma clang loop unroll(disable)
      for (int t = 0; t < 32; ++t) {
        if ((t & 7) == 4) wait_all8(F_KDONE, (t >> 3) + 1, lane);
        const u32 dst = t < 4 ? L_EXT + t * 4096 : (t - 4) * 4096;
#pragma unroll
        for (int q = 0; q < 4; ++q) { glds16(vb + t * 4096, voff[q], dst + q * 1024); waitvm<LWIN>(); }
        pub += 4; PUBLISH(LWIN);
      }
    }
    STAMP(2);
    wait_word_sleep(F_RANK, 1u);
    const int rank = (int)__builtin_amdgcn_readfirstlane(lds_ld(L_FL + 4 * F_RANK)) - 1;
    const int role = rank < 72 ? ROLE_G : (rank < 216 ? ROLE_W : ROLE_F);
    if (role == ROLE_W) {
      const int jw = (rank - 72) / 9, cg = (rank - 72) % 9;
      const unsigned char* a = (const unsigned char*)(p.wo + (size_t)256 * jw * DM + 64 * cg);
      const u32 o0 = rowoff(0, DM * 2, lane), o1 = rowoff(1, DM * 2, lane);
      wait_all8(F_VDONE, 1, lane);
#pragma clang loop unroll(disable)
      for (int t = 0; t < 8; ++t) { tile_dma<LWIN>(a + (size_t)t * 32 * DM * 2, DM * 2, o0, o1, t * 4096); pub += 4; PUBLISH(LWIN); }
    } else if (role == ROLE_G) {
      const int gx = rank;
      const u32 o0 = rowoff(0, FF * 2, lane), o1 = rowoff(1, FF * 2, lane);
#pragma clang loop unroll(disable)
      for (int t = 0; t < 36; ++t) {
        if (t == 0 || t == 16) wait_all8(F_VDONE, (t >> 4) + 1, lane);
        const unsigned char* a = (const unsigned char*)(((t & 1) ? p.wu : p.wg) + (size_t)(t >> 1) * 32 * FF + 64 * gx);
        tile_dma<GWIN>(a, FF * 2, o0, o1, t < 4 ? L_EXT + t * 4096 : (t - 4) * 4096);
        pub += 4;
        if (4 * t + 4 > GWIN - LWIN) PUBLISH(GWIN);
      }
    }
    STAMP(3);
#define DR(N) if (GWIN > N && (role == ROLE_G || LWIN > N)) { waitvm<N>(); PUBLISH(N); }
    DR(44) DR(40) DR(36) DR(32) DR(28) DR(24) DR(20) DR(16) DR(12) DR(8) DR(4) DR(0)
#undef DR
#undef PUBLISH
    STAMP(14);
    STAMP_FLUSH();
    return;
  }

  const int ct = tid - 64;
  const rsrc_t rs = __builtin_amdgcn_make_buffer_rsrc((void*)p.ws, 0, WS_END, 0x00020000);
  const u32 T = *(const __attribute__((address_space(4))) u32*)(p.ws + WS_EPOCH) + 1u;
  const u32 T16 = T & 0xFFFFu, T8 = T & 0xFFu, T4 = T & 0xFu;
  const u32 tstart = (u32)now();
  if (ct == 448) __builtin_amdgcn_raw_buffer_store_b64(u32x2{tstart, T}, rs, WS_RANK + wg * 8, 0, AUX_SC1);
  float* const lf = (float*)lds;
  u32* const lu = (u32*)lds;
  {
    ESTAMP(8);
    asm volatile("" :: "v"(xr[0]), "v"(xr[4]));
    ESTAMP(9);
    float v[10]; bool valid[10];
#pragma unroll
    for (int m = 0; m < 5; ++m) { valid[2 * m] = valid[2 * m + 1] = (m < 4) || (lane < 32); v[2 * m] = bflo(xr[m]); v[2 * m + 1] = bfhi(xr[m]); }
    const float pivot = __uint_as_float(__builtin_amdgcn_readfirstlane(xr[0]) << 16);
    const RowStats st = row_stats<10>(v, valid, pivot);
    if (lane == 0) *(f32x2*)(lf + L_ST / 4 + 2 * cw) = f32x2{st.m, st.rs};
    ESTAMP(10);
    cbar(1, lane);
    STAMP(1);
    PMARK(1);
  }
  if (cw < nt) {
    const f32x2 sv = *(const f32x2*)(lf + L_ST / 4 + 2 * (lane & 7));
    const RowStats st = {sv.x, sv.y};
    u32x4 a;
#pragma unroll
    for (int q = 0; q < 4; ++q) a[q] = pack2(ln_apply(bflo(xa[q]), st), ln_apply(bfhi(xa[q]), st));
    const u32 base = cw < 4 ? L_EXT + cw * 4096 : L_QSUM;
    wait_word(F_LANDED, 4 * (cw + 1));
    u32x4 f[4]; tile_frags_lds(lds, base, lane, f);
    f32x4 d[4];
#pragma unroll
    for (int e = 0; e < 4; ++e) d[e] = mfma(f[e], a, f32x4{0.f, 0.f, 0.f, 0.f});
    if (c16 < 8) {
#pragma unroll
      for (int e = 0; e < 4; ++e) *(f32x4*)(lf + base / 4 + c16 * 64 + 16 * e + 4 * g4) = d[e];
    }
  }
  ESTAMP(11);
  cbar(2, lane);
  ESTAMP(12);
  {
    float sum = (lf[L_EXT / 4 + cw * 64 + lane] + lf[L_EXT / 4 + 1024 + cw * 64 + lane]) + (lf[L_EXT / 4 + 2048 + cw * 64 + lane] + lf[L_EXT / 4 + 3072 + cw * 64 + lane]);
    if (nt == 5) sum += lf[L_QSUM / 4 + cw * 64 + lane];
    const u32 v = put_tag<0xFu>(sum, T4);
    const u32x4 o = {v, (u32)__builtin_amdgcn_mov_dpp((int)v, 0x55, 0xf, 0xf, true), (u32)__builtin_amdgcn_mov_dpp((int)v, 0xAA, 0xf, 0xf, true),
                     (u32)__builtin_amdgcn_mov_dpp((int)v, 0xFF, 0xf, 0xf, true)};
    if ((lane & 3) == 0) st16c(rs, WS_QS + ((((j * 8 + cw) * 4 + kp) * 256) + 64 * cq + lane) * 4, o);
  }
  cbar(3, lane);
  if (ct == 0) lds_st(L_FL + 4 * F_QPUB, 1u);
  STAMP(2);
  PMARK(2);
  if (cw >= 2 && cw < 6) {
    u32x4 qg;
    __builtin_amdgcn_s_sleep(QDELAY);
    {
      unsigned spins = 0;
#pragma clang loop unroll(disable)
      for (;;) {
        qg = ld16c(rs, WS_QS + (((j * 8 + b) * 4 + (cw - 2)) * 256 + 4 * lane) * 4);
        if (__all(tag_ok<0xFu>(qg, T4)) || ++spins > SPIN_CAP) break;
        __builtin_amdgcn_s_sleep(4);
      }
    }
    *(f32x4*)(lf + L_QSUM / 4 + (cw - 2) * 256 + 4 * lane) = f32x4{get_tag<0xFu>(qg.x), get_tag<0xFu>(qg.y), get_tag<0xFu>(qg.z), get_tag<0xFu>(qg.w)};
  }
  cbar(4, lane);
  if (cw == 0) {
    f32x4 sv[4];
#pragma unroll
    for (int q = 0; q < 4; ++q) sv[q] = *(const f32x4*)(lf + L_QSUM / 4 + q * 256 + 4 * lane);
    const f32x4 sum = (sv[0] + sv[1]) + (sv[2] + sv[3]);
    *(u32x2*)(lu + L_Q / 4 + 2 * lane) = u32x2{pack2(sum.x, sum.y), pack2(sum.z, sum.w)};
    asm volatile("s_waitcnt lgkmcnt(0)" ::: "memory");
    if (lane == 0) lds_st(L_FL + 4 * F_QRDY, 1u);
  } else {
    wait_word(F_QRDY, 1u);
  }
  STAMP(3);
  PMARK(3);
  float s0, s1;
  {
    u32x4 qa[4];
#pragma unroll
    for (int ks = 0; ks < 4; ++ks) qa[ks] = *(const u32x4*)(lu + L_Q / 4 + (lane & 1) * 64 + 16 * ks + 4 * g4);
    f32x4 d[4];
#pragma unroll
    for (int m = 0; m < 4; ++m) {
      const int tile = cw + 8 * m;
      wait_word_sleep(F_LANDED, PK + 4 * (tile + 1));
      u32x4 kf[4];
#pragma unroll
      for (int ks = 0; ks < 4; ++ks) kf[ks] = *(const u32x4*)(lds + tile * 4096 + c16 * 256 + (((4 * ks + g4) ^ c16) << 4));
      f32x4 acc = mfma(qa[0], kf[0], f32x4{0.f, 0.f, 0.f, 0.f});
      acc = mfma(qa[1], kf[1], acc); acc = mfma(qa[2], kf[2], acc); acc = mfma(qa[3], kf[3], acc);
      d[m] = acc;
      asm volatile("s_waitcnt lgkmcnt(0)" ::: "memory");
      if (lane == 0) lds_st(L_FL + 4 * (F_KDONE + cw), m + 1);
    }
    const float x0 = g4 == 0 ? d[0][0] : g4 == 1 ? d[1][0] : g4 == 2 ? d[2][0] : d[3][0];
    const float x1 = g4 == 0 ? d[0][1] : g4 == 1 ? d[1][1] : g4 == 2 ? d[2][1] : d[3][1];
    constexpr float SCALE = 0.08838834764831845f;
    s0 = rbf(rbf(x0) * SCALE); s1 = rbf(rbf(x1) * SCALE);
  }
  STAMP(4);
  PMARK(4);
  u32x4 mine;
  {
    constexpr float L2E = 1.4426950408889634f;
    const float m0 = wave_max(s0), m1 = wave_max(s1);
    const float l0 = wave_sum(__builtin_amdgcn_exp2f((s0 - m0) * L2E)), l1 = wave_sum(__builtin_amdgcn_exp2f((s1 - m1) * L2E));
    if (lane == 0) *(f32x4*)(lf + L_ST / 4 + 16 + 4 * cw) = f32x4{m0, m1, l0, l1};
    cbar(5, lane);
    float M0 = -INFINITY, M1 = -INFINITY;
    f32x4 st[8];
#pragma unroll
    for (int ww = 0; ww < 8; ++ww) { st[ww] = *(const f32x4*)(lf + L_ST / 4 + 16 + 4 * ww); M0 = fmax_(M0, st[ww].x); M1 = fmax_(M1, st[ww].y); }
    float L0 = 0.f, L1 = 0.f;
#pragma unroll
    for (int ww = 0; ww < 8; ++ww) {
      L0 = fmaf(st[ww].z, __builtin_amdgcn_exp2f((st[ww].x - M0) * L2E), L0);
      L1 = fmaf(st[ww].w, __builtin_amdgcn_exp2f((st[ww].y - M1) * L2E), L1);
    }
    const int kl = 16 * (cw + 8 * g4) + c16;
    __bf16* P = (__bf16*)(lds + L_P);
    P[kl] = (__bf16)__builtin_amdgcn_exp2f((s0 - M0) * L2E);
    P[512 + kl] = (__bf16)__builtin_amdgcn_exp2f((s1 - M1) * L2E);
    mine = u32x4{__float_as_uint(M0) | T16, __float_as_uint(M1) | T16, put_tag<0xFFu>(L0, T8), put_tag<0xFFu>(L1, T8)};
    if (ct == 64) st16c(rs, WS_SC + wg * 128, mine);
  }
  cbar(6, lane);
  STAMP(6);
  if (cw == 7) {
    u32x4 e0, e1; unsigned spins = 0;
#pragma clang loop unroll(disable)
    for (;;) {
      e0 = ld16c(rs, WS_RANK + lane * 32); e1 = ld16c(rs, WS_RANK + lane * 32 + 16);
      if (__all((e0.y == T) & (e0.w == T) & (e1.y == T) & (e1.w == T)) || ++spins > SPIN_CAP) break;
      __builtin_amdgcn_s_sleep(4);
    }
    auto before = [&](u32 t, int g) -> float { const int d = (int)(t - tstart); return (d < 0 || (d == 0 && g < wg)) ? 1.f : 0.f; };
    const float n = wave_sum((before(e0.x, 4 * lane) + before(e0.z, 4 * lane + 1)) + (before(e1.x, 4 * lane + 2) + before(e1.z, 4 * lane + 3)));
    if (lane == 0) lds_st(L_FL + 4 * F_RANK, (u32)n + 1u);
  }
  if (cw == 1) {
    constexpr float L2E = 1.4426950408889634f;
    u32x4 oth; unsigned spins = 0;
#pragma clang loop unroll(disable)
    for (;;) {
      oth = ld16c(rs, WS_SC + (wg ^ 8) * 128);
      if (__all(((((oth.x ^ T16) | (oth.y ^ T16)) & 0xFFFFu) | (((oth.z ^ T8) | (oth.w ^ T8)) & 0xFFu)) == 0u) || ++spins > SPIN_CAP) break;
      __builtin_amdgcn_s_sleep(4);
    }
    const float M0 = bfhi(mine.x), M1 = bfhi(mine.y), om0 = bfhi(oth.x), om1 = bfhi(oth.y);
    const float G0 = fmax_(M0, om0), G1 = fmax_(M1, om1);
    const float e0 = __builtin_amdgcn_exp2f((M0 - G0) * L2E), e1 = __builtin_amdgcn_exp2f((M1 - G1) * L2E);
    const float S0 = get_tag<0xFFu>(mine.z) * e0 + get_tag<0xFFu>(oth.z) * __builtin_amdgcn_exp2f((om0 - G0) * L2E);
    const float S1 = get_tag<0xFFu>(mine.w) * e1 + get_tag<0xFFu>(oth.w) * __builtin_amdgcn_exp2f((om1 - G1) * L2E);
    if (lane == 0) *(f32x2*)(lf + L_ST / 4 + 48) = f32x2{e0 / S0, e1 / S1};
    STAMP(5);
  }
  {
    f32x4 d[8];
#pragma unroll
    for (int e = 0; e < 8; ++e) d[e] = f32x4{0.f, 0.f, 0.f, 0.f};
    const int q4 = (lane & 15) >> 2, p4 = lane & 3;
#pragma unroll
    for (int m = 0; m < 2; ++m) {
      const int ks = cw + 8 * m;
      const u32x4 pf = *(const u32x4*)(lds + L_P + (lane & 1) * 1024 + (32 * ks + 8 * g4) * 2);
      const u32 base = ks < 2 ? L_EXT + ks * 8192 : (ks - 2) * 8192;
      wait_word(F_LANDED, PV + 8 * (ks + 1));
      u32x4 f[8];
#pragma unroll
      for (int e = 0; e < 8; ++e) {
        u32x2 h[2];
#pragma unroll
        for (int t = 0; t < 2; ++t) {
          const int row = 8 * g4 + 4 * t + q4;
          const u32 a = base + row * 256 + (((2 * e + (p4 >> 1)) ^ vswz(row)) << 4) + 8 * (p4 & 1);
          h[t] = __builtin_bit_cast(u32x2, __builtin_amdgcn_ds_read_tr16_b64_v4i16((LAS s16x4*)(lds + a)));
        }
        f[e] = u32x4{h[0].x, h[0].y, h[1].x, h[1].y};
      }
      SB();
#pragma unroll
      for (int e = 0; e < 8; ++e) d[e] = mfma(f[e], pf, d[e]);
      asm volatile("s_waitcnt lgkmcnt(0)" ::: "memory");
      if (lane == 0) lds_st(L_FL + 4 * (F_VDONE + cw), m + 1);
    }
    if (c16 < 2) {
#pragma unroll
      for (int e = 0; e < 8; ++e) *(f32x4*)(lf + L_QSUM / 4 + cw * 256 + c16 * 128 + 16 * e + 4 * g4) = d[e];
    }
  }
  cbar(7, lane);
  auto gather4 = [&](u32 v) -> u32x4 {
    return u32x4{v, (u32)__builtin_amdgcn_mov_dpp((int)v, 0x55, 0xf, 0xf, true), (u32)__builtin_amdgcn_mov_dpp((int)v, 0xAA, 0xf, 0xf, true),
                 (u32)__builtin_amdgcn_mov_dpp((int)v, 0xFF, 0xf, 0xf, true)};
  };
  if (cw < 4) {
    float sv[8];
#pragma unroll
    for (int ww = 0; ww < 8; ++ww) sv[ww] = lf[L_QSUM / 4 + ww * 256 + ct];
    const float sum = (((sv[0] + sv[1]) + (sv[2] + sv[3])) + ((sv[4] + sv[5]) + (sv[6] + sv[7]))) * lf[L_ST / 4 + 48 + (cw >> 1)];
    const u32x4 o = gather4(put_tag<0xFFu>(sum, T8));
    if ((lane & 3) == 0) st16c(rs, WS_OS + (((j * 8 + b) * 2 + half) * 256 + ct) * 4, o);
  }
  const unsigned long long to = now();
  wait_word(F_RANK, 1u);
  const int rank = (int)__builtin_amdgcn_readfirstlane(lds_ld(L_FL + 4 * F_RANK)) - 1;
  const int role = rank < 72 ? ROLE_G : (rank < 216 ? ROLE_W : ROLE_F);
  STAMP(7);
  PMARK(7);

  auto acc_to_lds = [&](int base, const f32x4 (&d)[4]) {
    if (c16 < 8) {
#pragma unroll
      for (int e = 0; e < 4; ++e) *(f32x4*)(lf + base / 4 + (cw * 8 + c16) * 64 + 16 * e + 4 * g4) = d[e];
    }
  };
  auto publish_partial = [&](int base, int jw, int cg) {
    float sv[8];
#pragma unroll
    for (int ww = 0; ww < 8; ++ww) sv[ww] = lf[L_EXT / 4 + ww * 512 + cw * 64 + lane];
    const float sum = ((sv[0] + sv[1]) + (sv[2] + sv[3])) + ((sv[4] + sv[5]) + (sv[6] + sv[7]));
    const u32x4 o = gather4(put_tag<0xFFu>(sum, T8));
    if ((lane & 3) == 0) st16c(rs, base + (((cw * 9 + cg) * 16 + jw) * 64 + lane) * 4, o);
  };

  if (role == ROLE_W) {
    const int jw = (rank - 72) / 9, cg = (rank - 72) % 9;
    u32x4 f[4];
    wait_word(F_LANDED, PW + 32);
    tile_frags_lds(lds, cw * 4096, lane, f);
    {
      const int off = WS_OS + ((jw * 8 + (lane & 7)) * 512 + 32 * cw + 8 * g4) * 4;
      u32x4 a0 = {0u, 0u, 0u, 0u}, a1 = a0, b0 = a0, b1 = a0;
      if (c16 < 8)
        POLL((a0 = ld16c(rs, off), a1 = ld16c(rs, off + 16), b0 = ld16c(rs, off + 1024), b1 = ld16c(rs, off + 1040),
              tag_ok<0xFFu>(a0, T8) & tag_ok<0xFFu>(a1, T8) & tag_ok<0xFFu>(b0, T8) & tag_ok<0xFFu>(b1, T8)));
      STAMP(8);
      RSTAMP(0);
#define S2(x, y) (get_tag<0xFFu>(x) + get_tag<0xFFu>(y))
      const u32x4 a = {pack2(S2(a0.x, b0.x), S2(a0.y, b0.y)), pack2(S2(a0.z, b0.z), S2(a0.w, b0.w)), pack2(S2(a1.x, b1.x), S2(a1.y, b1.y)), pack2(S2(a1.z, b1.z), S2(a1.w, b1.w))};
#undef S2
      f32x4 d[4];
#pragma unroll
      for (int e = 0; e < 4; ++e) d[e] = mfma(f[e], a, f32x4{0.f, 0.f, 0.f, 0.f});
      acc_to_lds(L_EXT, d);
    }
    wg_barrier();
    publish_partial(WS_PW, jw, cg);
    const unsigned long long tw = now();
    STAMP(9);
    u32x4 f2[4];
    {
      const unsigned char* a = (const unsigned char*)(p.wd + (size_t)(288 * jw + 32 * cw) * DM + 64 * cg);
      const u32 o0 = rowoff(0, DM * 2, lane), o1 = rowoff(1, DM * 2, lane);
      tile_dma<-1>(a, DM * 2, o0, o1, (8 + cw) * 4096);
      if (cw == 0) tile_dma<-1>(a + 256 * DM * 2, DM * 2, o0, o1, 16 * 4096);
      waitvm<0>();
      tile_frags_lds(lds, (8 + cw) * 4096, lane, f);
      if (cw == 0) tile_frags_lds(lds, 16 * 4096, lane, f2);
    }
    {
      const int off = WS_AS + ((lane & 7) * FF + 288 * jw + 32 * cw + 8 * g4) * 4;
      u32x4 R[4] = {};
      not_before(tw, GATE_ACT);
      if (c16 < 8) {
        if (cw == 0) poll4(rs, off, off + 16, off + 1024, off + 1040, T16, 0xFFFFu, R);
        else poll2(rs, off, T16, 0xFFFFu, R[0], R[1]);
      }
      STAMP(12);
      const u32x4 a = afrag(R[0], R[1]);
      f32x4 d[4];
#pragma unroll
      for (int e = 0; e < 4; ++e) d[e] = mfma(f[e], a, f32x4{0.f, 0.f, 0.f, 0.f});
      if (cw == 0) {
        const u32x4 ab = afrag(R[2], R[3]);
#pragma unroll
        for (int e = 0; e < 4; ++e) d[e] = mfma(f2[e], ab, d[e]);
      }
      acc_to_lds(L_EXT, d);
    }
    wg_barrier();
    publish_partial(WS_PD, jw, cg);
    STAMP(13);
  } else if (role == ROLE_G) {
    const int gx = rank;
    u32x4 f[5][4];
#pragma unroll
    for (int tl = 0; tl < 5; ++tl) {
      if (tl < 4 || cw < 4) {
        const int tau = cw + 8 * tl;
        wait_word(F_LANDED, PW + 4 * (tau + 1));
        tile_frags_lds(lds, tau < 4 ? L_EXT + tau * 4096 : (tau - 4) * 4096, lane, f[tl]);
      }
    }
    wait_word(F_LANDED, PW + 144);
    STAMP(8);
    {
      const int off = WS_HS + (xcd * 8 + cw) * DM * 4;
      u32x4 h0, h1; u32 h2;
      not_before(to, GATE_H);
      poll3(rs, off + lane * 32, off + 2048 + lane * 4, T16, 0xFFFFu, h0, h1, h2);
      STAMP(10);
      float v[9]; bool valid[9];
#pragma unroll
      for (int e = 0; e < 4; ++e) { v[e] = bfhi(h0[e]); v[4 + e] = bfhi(h1[e]); }
      v[8] = bfhi(h2);
#pragma unroll
      for (int e = 0; e < 9; ++e) valid[e] = true;
      const float pivot = __uint_as_float(__builtin_amdgcn_readfirstlane(h0.x) & 0xFFFF0000u);
      const RowStats st = row_stats<9>(v, valid, pivot);
      u32* hn = lu + L_HN / 4 + cw * HN_STRIDE;
      *(u32x4*)(hn + 4 * lane) = u32x4{pack2(ln_apply(v[0], st), ln_apply(v[1], st)), pack2(ln_apply(v[2], st), ln_apply(v[3], st)),
                                       pack2(ln_apply(v[4], st), ln_apply(v[5], st)), pack2(ln_apply(v[6], st), ln_apply(v[7], st))};
      ((__bf16*)(hn + 256))[lane] = (__bf16)ln_apply(v[8], st);
    }
    wg_barrier();
    {
      f32x4 d[4];
      const u32* hn = lu + L_HN / 4 + (lane & 7) * HN_STRIDE + 16 * (cw >> 1) + 4 * g4;
      u32x4 a[5];
#pragma unroll
      for (int tl = 0; tl < 5; ++tl) a[tl] = *(const u32x4*)(hn + (tl < 4 || cw < 4 ? 64 * tl : 0));
      SB();
#pragma unroll
      for (int e = 0; e < 4; ++e) d[e] = mfma(f[0][e], a[0], f32x4{0.f, 0.f, 0.f, 0.f});
#pragma unroll
      for (int tl = 1; tl < 5; ++tl) {
        if (tl < 4 || cw < 4) {
#pragma unroll
          for (int e = 0; e < 4; ++e) d[e] = mfma(f[tl][e], a[tl], d[e]);
        }
      }
      acc_to_lds(0, d);
    }
    wg_barrier();
    {
      float gv[4], uv[4];
#pragma unroll
      for (int ww = 0; ww < 4; ++ww) { gv[ww] = lf[(2 * ww) * 512 + cw * 64 + lane]; uv[ww] = lf[(2 * ww + 1) * 512 + cw * 64 + lane]; }
      const float gg = rbf((gv[0] + gv[1]) + (gv[2] + gv[3])), uu = rbf((uv[0] + uv[1]) + (uv[2] + uv[3]));
      const float sl = rbf(gg * __builtin_amdgcn_rcpf(1.0f + __builtin_amdgcn_exp2f(gg * -1.4426950408889634f)));
      const u32x4 o = gather4(bfbits_hi(sl * uu) | T16);
      if ((lane & 3) == 0) st16c(rs, WS_AS + (cw * FF + 64 * gx + lane) * 4, o);
    }
    STAMP(11);
  } else {
    const int f = rank - 216;
    const int job = 2 * f + (cw >> 2);
    if ((cw & 3) == 0 && job < 72) {
      const int jb = job / 9, cg = job % 9;
      const u32x2 xq = *(const GAS u32x2*)(p.x + jb * DM + 64 * cg + 4 * c16);
      const u32x4 wr0 = *(const GAS u32x4*)(p.wr + 8 * lane);
      const u16 wr1 = ((const GAS u16*)p.wr)[512 + lane];
      auto gather16 = [&](int base) -> f32x4 {
        const int off = base + (((jb * 9 + cg) * 16 + g4) * 64 + 4 * c16) * 4;
        u32x4 R[4];
        poll4(rs, off, off + 1024, off + 2048, off + 3072, T8, 0xFFu, R);
        const u32x4 q0 = R[0], q1 = R[1], q2 = R[2], q3 = R[3];
        f32x4 sum;
#pragma unroll
        for (int e = 0; e < 4; ++e) {
          float t = (get_tag<0xFFu>(q0[e]) + get_tag<0xFFu>(q1[e])) + (get_tag<0xFFu>(q2[e]) + get_tag<0xFFu>(q3[e]));
          t = swap16_add(t, t); t = swap32_add(t, t);
          sum[e] = t;
        }
        return sum;
      };
      STAMP(3);
      not_before(to, GATE_PW);
      const f32x4 at = gather16(WS_PW);
      STAMP(8);
      PMARK(8);
      RSTAMP(0);
      f32x4 hv;
      hv[0] = rbf(bflo(xq.x) + rbf(at[0])); hv[1] = rbf(bfhi(xq.x) + rbf(at[1]));
      hv[2] = rbf(bflo(xq.y) + rbf(at[2])); hv[3] = rbf(bfhi(xq.y) + rbf(at[3]));
      {
        const u32x4 hq = {__float_as_uint(hv[0]) | T16, __float_as_uint(hv[1]) | T16, __float_as_uint(hv[2]) | T16, __float_as_uint(hv[3]) | T16};
        const int o = WS_HS + ((g4 * 8 + jb) * DM + 64 * cg + 4 * c16) * 4;
        st16c(rs, o, hq); st16c(rs, o + 4 * 8 * DM * 4, hq);
      }
      const unsigned long long th = now();
      STAMP(9);
      RowStats st; float sig;
      {
        const int off = WS_HS + (xcd * 8 + jb) * DM * 4;
        u32x4 h0, h1; u32 h2;
        {
          unsigned spins = 0;
#pragma clang loop unroll(disable)
          for (;;) {
            h0 = ld16c(rs, off + lane * 32); h1 = ld16c(rs, off + lane * 32 + 16); h2 = ld4c(rs, off + 2048 + lane * 4);
            if (__all(tag_ok<0xFFFFu>(h0, T16) & tag_ok<0xFFFFu>(h1, T16) & ((h2 & 0xFFFFu) == T16)) || ++spins > SPIN_CAP) break;
            __builtin_amdgcn_s_sleep(2);
          }
        }
        float v[9]; bool valid[9];
#pragma unroll
        for (int e = 0; e < 4; ++e) { v[e] = bfhi(h0[e]); v[4 + e] = bfhi(h1[e]); }
        v[8] = bfhi(h2);
#pragma unroll
        for (int e = 0; e < 9; ++e) valid[e] = true;
        const float pivot = __uint_as_float(__builtin_amdgcn_readfirstlane(h0.x) & 0xFFFF0000u);
        st = row_stats<9>(v, valid, pivot);
        float r = rbf(ln_apply(v[8], st)) * bflo((u32)wr1);
#pragma unroll
        for (int e = 0; e < 8; ++e) r = fmaf(rbf(ln_apply(v[e], st)), (e & 1) ? bfhi(wr0[e >> 1]) : bflo(wr0[e >> 1]), r);
        r = rbf(wave_sum(r));
        sig = rbf(__builtin_amdgcn_rcpf(1.0f + __builtin_amdgcn_exp2f(r * -1.4426950408889634f)));
      }
      float hn[4];
#pragma unroll
      for (int e = 0; e < 4; ++e) hn[e] = rbf(ln_apply(hv[e], st));
      STAMP(10);
      not_before(th, GATE_PD);
      const f32x4 y = gather16(WS_PD);
      STAMP(12);
      RSTAMP(1);
      float o[4];
#pragma unroll
      for (int e = 0; e < 4; ++e) o[e] = hv[e] + rbf(hn[e] + rbf(rbf(y[e]) * sig));
      if (lane < 16) *(GAS u32x2*)(p.out + jb * DM + 64 * cg + 4 * lane) = u32x2{pack2(o[0], o[1]), pack2(o[2], o[3])};
      PMARK(9);
      if (job == 0 && lane == 0) __hip_atomic_store((GAS u32*)(p.ws + WS_EPOCH), T, __ATOMIC_RELAXED, __HIP_MEMORY_SCOPE_AGENT);
      STAMP(13);
    }
  }
  STAMP(14);
  STAMP_FLUSH();
}

}

extern "C" void kernel_launch(void* const* d_in, const int* in_sizes, int n_in, void* d_out, int out_size, void* d_ws, size_t ws_size,
                              hipStream_t stream) {
  Params p;
  p.x = (const u16*)d_in[0];
  p.kc = (const u16*)d_in[1];
  p.vc = (const u16*)d_in[2];
  p.wq = (const u16*)d_in[3];
  p.wo = (const u16*)d_in[4];
  p.wr = (const u16*)d_in[6];
  p.wg = (const u16*)d_in[7];
  p.wu = (const u16*)d_in[8];
  p.wd = (const u16*)d_in[9];
  p.out = (u16*)d_out;
  p.ws = (unsigned char*)d_ws;
  (void)hipFuncSetAttribute((const void*)fused_kernel, hipFuncAttributeMaxDynamicSharedMemorySize, LDS_BYTES);
  hipLaunchKernelGGL(fused_kernel, dim3(NWG), dim3(NTHR), LDS_BYTES, stream, p);
}
```

```cpp
#include <hip/hip_runtime.h>
#include <stdint.h>

typedef unsigned u32;
typedef unsigned short u16;
typedef u32 u32x4 __attribute__((ext_vector_type(4)));
typedef u32 u32x2 __attribute__((ext_vector_type(2)));
typedef float f32x4 __attribute__((ext_vector_type(4)));
typedef float f32x2 __attribute__((ext_vector_type(2)));
typedef short s16x4 __attribute__((ext_vector_type(4)));
typedef __bf16 bf16x8 __attribute__((ext_vector_type(8)));
typedef __bf16 bf16x2 __attribute__((ext_vector_type(2)));
#define GAS __attribute__((address_space(1)))
#define LAS __attribute__((address_space(3)))

#define STAMPS 0
#define LWIN 20
#define GWIN 40
#define QDELAY 8
#define PSLEEP 1
#define QS_T4 1
#define OS_B 1

namespace {

constexpr int DM = 576, HKV = 16, SK = 4096, DH = 128, WIN = 1024, NQ = 4096, FF = 4608;
constexpr int NWG = 256, NTHR = 576;

constexpr int WS_EPOCH = 0;
constexpr int WS_QS = 4096;
constexpr int WS_SC = WS_QS + 16 * 8 * 4 * 256 * 4;
constexpr int WS_OS = WS_SC + 256 * 128;
constexpr int WS_PW = WS_OS + 16 * 8 * 2 * 256 * 4;
constexpr int WS_HS = WS_PW + 8 * 9 * 16 * 64 * 4;
constexpr int WS_AS = WS_HS + 8 * 576 * 4;
constexpr int WS_PD = WS_AS + 8 * 4608 * 4;
constexpr int WS_END = WS_PD + 8 * 9 * 16 * 64 * 4;
constexpr int WS_STAMP = (WS_END + 4095) / 4096 * 4096;
constexpr int AUX_SC1 = 16;
constexpr unsigned SPIN_CAP = 1u << 16, LSPIN_CAP = 1u << 22;

constexpr int L_EXT = 128 * 1024;
constexpr int L_FL = 144 * 1024;
constexpr int L_ST = L_FL + 128;
constexpr int L_P = L_ST + 256;
constexpr int L_QSUM = L_P + 2048;
constexpr int L_Q = L_QSUM + 8192;
constexpr int L_HN = L_P;
constexpr int HN_STRIDE = 292;
constexpr int LDS_BYTES = 160 * 1024;
static_assert(L_Q + 512 <= LDS_BYTES && L_HN + 8 * HN_STRIDE * 4 <= LDS_BYTES, "LDS");
enum { F_LANDED = 0, F_QPUB = 1, F_BAR = 2, F_QRDY = 3, F_SRDY = 4, F_KDONE = 8, F_VDONE = 16 };

struct Params {
  const u16 *x, *kc, *vc, *wq, *wo, *wr, *wg, *wu, *wd;
  u16* out;
  unsigned char* ws;
};

__device__ __forceinline__ float bflo(u32 d) { return __uint_as_float(d << 16); }
__device__ __forceinline__ float bfhi(u32 d) { return __uint_as_float(d & 0xFFFF0000u); }
__device__ __forceinline__ float rbf(float f) { return (float)(__bf16)f; }
__device__ __forceinline__ u32 bfbits_hi(float f) { return __float_as_uint(rbf(f)); }
__device__ __forceinline__ u32 pack2(float lo, float hi) {
  bf16x2 v = {(__bf16)lo, (__bf16)hi};
  return __builtin_bit_cast(u32, v);
}
template <int CTRL> __device__ __forceinline__ float dpp(float x) {
  return __uint_as_float((u32)__builtin_amdgcn_mov_dpp((int)__float_as_uint(x), CTRL, 0xf, 0xf, true));
}
constexpr int XOR1 = 0xB1, XOR2 = 0x4E, XOR7 = 0x141, XOR8 = 0x128;
__device__ __forceinline__ float swap32_add(float a, float b) {
  auto r = __builtin_amdgcn_permlane32_swap(__float_as_uint(a), __float_as_uint(b), false, false);
  return __uint_as_float(r[0]) + __uint_as_float(r[1]);
}
__device__ __forceinline__ float swap16_add(float a, float b) {
  auto r = __builtin_amdgcn_permlane16_swap(__float_as_uint(a), __float_as_uint(b), false, false);
  return __uint_as_float(r[0]) + __uint_as_float(r[1]);
}
__device__ __forceinline__ float wave_sum(float x) {
  x += dpp<XOR1>(x); x += dpp<XOR2>(x); x += dpp<XOR7>(x); x += dpp<XOR8>(x);
  x = swap16_add(x, x); x = swap32_add(x, x);
  return x;
}
__device__ __forceinline__ float fmax_(float a, float b) { return __builtin_amdgcn_fmed3f(a, b, INFINITY); }
__device__ __forceinline__ float wave_max(float x) {
  x = fmax_(x, dpp<XOR1>(x)); x = fmax_(x, dpp<XOR2>(x)); x = fmax_(x, dpp<XOR7>(x)); x = fmax_(x, dpp<XOR8>(x));
  auto r = __builtin_amdgcn_permlane16_swap(__float_as_uint(x), __float_as_uint(x), false, false);
  x = fmax_(__uint_as_float(r[0]), __uint_as_float(r[1]));
  auto t = __builtin_amdgcn_permlane32_swap(__float_as_uint(x), __float_as_uint(x), false, false);
  return fmax_(__uint_as_float(t[0]), __uint_as_float(t[1]));
}
__device__ __forceinline__ f32x4 mfma(u32x4 a, u32x4 b, f32x4 c) {
  return __builtin_amdgcn_mfma_f32_16x16x32_bf16(__builtin_bit_cast(bf16x8, a), __builtin_bit_cast(bf16x8, b), c, 0, 0, 0);
}
#define SB() __builtin_amdgcn_sched_barrier(0)
template <int N> __device__ __forceinline__ void waitvm() { asm volatile("s_waitcnt vmcnt(%0)" ::"n"(N) : "memory"); }
typedef __amdgpu_buffer_rsrc_t rsrc_t;
__device__ __forceinline__ u32x4 ld16c(rsrc_t rs, int off) { return __builtin_amdgcn_raw_buffer_load_b128(rs, off, 0, AUX_SC1); }
__device__ __forceinline__ u32 ld4c(rsrc_t rs, int off) { return __builtin_amdgcn_raw_buffer_load_b32(rs, off, 0, AUX_SC1); }
__device__ __forceinline__ void st16c(rsrc_t rs, int off, u32x4 v) { __builtin_amdgcn_raw_buffer_store_b128(v, rs, off, 0, AUX_SC1); }
template <u32 M> __device__ __forceinline__ bool tag_ok(u32x4 v, u32 t) { return ((((v.x ^ t) | (v.y ^ t)) | ((v.z ^ t) | (v.w ^ t))) & M) == 0u; }
template <u32 M> __device__ __forceinline__ u32 put_tag(float f, u32 t) { return ((__float_as_uint(f) + (M + 1) / 2) & ~M) | t; }
template <u32 M> __device__ __forceinline__ float get_tag(u32 d) { return __uint_as_float(d & ~M); }
#define POLL_COUNT(n)
#define POLL(cond_load) do { unsigned spins_ = 0; _Pragma("clang loop unroll(disable)") while (!(cond_load) && ++spins_ < SPIN_CAP) __builtin_amdgcn_s_sleep(PSLEEP); POLL_COUNT(spins_); } while (0)

__device__ __forceinline__ u32 lds_ld(u32 addr) {
  u32 v;
  asm volatile("ds_read_b32 %0, %1\n\ts_waitcnt lgkmcnt(0)" : "=v"(v) : "v"(addr) : "memory");
  return v;
}
__device__ __forceinline__ void lds_st(u32 addr, u32 v) { asm volatile("ds_write_b32 %0, %1" ::"v"(addr), "v"(v) : "memory"); }
__device__ __forceinline__ void wait_word(int word, u32 want) {
  unsigned spins = 0;
#pragma clang loop unroll(disable)
  while ((u32)__builtin_amdgcn_readfirstlane(lds_ld(L_FL + 4 * word)) < want)
    if (++spins > LSPIN_CAP) break;
}
__device__ __forceinline__ void wait_word_sleep(int word, u32 want) {
  unsigned spins = 0;
#pragma clang loop unroll(disable)
  while ((u32)__builtin_amdgcn_readfirstlane(lds_ld(L_FL + 4 * word)) < want) {
    __builtin_amdgcn_s_sleep(2);
    if (++spins > LSPIN_CAP) break;
  }
}
__device__ __forceinline__ void wait_all8(int word0, u32 want, int lane) {
  unsigned spins = 0;
#pragma clang loop unroll(disable)
  while (!__all(lds_ld(L_FL + 4 * (word0 + (lane & 7))) >= want)) {
    __builtin_amdgcn_s_sleep(1);
    if (++spins > LSPIN_CAP) break;
  }
}
__device__ __forceinline__ void cbar(u32 n, int lane) {
  asm volatile("s_waitcnt lgkmcnt(0)" ::: "memory");
  if (lane == 0) asm volatile("ds_add_u32 %0, %1" ::"v"((u32)(L_FL + 4 * F_BAR)), "v"(1u) : "memory");
  wait_word(F_BAR, 8u * n);
}
__device__ __forceinline__ void hw_barrier() { __builtin_amdgcn_sched_barrier(0); __builtin_amdgcn_s_barrier(); __builtin_amdgcn_sched_barrier(0); }
__device__ __forceinline__ void wg_barrier() {
  asm volatile("s_waitcnt lgkmcnt(0)" ::: "memory");
  hw_barrier();
  asm volatile("" ::: "memory");
}
__device__ __forceinline__ void glds16(const void* sbase, u32 voff, u32 lds_dst) {
  asm volatile("s_mov_b32 m0, %2\n\ts_nop 0\n\tglobal_load_lds_dwordx4 %0, %1 nt" ::"v"(voff), "s"(sbase), "s"(lds_dst) : "memory");
}
__device__ __forceinline__ int vswz(int row) { return ((row & 3) << 2) | ((row >> 2) & 3); }
__device__ __forceinline__ int swz(int row) { return ((row >> 1) & 1) | ((row >> 2) & 2); }
__device__ __forceinline__ u32 rowoff(int q, int stride, int lane) { const int row = 8 * q + (lane >> 3); return row * stride + (((lane & 7) ^ (swz(row) << 1)) & 7) * 16; }
template <int W> __device__ __forceinline__ void tile_dma(const unsigned char* a, int stride, u32 o0, u32 o1, u32 dst) {
  glds16(a, o0, dst); if (W >= 0) waitvm<W>();
  glds16(a, o1, dst + 1024); if (W >= 0) waitvm<W>();
  glds16(a + (size_t)16 * stride, o0, dst + 2048); if (W >= 0) waitvm<W>();
  glds16(a + (size_t)16 * stride, o1, dst + 3072); if (W >= 0) waitvm<W>();
}
__device__ __forceinline__ void tile_frags_lds(const unsigned char* lds, u32 base, int lane, u32x4 (&f)[4]) {
  const int g = lane >> 4, q = (lane & 15) >> 2, p = lane & 3;
#pragma unroll
  for (int e = 0; e < 4; ++e) {
    u32x2 h[2];
#pragma unroll
    for (int t = 0; t < 2; ++t) {
      const int row = 8 * g + 4 * t + q;
      const u32 a = base + row * 128 + (((2 * e + (p >> 1)) ^ (swz(row) << 1)) << 4) + 8 * (p & 1);
      h[t] = __builtin_bit_cast(u32x2, __builtin_amdgcn_ds_read_tr16_b64_v4i16((LAS s16x4*)(lds + a)));
    }
    f[e] = u32x4{h[0].x, h[0].y, h[1].x, h[1].y};
  }
}

#define ESTAMP(k) do { } while (0)
#define STAMP(k) do { } while (0)
#define STAMP_FLUSH() do { } while (0)

__device__ __forceinline__ u32x4 afrag(u32x4 a, u32x4 b) {
  u32x4 f;
  f.x = __builtin_amdgcn_perm(a.y, a.x, 0x07060302u);
  f.y = __builtin_amdgcn_perm(a.w, a.z, 0x07060302u);
  f.z = __builtin_amdgcn_perm(b.y, b.x, 0x07060302u);
  f.w = __builtin_amdgcn_perm(b.w, b.z, 0x07060302u);
  return f;
}

struct RowStats { float m, rs; };
template <int N> __device__ __forceinline__ RowStats row_stats(const float (&v)[N], const bool (&valid)[N], float pivot) {
  float s1[2] = {0.f, 0.f}, s2[2] = {0.f, 0.f};
#pragma unroll
  for (int e = 0; e < N; ++e) { const float d = valid[e] ? v[e] - pivot : 0.f; s1[e & 1] += d; s2[e & 1] = fmaf(d, d, s2[e & 1]); }
  const float m1 = wave_sum(s1[0] + s1[1]) * (1.0f / DM), m2 = wave_sum(s2[0] + s2[1]) * (1.0f / DM);
  const float var = rbf(fmax_(fmaf(-m1, m1, m2), 0.f));
  RowStats r;
  r.m = rbf(pivot + m1);
  r.rs = __builtin_amdgcn_rcpf(rbf(__builtin_amdgcn_sqrtf(rbf(var + 1e-5f))));
  return r;
}
__device__ __forceinline__ float ln_apply(float v, RowStats s) { return rbf(v - s.m) * s.rs; }

enum { ROLE_W = 0, ROLE_G = 1, ROLE_F = 2 };

__global__ void __launch_bounds__(NTHR, 3) fused_kernel(Params p) {
  extern __shared__ __attribute__((aligned(16))) unsigned char lds[];
  const int tid = threadIdx.x, lane = tid & 63, w = __builtin_amdgcn_readfirstlane(tid >> 6);
  const int wg = blockIdx.x, xcd = wg & 7, s = wg >> 3, j = xcd + 8 * (s >> 4), i = s & 15, b = i >> 1, half = i & 1;
  const int cidx = (s >> 4) * 7 + (i - 9);
  const int role = i < 9 ? ROLE_W : (cidx < 9 ? ROLE_G : ROLE_F);
  const size_t ubase = ((size_t)(b * HKV + j) * SK + (SK - WIN)) * DH;
  const int cq = i & 3, kp = i >> 2;
  const int ks0 = kp < 3 ? 4 * kp : 13, nt = kp < 2 ? 4 : 5;
  const u32 PK = 4 * nt, PV = PK + 128, PW = PK + 256;
  const int g4 = lane >> 4, c16 = lane & 15;
#define RSTAMP(k)
#define PMARK(n)
  STAMP(0);
  const int cw = (w + 7) & 7;
  u32 xr[5];
  u32x4 xa;
  {
    const GAS u32* xrow = (const GAS u32*)(p.x + cw * DM);
#pragma unroll
    for (int m = 0; m < 4; ++m) xr[m] = __builtin_nontemporal_load(xrow + lane + 64 * m);
    xr[4] = __builtin_nontemporal_load(xrow + (lane & 31) + 256);
    xa = __builtin_nontemporal_load((const GAS u32x4*)(p.x + (lane & 7) * DM + 32 * (ks0 + (cw < nt ? cw : 0)) + 8 * g4));
  }
  if (w == 0) {
    if (lane < 32) lds_st(L_FL + 4 * lane, 0u);
    asm volatile("s_waitcnt lgkmcnt(0)" ::: "memory");
  }
  hw_barrier();

  if (w == 0) {
    __builtin_amdgcn_s_setprio(3);
    u32 pub = 0;
#define PUBLISH(W) lds_st(L_FL + 4 * F_LANDED, pub - (W))
    {
      const unsigned char* a = (const unsigned char*)(p.wq + (size_t)32 * ks0 * NQ + 256 * j + 64 * cq);
      const u32 o0 = rowoff(0, NQ * 2, lane), o1 = rowoff(1, NQ * 2, lane);
#pragma unroll
      for (int t = 0; t < 4; ++t) tile_dma<-1>(a + (size_t)t * 32 * NQ * 2, NQ * 2, o0, o1, L_EXT + t * 4096);
      if (nt == 5) tile_dma<-1>(a + (size_t)4 * 32 * NQ * 2, NQ * 2, o0, o1, L_QSUM);
      pub = PK;
    }
    ESTAMP(8);
    {
      const unsigned char* kb = (const unsigned char*)(p.kc + ubase + (size_t)half * 512 * DH);
      u32 koff[4];
#pragma unroll
      for (int q = 0; q < 4; ++q) { const int key = 4 * q + g4; koff[q] = key * 256 + ((c16 ^ key) & 15) * 16; }
#pragma clang loop unroll(disable)
      for (int t = 0; t < 32; ++t) {
#pragma unroll
        for (int q = 0; q < 4; ++q) { glds16(kb + t * 4096, koff[q], (t * 4 + q) * 1024); waitvm<LWIN>(); }
        pub += 4;
        if (pub > LWIN) PUBLISH(LWIN);
      }
    }
    STAMP(1);
    {
      const unsigned char* vb = (const unsigned char*)(p.vc + ubase + (size_t)half * 512 * DH);
      u32 voff[4];
#pragma unroll
      for (int q = 0; q < 4; ++q) { const int key = 4 * q + g4; voff[q] = key * 256 + ((c16 ^ vswz(key)) & 15) * 16; }
      wait_word(F_QPUB, 1u);
#pragma clang loop unroll(disable)
      for (int t = 0; t < 32; ++t) {
        if ((t & 7) == 4) wait_all8(F_KDONE, (t >> 3) + 1, lane);
        const u32 dst = t < 4 ? L_EXT + t * 4096 : (t - 4) * 4096;
#pragma unroll
        for (int q = 0; q < 4; ++q) { glds16(vb + t * 4096, voff[q], dst + q * 1024); waitvm<LWIN>(); }
        pub += 4; PUBLISH(LWIN);
      }
    }
    STAMP(2);
    if (role == ROLE_W) {
      const unsigned char* a = (const unsigned char*)(p.wo + (size_t)256 * j * DM + 64 * i);
      const u32 o0 = rowoff(0, DM * 2, lane), o1 = rowoff(1, DM * 2, lane);
      wait_all8(F_VDONE, 1, lane);
#pragma clang loop unroll(disable)
      for (int t = 0; t < 8; ++t) { tile_dma<LWIN>(a + (size_t)t * 32 * DM * 2, DM * 2, o0, o1, t * 4096); pub += 4; PUBLISH(LWIN); }
    } else if (role == ROLE_G) {
      const int gx = xcd * 9 + cidx;
      const u32 o0 = rowoff(0, FF * 2, lane), o1 = rowoff(1, FF * 2, lane);
#pragma clang loop unroll(disable)
      for (int t = 0; t < 36; ++t) {
        if (t == 0 || t == 16) wait_all8(F_VDONE, (t >> 4) + 1, lane);
        const unsigned char* a = (const unsigned char*)(((t & 1) ? p.wu : p.wg) + (size_t)(t >> 1) * 32 * FF + 64 * gx);
        tile_dma<GWIN>(a, FF * 2, o0, o1, t < 4 ? L_EXT + t * 4096 : (t - 4) * 4096);
        pub += 4;
        if (4 * t + 4 > GWIN - LWIN) PUBLISH(GWIN);
      }
    }
    STAMP(3);
#define DR(N) if (GWIN > N && (role == ROLE_G || LWIN > N)) { waitvm<N>(); PUBLISH(N); }
    DR(44) DR(40) DR(36) DR(32) DR(28) DR(24) DR(20) DR(16) DR(12) DR(8) DR(4) DR(0)
#undef DR
#undef PUBLISH
    STAMP(14);
    STAMP_FLUSH();
    return;
  }

  const int ct = tid - 64;
  const rsrc_t rs = __builtin_amdgcn_make_buffer_rsrc((void*)p.ws, 0, WS_END, 0x00020000);
  const u32 T = *(const __attribute__((address_space(4))) u32*)(p.ws + WS_EPOCH) + 1u;
  const u32 T16 = T & 0xFFFFu, T8 = T & 0xFFu, T4 = T & 0xFu;
  float* const lf = (float*)lds;
  u32* const lu = (u32*)lds;
  {
    ESTAMP(8);
    asm volatile("" :: "v"(xr[0]), "v"(xr[4]));
    ESTAMP(9);
    float v[10]; bool valid[10];
#pragma unroll
    for (int m = 0; m < 5; ++m) { valid[2 * m] = valid[2 * m + 1] = (m < 4) || (lane < 32); v[2 * m] = bflo(xr[m]); v[2 * m + 1] = bfhi(xr[m]); }
    const float pivot = __uint_as_float(__builtin_amdgcn_readfirstlane(xr[0]) << 16);
    const RowStats st = row_stats<10>(v, valid, pivot);
    if (lane == 0) *(f32x2*)(lf + L_ST / 4 + 2 * cw) = f32x2{st.m, st.rs};
    ESTAMP(10);
    cbar(1, lane);
    STAMP(1);
    PMARK(1);
  }
  if (cw < nt) {
    const f32x2 sv = *(const f32x2*)(lf + L_ST / 4 + 2 * (lane & 7));
    const RowStats st = {sv.x, sv.y};
    u32x4 a;
#pragma unroll
    for (int q = 0; q < 4; ++q) a[q] = pack2(ln_apply(bflo(xa[q]), st), ln_apply(bfhi(xa[q]), st));
    const u32 base = cw < 4 ? L_EXT + cw * 4096 : L_QSUM;
    wait_word(F_LANDED, 4 * (cw + 1));
    u32x4 f[4]; tile_frags_lds(lds, base, lane, f);
    f32x4 d[4];
#pragma unroll
    for (int e = 0; e < 4; ++e) d[e] = mfma(f[e], a, f32x4{0.f, 0.f, 0.f, 0.f});
    if (c16 < 8) {
#pragma unroll
      for (int e = 0; e < 4; ++e) *(f32x4*)(lf + base / 4 + c16 * 64 + 16 * e + 4 * g4) = d[e];
    }
  }
  ESTAMP(11);
  cbar(2, lane);
  ESTAMP(12);
  {
    float sum = (lf[L_EXT / 4 + cw * 64 + lane] + lf[L_EXT / 4 + 1024 + cw * 64 + lane]) + (lf[L_EXT / 4 + 2048 + cw * 64 + lane] + lf[L_EXT / 4 + 3072 + cw * 64 + lane]);
    if (nt == 5) sum += lf[L_QSUM / 4 + cw * 64 + lane];
    const u32 v = put_tag<0xFu>(sum, T4);
    const u32x4 o = {v, (u32)__builtin_amdgcn_mov_dpp((int)v, 0x55, 0xf, 0xf, true), (u32)__builtin_amdgcn_mov_dpp((int)v, 0xAA, 0xf, 0xf, true),
                     (u32)__builtin_amdgcn_mov_dpp((int)v, 0xFF, 0xf, 0xf, true)};
    if ((lane & 3) == 0) st16c(rs, WS_QS + ((((j * 8 + cw) * 4 + kp) * 256) + 64 * cq + lane) * 4, o);
  }
  cbar(3, lane);
  if (ct == 0) lds_st(L_FL + 4 * F_QPUB, 1u);
  STAMP(2);
  PMARK(2);
  if (cw >= 2 && cw < 6) {
    u32x4 qg;
    __builtin_amdgcn_s_sleep(QDELAY);
    {
      unsigned spins = 0;
#pragma clang loop unroll(disable)
      for (;;) {
        qg = ld16c(rs, WS_QS + (((j * 8 + b) * 4 + (cw - 2)) * 256 + 4 * lane) * 4);
        if (tag_ok<0xFu>(qg, T4) || ++spins > SPIN_CAP) break;
        __builtin_amdgcn_s_sleep(4);
      }
    }
    *(f32x4*)(lf + L_QSUM / 4 + (cw - 2) * 256 + 4 * lane) = f32x4{get_tag<0xFu>(qg.x), get_tag<0xFu>(qg.y), get_tag<0xFu>(qg.z), get_tag<0xFu>(qg.w)};
  }
  cbar(4, lane);
  if (cw == 0) {
    f32x4 sv[4];
#pragma unroll
    for (int q = 0; q < 4; ++q) sv[q] = *(const f32x4*)(lf + L_QSUM / 4 + q * 256 + 4 * lane);
    const f32x4 sum = (sv[0] + sv[1]) + (sv[2] + sv[3]);
    *(u32x2*)(lu + L_Q / 4 + 2 * lane) = u32x2{pack2(sum.x, sum.y), pack2(sum.z, sum.w)};
    asm volatile("s_waitcnt lgkmcnt(0)" ::: "memory");
    if (lane == 0) lds_st(L_FL + 4 * F_QRDY, 1u);
  } else {
    wait_word(F_QRDY, 1u);
  }
  STAMP(3);
  PMARK(3);
  float s0, s1;
  {
    u32x4 qa[4];
#pragma unroll
    for (int ks = 0; ks < 4; ++ks) qa[ks] = *(const u32x4*)(lu + L_Q / 4 + (lane & 1) * 64 + 16 * ks + 4 * g4);
    f32x4 d[4];
#pragma unroll
    for (int m = 0; m < 4; ++m) {
      const int tile = cw + 8 * m;
      wait_word_sleep(F_LANDED, PK + 4 * (tile + 1));
      u32x4 kf[4];
#pragma unroll
      for (int ks = 0; ks < 4; ++ks) kf[ks] = *(const u32x4*)(lds + tile * 4096 + c16 * 256 + (((4 * ks + g4) ^ c16) << 4));
      f32x4 acc = mfma(qa[0], kf[0], f32x4{0.f, 0.f, 0.f, 0.f});
      acc = mfma(qa[1], kf[1], acc); acc = mfma(qa[2], kf[2], acc); acc = mfma(qa[3], kf[3], acc);
      d[m] = acc;
      asm volatile("s_waitcnt lgkmcnt(0)" ::: "memory");
      if (lane == 0) lds_st(L_FL + 4 * (F_KDONE + cw), m + 1);
    }
    const float x0 = g4 == 0 ? d[0][0] : g4 == 1 ? d[1][0] : g4 == 2 ? d[2][0] : d[3][0];
    const float x1 = g4 == 0 ? d[0][1] : g4 == 1 ? d[1][1] : g4 == 2 ? d[2][1] : d[3][1];
    constexpr float SCALE = 0.08838834764831845f;
    s0 = rbf(rbf(x0) * SCALE); s1 = rbf(rbf(x1) * SCALE);
  }
  STAMP(4);
  PMARK(4);
  {
    constexpr float L2E = 1.4426950408889634f;
    const float m0 = wave_max(s0), m1 = wave_max(s1);
    const float l0 = wave_sum(__builtin_amdgcn_exp2f((s0 - m0) * L2E)), l1 = wave_sum(__builtin_amdgcn_exp2f((s1 - m1) * L2E));
    if (lane == 0) *(f32x4*)(lf + L_ST / 4 + 16 + 4 * cw) = f32x4{m0, m1, l0, l1};
    cbar(5, lane);
    float M0 = -INFINITY, M1 = -INFINITY;
    f32x4 st[8];
#pragma unroll
    for (int ww = 0; ww < 8; ++ww) { st[ww] = *(const f32x4*)(lf + L_ST / 4 + 16 + 4 * ww); M0 = fmax_(M0, st[ww].x); M1 = fmax_(M1, st[ww].y); }
    float L0 = 0.f, L1 = 0.f;
#pragma unroll
    for (int ww = 0; ww < 8; ++ww) {
      L0 = fmaf(st[ww].z, __builtin_amdgcn_exp2f((st[ww].x - M0) * L2E), L0);
      L1 = fmaf(st[ww].w, __builtin_amdgcn_exp2f((st[ww].y - M1) * L2E), L1);
    }
    const u32x4 mine = {__float_as_uint(M0) | T16, __float_as_uint(M1) | T16, put_tag<0xFFu>(L0, T8), put_tag<0xFFu>(L1, T8)};
    u32x4 oth;
    if (cw == 1) {
      if (lane == 0) st16c(rs, WS_SC + wg * 128, mine);
      unsigned spins = 0;
#pragma clang loop unroll(disable)
      for (;;) {
        oth = ld16c(rs, WS_SC + (wg ^ 8) * 128);
        if (__all(((((oth.x ^ T16) | (oth.y ^ T16)) & 0xFFFFu) | (((oth.z ^ T8) | (oth.w ^ T8)) & 0xFFu)) == 0u) || ++spins > SPIN_CAP) break;
        __builtin_amdgcn_s_sleep(4);
      }
      if (lane == 0) { *(u32x4*)(lu + L_ST / 4 + 48) = oth; asm volatile("s_waitcnt lgkmcnt(0)" ::: "memory"); lds_st(L_FL + 4 * F_SRDY, 1u); }
    } else {
      wait_word_sleep(F_SRDY, 1u);
      oth = *(const u32x4*)(lu + L_ST / 4 + 48);
    }
    STAMP(5);
    const float om0 = bfhi(oth.x), om1 = bfhi(oth.y);
    const float G0 = fmax_(M0, om0), G1 = fmax_(M1, om1);
    const float S0 = get_tag<0xFFu>(mine.z) * __builtin_amdgcn_exp2f((M0 - G0) * L2E) + get_tag<0xFFu>(oth.z) * __builtin_amdgcn_exp2f((om0 - G0) * L2E);
    const float S1 = get_tag<0xFFu>(mine.w) * __builtin_amdgcn_exp2f((M1 - G1) * L2E) + get_tag<0xFFu>(oth.w) * __builtin_amdgcn_exp2f((om1 - G1) * L2E);
    const int kl = 16 * (cw + 8 * g4) + c16;
    __bf16* P = (__bf16*)(lds + L_P);
    P[kl] = (__bf16)(__builtin_amdgcn_exp2f((s0 - G0) * L2E) * (1.0f / S0));
    P[512 + kl] = (__bf16)(__builtin_amdgcn_exp2f((s1 - G1) * L2E) * (1.0f / S1));
  }
  cbar(6, lane);
  STAMP(6);
  {
    f32x4 d[8];
#pragma unroll
    for (int e = 0; e < 8; ++e) d[e] = f32x4{0.f, 0.f, 0.f, 0.f};
    const int q4 = (lane & 15) >> 2, p4 = lane & 3;
#pragma unroll
    for (int m = 0; m < 2; ++m) {
      const int ks = cw + 8 * m;
      const u32x4 pf = *(const u32x4*)(lds + L_P + (lane & 1) * 1024 + (32 * ks + 8 * g4) * 2);
      const u32 base = ks < 2 ? L_EXT + ks * 8192 : (ks - 2) * 8192;
      wait_word(F_LANDED, PV + 8 * (ks + 1));
      u32x4 f[8];
#pragma unroll
      for (int e = 0; e < 8; ++e) {
        u32x2 h[2];
#pragma unroll
        for (int t = 0; t < 2; ++t) {
          const int row = 8 * g4 + 4 * t + q4;
          const u32 a = base + row * 256 + (((2 * e + (p4 >> 1)) ^ vswz(row)) << 4) + 8 * (p4 & 1);
          h[t] = __builtin_bit_cast(u32x2, __builtin_amdgcn_ds_read_tr16_b64_v4i16((LAS s16x4*)(lds + a)));
        }
        f[e] = u32x4{h[0].x, h[0].y, h[1].x, h[1].y};
      }
      SB();
#pragma unroll
      for (int e = 0; e < 8; ++e) d[e] = mfma(f[e], pf, d[e]);
      asm volatile("s_waitcnt lgkmcnt(0)" ::: "memory");
      if (lane == 0) lds_st(L_FL + 4 * (F_VDONE + cw), m + 1);
    }
    if (c16 < 2) {
#pragma unroll
      for (int e = 0; e < 8; ++e) *(f32x4*)(lf + L_QSUM / 4 + cw * 256 + c16 * 128 + 16 * e + 4 * g4) = d[e];
    }
  }
  cbar(7, lane);
  auto gather4 = [&](u32 v) -> u32x4 {
    return u32x4{v, (u32)__builtin_amdgcn_mov_dpp((int)v, 0x55, 0xf, 0xf, true), (u32)__builtin_amdgcn_mov_dpp((int)v, 0xAA, 0xf, 0xf, true),
                 (u32)__builtin_amdgcn_mov_dpp((int)v, 0xFF, 0xf, 0xf, true)};
  };
  if (cw < 4) {
    float sv[8];
#pragma unroll
    for (int ww = 0; ww < 8; ++ww) sv[ww] = lf[L_QSUM / 4 + ww * 256 + ct];
    const float sum = ((sv[0] + sv[1]) + (sv[2] + sv[3])) + ((sv[4] + sv[5]) + (sv[6] + sv[7]));
    const u32x4 o = gather4(put_tag<0xFFu>(sum, T8));
    if ((lane & 3) == 0) st16c(rs, WS_OS + (((j * 8 + b) * 2 + half) * 256 + ct) * 4, o);
  }
  STAMP(7);
  PMARK(7);

  auto acc_to_lds = [&](int base, const f32x4 (&d)[4]) {
    if (c16 < 8) {
#pragma unroll
      for (int e = 0; e < 4; ++e) *(f32x4*)(lf + base / 4 + (cw * 8 + c16) * 64 + 16 * e + 4 * g4) = d[e];
    }
  };
  auto publish_partial = [&](int base, int cg) {
    float sv[8];
#pragma unroll
    for (int ww = 0; ww < 8; ++ww) sv[ww] = lf[L_EXT / 4 + ww * 512 + cw * 64 + lane];
    const float sum = ((sv[0] + sv[1]) + (sv[2] + sv[3])) + ((sv[4] + sv[5]) + (sv[6] + sv[7]));
    const u32x4 o = gather4(put_tag<0xFFu>(sum, T8));
    if ((lane & 3) == 0) st16c(rs, base + (((cw * 9 + cg) * 16 + j) * 64 + lane) * 4, o);
  };

  if (role == ROLE_W) {
    const int cg = i;
    u32x4 f[4];
    wait_word(F_LANDED, PW + 32);
    tile_frags_lds(lds, cw * 4096, lane, f);
    {
      const int off = WS_OS + ((j * 8 + (lane & 7)) * 512 + 32 * cw + 8 * g4) * 4;
      u32x4 a0, a1, b0, b1;
      POLL((a0 = ld16c(rs, off), a1 = ld16c(rs, off + 16), b0 = ld16c(rs, off + 1024), b1 = ld16c(rs, off + 1040),
            tag_ok<0xFFu>(a0, T8) & tag_ok<0xFFu>(a1, T8) & tag_ok<0xFFu>(b0, T8) & tag_ok<0xFFu>(b1, T8)));
      STAMP(8);
      RSTAMP(0);
#define S2(x, y) (get_tag<0xFFu>(x) + get_tag<0xFFu>(y))
      const u32x4 a = {pack2(S2(a0.x, b0.x), S2(a0.y, b0.y)), pack2(S2(a0.z, b0.z), S2(a0.w, b0.w)), pack2(S2(a1.x, b1.x), S2(a1.y, b1.y)), pack2(S2(a1.z, b1.z), S2(a1.w, b1.w))};
#undef S2
      f32x4 d[4];
#pragma unroll
      for (int e = 0; e < 4; ++e) d[e] = mfma(f[e], a, f32x4{0.f, 0.f, 0.f, 0.f});
      acc_to_lds(L_EXT, d);
    }
    wg_barrier();
    publish_partial(WS_PW, cg);
    STAMP(9);
    u32x4 f2[4];
    {
      const unsigned char* a = (const unsigned char*)(p.wd + (size_t)(288 * j + 32 * cw) * DM + 64 * cg);
      const u32 o0 = rowoff(0, DM * 2, lane), o1 = rowoff(1, DM * 2, lane);
      tile_dma<-1>(a, DM * 2, o0, o1, (8 + cw) * 4096);
      if (cw == 0) tile_dma<-1>(a + 256 * DM * 2, DM * 2, o0, o1, 16 * 4096);
      waitvm<0>();
      tile_frags_lds(lds, (8 + cw) * 4096, lane, f);
      if (cw == 0) tile_frags_lds(lds, 16 * 4096, lane, f2);
    }
    {
      const int off = WS_AS + ((lane & 7) * FF + 288 * j + 32 * cw + 8 * g4) * 4;
      u32x4 a0, a1, a2, a3;
      if (cw == 0) POLL((a0 = ld16c(rs, off), a1 = ld16c(rs, off + 16), a2 = ld16c(rs, off + 1024), a3 = ld16c(rs, off + 1040),
                         tag_ok<0xFFFFu>(a0, T16) & tag_ok<0xFFFFu>(a1, T16) & tag_ok<0xFFFFu>(a2, T16) & tag_ok<0xFFFFu>(a3, T16)));
      else POLL((a0 = ld16c(rs, off), a1 = ld16c(rs, off + 16), tag_ok<0xFFFFu>(a0, T16) & tag_ok<0xFFFFu>(a1, T16)));
      STAMP(12);
      RSTAMP(1);
      const u32x4 a = afrag(a0, a1);
      f32x4 d[4];
#pragma unroll
      for (int e = 0; e < 4; ++e) d[e] = mfma(f[e], a, f32x4{0.f, 0.f, 0.f, 0.f});
      if (cw == 0) {
        const u32x4 ab = afrag(a2, a3);
#pragma unroll
        for (int e = 0; e < 4; ++e) d[e] = mfma(f2[e], ab, d[e]);
      }
      acc_to_lds(L_EXT, d);
    }
    wg_barrier();
    publish_partial(WS_PD, cg);
    STAMP(13);
  } else if (role == ROLE_G) {
    const int gx = xcd * 9 + cidx;
    u32x4 f[5][4];
#pragma unroll
    for (int tl = 0; tl < 5; ++tl) {
      if (tl < 4 || cw < 4) {
        const int tau = cw + 8 * tl;
        wait_word(F_LANDED, PW + 4 * (tau + 1));
        tile_frags_lds(lds, tau < 4 ? L_EXT + tau * 4096 : (tau - 4) * 4096, lane, f[tl]);
      }
    }
    wait_word(F_LANDED, PW + 144);
    STAMP(8);
    {
      const int off = WS_HS + cw * DM * 4;
      u32x4 h0, h1; u32 h2;
      POLL((h0 = ld16c(rs, off + lane * 32), h1 = ld16c(rs, off + lane * 32 + 16), h2 = ld4c(rs, off + 2048 + lane * 4),
            tag_ok<0xFFFFu>(h0, T16) & tag_ok<0xFFFFu>(h1, T16) & ((h2 & 0xFFFFu) == T16)));
      STAMP(10);
      RSTAMP(0);
      float v[9]; bool valid[9];
#pragma unroll
      for (int e = 0; e < 4; ++e) { v[e] = bfhi(h0[e]); v[4 + e] = bfhi(h1[e]); }
      v[8] = bfhi(h2);
#pragma unroll
      for (int e = 0; e < 9; ++e) valid[e] = true;
      const float pivot = __uint_as_float(__builtin_amdgcn_readfirstlane(h0.x) & 0xFFFF0000u);
      const RowStats st = row_stats<9>(v, valid, pivot);
      u32* hn = lu + L_HN / 4 + cw * HN_STRIDE;
      *(u32x4*)(hn + 4 * lane) = u32x4{pack2(ln_apply(v[0], st), ln_apply(v[1], st)), pack2(ln_apply(v[2], st), ln_apply(v[3], st)),
                                       pack2(ln_apply(v[4], st), ln_apply(v[5], st)), pack2(ln_apply(v[6], st), ln_apply(v[7], st))};
      ((__bf16*)(hn + 256))[lane] = (__bf16)ln_apply(v[8], st);
    }
    wg_barrier();
    {
      f32x4 d[4];
      const u32* hn = lu + L_HN / 4 + (lane & 7) * HN_STRIDE + 16 * (cw >> 1) + 4 * g4;
      u32x4 a[5];
#pragma unroll
      for (int tl = 0; tl < 5; ++tl) a[tl] = *(const u32x4*)(hn + (tl < 4 || cw < 4 ? 64 * tl : 0));
      SB();
#pragma unroll
      for (int e = 0; e < 4; ++e) d[e] = mfma(f[0][e], a[0], f32x4{0.f, 0.f, 0.f, 0.f});
#pragma unroll
      for (int tl = 1; tl < 5; ++tl) {
        if (tl < 4 || cw < 4) {
#pragma unroll
          for (int e = 0; e < 4; ++e) d[e] = mfma(f[tl][e], a[tl], d[e]);
        }
      }
      acc_to_lds(0, d);
    }
    wg_barrier();
    {
      float gv[4], uv[4];
#pragma unroll
      for (int ww = 0; ww < 4; ++ww) { gv[ww] = lf[(2 * ww) * 512 + cw * 64 + lane]; uv[ww] = lf[(2 * ww + 1) * 512 + cw * 64 + lane]; }
      const float gg = rbf((gv[0] + gv[1]) + (gv[2] + gv[3])), uu = rbf((uv[0] + uv[1]) + (uv[2] + uv[3]));
      const float sl = rbf(gg * __builtin_amdgcn_rcpf(1.0f + __builtin_amdgcn_exp2f(gg * -1.4426950408889634f)));
      const u32x4 o = gather4(bfbits_hi(sl * uu) | T16);
      if ((lane & 3) == 0) st16c(rs, WS_AS + (cw * FF + 64 * gx + lane) * 4, o);
    }
    STAMP(11);
  } else {
    const int f = xcd * 5 + (cidx - 9);
    const int job = 2 * f + (cw >> 2);
    if ((cw & 3) == 0 && job < 72) {
      const int jb = job / 9, cg = job % 9;
      const u32x2 xq = *(const GAS u32x2*)(p.x + jb * DM + 64 * cg + 4 * c16);
      const u32x4 wr0 = *(const GAS u32x4*)(p.wr + 8 * lane);
      const u16 wr1 = ((const GAS u16*)p.wr)[512 + lane];
      auto gather16 = [&](int base) -> f32x4 {
        const int off = base + (((jb * 9 + cg) * 16 + g4) * 64 + 4 * c16) * 4;
        u32x4 q0, q1, q2, q3;
        POLL((q0 = ld16c(rs, off), q1 = ld16c(rs, off + 1024), q2 = ld16c(rs, off + 2048), q3 = ld16c(rs, off + 3072),
              tag_ok<0xFFu>(q0, T8) & tag_ok<0xFFu>(q1, T8) & tag_ok<0xFFu>(q2, T8) & tag_ok<0xFFu>(q3, T8)));
        f32x4 sum;
#pragma unroll
        for (int e = 0; e < 4; ++e) {
          float t = (get_tag<0xFFu>(q0[e]) + get_tag<0xFFu>(q1[e])) + (get_tag<0xFFu>(q2[e]) + get_tag<0xFFu>(q3[e]));
          t = swap16_add(t, t); t = swap32_add(t, t);
          sum[e] = t;
        }
        return sum;
      };
      STAMP(3);
      const f32x4 at = gather16(WS_PW);
      STAMP(8);
      PMARK(8);
      RSTAMP(0);
      f32x4 hv;
      hv[0] = rbf(bflo(xq.x) + rbf(at[0])); hv[1] = rbf(bfhi(xq.x) + rbf(at[1]));
      hv[2] = rbf(bflo(xq.y) + rbf(at[2])); hv[3] = rbf(bfhi(xq.y) + rbf(at[3]));
      if (lane < 16)
        st16c(rs, WS_HS + (jb * DM + 64 * cg + 4 * lane) * 4,
              u32x4{__float_as_uint(hv[0]) | T16, __float_as_uint(hv[1]) | T16, __float_as_uint(hv[2]) | T16, __float_as_uint(hv[3]) | T16});
      STAMP(9);
      RowStats st; float sig;
      {
        const int off = WS_HS + jb * DM * 4;
        u32x4 h0, h1; u32 h2;
        POLL((h0 = ld16c(rs, off + lane * 32), h1 = ld16c(rs, off + lane * 32 + 16), h2 = ld4c(rs, off + 2048 + lane * 4),
              tag_ok<0xFFFFu>(h0, T16) & tag_ok<0xFFFFu>(h1, T16) & ((h2 & 0xFFFFu) == T16)));
        float v[9]; bool valid[9];
#pragma unroll
        for (int e = 0; e < 4; ++e) { v[e] = bfhi(h0[e]); v[4 + e] = bfhi(h1[e]); }
        v[8] = bfhi(h2);
#pragma unroll
        for (int e = 0; e < 9; ++e) valid[e] = true;
        const float pivot = __uint_as_float(__builtin_amdgcn_readfirstlane(h0.x) & 0xFFFF0000u);
        st = row_stats<9>(v, valid, pivot);
        float r = rbf(ln_apply(v[8], st)) * bflo((u32)wr1);
#pragma unroll
        for (int e = 0; e < 8; ++e) r = fmaf(rbf(ln_apply(v[e], st)), (e & 1) ? bfhi(wr0[e >> 1]) : bflo(wr0[e >> 1]), r);
        r = rbf(wave_sum(r));
        sig = rbf(__builtin_amdgcn_rcpf(1.0f + __builtin_amdgcn_exp2f(r * -1.4426950408889634f)));
      }
      float hn[4];
#pragma unroll
      for (int e = 0; e < 4; ++e) hn[e] = rbf(ln_apply(hv[e], st));
      STAMP(10);
      const f32x4 y = gather16(WS_PD);
      STAMP(12);
      RSTAMP(1);
      float o[4];
#pragma unroll
      for (int e = 0; e < 4; ++e) o[e] = hv[e] + rbf(hn[e] + rbf(rbf(y[e]) * sig));
      if (lane < 16) *(GAS u32x2*)(p.out + jb * DM + 64 * cg + 4 * lane) = u32x2{pack2(o[0], o[1]), pack2(o[2], o[3])};
      PMARK(9);
      if (job == 0 && lane == 0) __hip_atomic_store((GAS u32*)(p.ws + WS_EPOCH), T, __ATOMIC_RELAXED, __HIP_MEMORY_SCOPE_AGENT);
      STAMP(13);
    }
  }
  STAMP(14);
  STAMP_FLUSH();
}

}

extern "C" void kernel_launch(void* const* d_in, const int* in_sizes, int n_in, void* d_out, int out_size, void* d_ws, size_t ws_size,
                              hipStream_t stream) {
  Params p;
  p.x = (const u16*)d_in[0];
  p.kc = (const u16*)d_in[1];
  p.vc = (const u16*)d_in[2];
  p.wq = (const u16*)d_in[3];
  p.wo = (const u16*)d_in[4];
  p.wr = (const u16*)d_in[6];
  p.wg = (const u16*)d_in[7];
  p.wu = (const u16*)d_in[8];
  p.wd = (const u16*)d_in[9];
  p.out = (u16*)d_out;
  p.ws = (unsigned char*)d_ws;
  (void)hipFuncSetAttribute((const void*)fused_kernel, hipFuncAttributeMaxDynamicSharedMemorySize, LDS_BYTES);
  hipLaunchKernelGGL(fused_kernel, dim3(NWG), dim3(NTHR), LDS_BYTES, stream, p);
}
```

```cpp
#include <hip/hip_runtime.h>
#include <stdint.h>

typedef unsigned u32;
typedef unsigned short u16;
typedef u32 u32x4 __attribute__((ext_vector_type(4)));
typedef u32 u32x2 __attribute__((ext_vector_type(2)));
typedef float f32x4 __attribute__((ext_vector_type(4)));
typedef float f32x2 __attribute__((ext_vector_type(2)));
typedef short s16x4 __attribute__((ext_vector_type(4)));
typedef __bf16 bf16x8 __attribute__((ext_vector_type(8)));
typedef __bf16 bf16x2 __attribute__((ext_vector_type(2)));
#define GAS __attribute__((address_space(1)))
#define LAS __attribute__((address_space(3)))

#define STAMPS 0
#define LWIN 20
#define GWIN 40
#define QDELAY 8
#define GATE_PW 90
#define GATE_H 150
#define GATE_ACT 220
#define GATE_PD 270
#define PSTAG 6
#define PSLEEP 1
#define QS_T4 1
#define PROBE 0
#define DYNROLE 1
#define OS_B 1

namespace {

constexpr int DM = 576, HKV = 16, SK = 4096, DH = 128, WIN = 1024, NQ = 4096, FF = 4608;
constexpr int NWG = 256, NTHR = 576;

constexpr int WS_EPOCH = 0;
constexpr int WS_RANK = 2048;
constexpr int WS_QS = 4096;
constexpr int WS_SC = WS_QS + 16 * 8 * 4 * 256 * 4;
constexpr int WS_OS = WS_SC + 256 * 128;
constexpr int WS_PW = WS_OS + 16 * 8 * 2 * 256 * 4;
constexpr int HREP = 8;
constexpr int WS_HS = WS_PW + 8 * 9 * 16 * 64 * 4;
constexpr int WS_AS = WS_HS + HREP * 8 * 576 * 4;
constexpr int WS_PD = WS_AS + 8 * 4608 * 4;
constexpr int WS_END = WS_PD + 8 * 9 * 16 * 64 * 4;
constexpr int WS_STAMP = (WS_END + 4095) / 4096 * 4096;
constexpr int AUX_SC1 = 16;
constexpr unsigned SPIN_CAP = 1u << 16, LSPIN_CAP = 1u << 22;

constexpr int L_EXT = 128 * 1024;
constexpr int L_FL = 144 * 1024;
constexpr int L_ST = L_FL + 128;
constexpr int L_P = L_ST + 256;
constexpr int L_QSUM = L_P + 2048;
constexpr int L_Q = L_QSUM + 8192;
constexpr int L_HN = L_P;
constexpr int HN_STRIDE = 292;
constexpr int LDS_BYTES = 160 * 1024;
static_assert(L_Q + 512 <= LDS_BYTES && L_HN + 8 * HN_STRIDE * 4 <= LDS_BYTES, "LDS");
enum { F_LANDED = 0, F_QPUB = 1, F_BAR = 2, F_QRDY = 3, F_RANK = 4, F_KDONE = 8, F_VDONE = 16 };

struct Params {
  const u16 *x, *kc, *vc, *wq, *wo, *wr, *wg, *wu, *wd;
  u16* out;
  unsigned char* ws;
};

__device__ __forceinline__ float bflo(u32 d) { return __uint_as_float(d << 16); }
__device__ __forceinline__ float bfhi(u32 d) { return __uint_as_float(d & 0xFFFF0000u); }
__device__ __forceinline__ float rbf(float f) { return (float)(__bf16)f; }
__device__ __forceinline__ u32 bfbits_hi(float f) { return __float_as_uint(rbf(f)); }
__device__ __forceinline__ u32 pack2(float lo, float hi) {
  bf16x2 v = {(__bf16)lo, (__bf16)hi};
  return __builtin_bit_cast(u32, v);
}
template <int CTRL> __device__ __forceinline__ float dpp(float x) {
  return __uint_as_float((u32)__builtin_amdgcn_mov_dpp((int)__float_as_uint(x), CTRL, 0xf, 0xf, true));
}
constexpr int XOR1 = 0xB1, XOR2 = 0x4E, XOR7 = 0x141, XOR8 = 0x128;
__device__ __forceinline__ float swap32_add(float a, float b) {
  auto r = __builtin_amdgcn_permlane32_swap(__float_as_uint(a), __float_as_uint(b), false, false);
  return __uint_as_float(r[0]) + __uint_as_float(r[1]);
}
__device__ __forceinline__ float swap16_add(float a, float b) {
  auto r = __builtin_amdgcn_permlane16_swap(__float_as_uint(a), __float_as_uint(b), false, false);
  return __uint_as_float(r[0]) + __uint_as_float(r[1]);
}
__device__ __forceinline__ float wave_sum(float x) {
  x += dpp<XOR1>(x); x += dpp<XOR2>(x); x += dpp<XOR7>(x); x += dpp<XOR8>(x);
  x = swap16_add(x, x); x = swap32_add(x, x);
  return x;
}
__device__ __forceinline__ float fmax_(float a, float b) { return __builtin_amdgcn_fmed3f(a, b, INFINITY); }
__device__ __forceinline__ float wave_max(float x) {
  x = fmax_(x, dpp<XOR1>(x)); x = fmax_(x, dpp<XOR2>(x)); x = fmax_(x, dpp<XOR7>(x)); x = fmax_(x, dpp<XOR8>(x));
  auto r = __builtin_amdgcn_permlane16_swap(__float_as_uint(x), __float_as_uint(x), false, false);
  x = fmax_(__uint_as_float(r[0]), __uint_as_float(r[1]));
  auto t = __builtin_amdgcn_permlane32_swap(__float_as_uint(x), __float_as_uint(x), false, false);
  return fmax_(__uint_as_float(t[0]), __uint_as_float(t[1]));
}
__device__ __forceinline__ f32x4 mfma(u32x4 a, u32x4 b, f32x4 c) {
  return __builtin_amdgcn_mfma_f32_16x16x32_bf16(__builtin_bit_cast(bf16x8, a), __builtin_bit_cast(bf16x8, b), c, 0, 0, 0);
}
#define SB() __builtin_amdgcn_sched_barrier(0)
template <int N> __device__ __forceinline__ void waitvm() { asm volatile("s_waitcnt vmcnt(%0)" ::"n"(N) : "memory"); }
typedef __amdgpu_buffer_rsrc_t rsrc_t;
__device__ __forceinline__ u32x4 ld16c(rsrc_t rs, int off) { return __builtin_amdgcn_raw_buffer_load_b128(rs, off, 0, AUX_SC1); }
__device__ __forceinline__ u32 ld4c(rsrc_t rs, int off) { return __builtin_amdgcn_raw_buffer_load_b32(rs, off, 0, AUX_SC1); }
__device__ __forceinline__ void st16c(rsrc_t rs, int off, u32x4 v) { __builtin_amdgcn_raw_buffer_store_b128(v, rs, off, 0, AUX_SC1); }
template <u32 M> __device__ __forceinline__ bool tag_ok(u32x4 v, u32 t) { return ((((v.x ^ t) | (v.y ^ t)) | ((v.z ^ t) | (v.w ^ t))) & M) == 0u; }
template <u32 M> __device__ __forceinline__ u32 put_tag(float f, u32 t) { return ((__float_as_uint(f) + (M + 1) / 2) & ~M) | t; }
template <u32 M> __device__ __forceinline__ float get_tag(u32 d) { return __uint_as_float(d & ~M); }
#define POLL_COUNT(n)
#define POLL(cond_load) do { unsigned spins_ = 0; _Pragma("clang loop unroll(disable)") while (!(cond_load) && ++spins_ < SPIN_CAP) __builtin_amdgcn_s_sleep(PSLEEP); POLL_COUNT(spins_); } while (0)

#define PV_CLOB "v128","v129","v130","v131","v132","v133","v134","v135","v136","v137","v138","v139","v140","v141","v142","v143", \
                "v144","v145","v146","v147","v148","v149","v150","v151","v152","v153","v154","v155","v156","v157","v158","v159","v160","v161","v162","v163", "vcc", "scc", "memory"
#define PV_LD4(r0, r1, r2, r3, r4, r5, r6, r7) \
  "buffer_load_dwordx4 v[" r0 ":" r1 "], %[o0], %[rs], 0 offen sc1\n\t" "buffer_load_dwordx4 v[" r2 ":" r3 "], %[o1], %[rs], 0 offen sc1\n\t" \
  "buffer_load_dwordx4 v[" r4 ":" r5 "], %[o2], %[rs], 0 offen sc1\n\t" "buffer_load_dwordx4 v[" r6 ":" r7 "], %[o3], %[rs], 0 offen sc1\n\t"
#define PV_X4(t, a, b, c, d) "v_xor_b32 " t ", %[T], v" a "\n\t" "v_bitop3_b32 " t ", " t ", v" b ", %[T] bitop3:0xf6\n\t" "v_bitop3_b32 " t ", " t ", v" c ", %[T] bitop3:0xf6\n\t" "v_bitop3_b32 " t ", " t ", v" d ", %[T] bitop3:0xf6\n\t"
#define PV_TEST "v_and_b32 v160, %[M], v160\n\tv_cmp_ne_u32 vcc, 0, v160\n\ts_cmp_eq_u64 vcc, 0\n\t"
#define PV_MOV2(o, a, b) "v_mov_b64 " o ", v[" a ":" b "]\n\t"
__device__ __forceinline__ void poll4(rsrc_t rs, int o0, int o1, int o2, int o3, u32 T, u32 M, u32x4 (&R)[4]) {
  u32x2 r0, r1, r2, r3, r4, r5, r6, r7; u32 cnt = SPIN_CAP;
  asm volatile(
      PV_LD4("128", "131", "132", "135", "136", "139", "140", "143")
      "s_sleep %[stag]\n"
      "1:\n\t"
      PV_LD4("144", "147", "148", "151", "152", "155", "156", "159")
      "s_waitcnt vmcnt(4)\n\t"
      PV_X4("v160", "128", "129", "130", "131") PV_X4("v161", "132", "133", "134", "135") PV_X4("v162", "136", "137", "138", "139") PV_X4("v163", "140", "141", "142", "143")
      "v_or3_b32 v160, v160, v161, v162\n\tv_or_b32 v160, v160, v163\n\t" PV_TEST
      "s_cbranch_scc1 7f\n\t"
      PV_LD4("128", "131", "132", "135", "136", "139", "140", "143")
      "s_waitcnt vmcnt(4)\n\t"
      PV_X4("v160", "144", "145", "146", "147") PV_X4("v161", "148", "149", "150", "151") PV_X4("v162", "152", "153", "154", "155") PV_X4("v163", "156", "157", "158", "159")
      "v_or3_b32 v160, v160, v161, v162\n\tv_or_b32 v160, v160, v163\n\t" PV_TEST
      "s_cbranch_scc1 8f\n\t"
      "s_sub_u32 %[cnt], %[cnt], 1\n\ts_cbranch_scc0 1b\n\t"
      "s_waitcnt vmcnt(0)\n"
      "7:\n\t"
      PV_MOV2("%[r0]", "128", "129") PV_MOV2("%[r1]", "130", "131") PV_MOV2("%[r2]", "132", "133") PV_MOV2("%[r3]", "134", "135")
      PV_MOV2("%[r4]", "136", "137") PV_MOV2("%[r5]", "138", "139") PV_MOV2("%[r6]", "140", "141") PV_MOV2("%[r7]", "142", "143")
      "s_branch 9f\n"
      "8:\n\t"
      PV_MOV2("%[r0]", "144", "145") PV_MOV2("%[r1]", "146", "147") PV_MOV2("%[r2]", "148", "149") PV_MOV2("%[r3]", "150", "151")
      PV_MOV2("%[r4]", "152", "153") PV_MOV2("%[r5]", "154", "155") PV_MOV2("%[r6]", "156", "157") PV_MOV2("%[r7]", "158", "159")
      "9:\n\t"
      : [r0] "=&v"(r0), [r1] "=&v"(r1), [r2] "=&v"(r2), [r3] "=&v"(r3), [r4] "=&v"(r4), [r5] "=&v"(r5), [r6] "=&v"(r6), [r7] "=&v"(r7), [cnt] "+s"(cnt)
      : [o0] "v"(o0), [o1] "v"(o1), [o2] "v"(o2), [o3] "v"(o3), [rs] "s"(rs), [T] "s"(T), [M] "s"(M), [stag] "n"(PSTAG)
      : PV_CLOB);
  R[0] = u32x4{r0.x, r0.y, r1.x, r1.y}; R[1] = u32x4{r2.x, r2.y, r3.x, r3.y}; R[2] = u32x4{r4.x, r4.y, r5.x, r5.y}; R[3] = u32x4{r6.x, r6.y, r7.x, r7.y};
}
#define PV_LD2(r0, r1, r2, r3) \
  "buffer_load_dwordx4 v[" r0 ":" r1 "], %[o0], %[rs], 0 offen sc1\n\t" "buffer_load_dwordx4 v[" r2 ":" r3 "], %[o0], %[rs], 0 offen offset:16 sc1\n\t"
__device__ __forceinline__ void poll2(rsrc_t rs, int o0, u32 T, u32 M, u32x4& a, u32x4& b) {
  u32x2 r0, r1, r2, r3; u32 cnt = SPIN_CAP;
  asm volatile(
      PV_LD2("128", "131", "132", "135")
      "s_sleep %[stag]\n"
      "1:\n\t"
      PV_LD2("144", "147", "148", "151")
      "s_waitcnt vmcnt(2)\n\t"
      PV_X4("v160", "128", "129", "130", "131") PV_X4("v161", "132", "133", "134", "135") "v_or_b32 v160, v160, v161\n\t" PV_TEST
      "s_cbranch_scc1 7f\n\t"
      PV_LD2("128", "131", "132", "135")
      "s_waitcnt vmcnt(2)\n\t"
      PV_X4("v160", "144", "145", "146", "147") PV_X4("v161", "148", "149", "150", "151") "v_or_b32 v160, v160, v161\n\t" PV_TEST
      "s_cbranch_scc1 8f\n\t"
      "s_sub_u32 %[cnt], %[cnt], 1\n\ts_cbranch_scc0 1b\n\t"
      "s_waitcnt vmcnt(0)\n"
      "7:\n\t"
      PV_MOV2("%[r0]", "128", "129") PV_MOV2("%[r1]", "130", "131") PV_MOV2("%[r2]", "132", "133") PV_MOV2("%[r3]", "134", "135")
      "s_branch 9f\n"
      "8:\n\t"
      PV_MOV2("%[r0]", "144", "145") PV_MOV2("%[r1]", "146", "147") PV_MOV2("%[r2]", "148", "149") PV_MOV2("%[r3]", "150", "151")
      "9:\n\t"
      : [r0] "=&v"(r0), [r1] "=&v"(r1), [r2] "=&v"(r2), [r3] "=&v"(r3), [cnt] "+s"(cnt)
      : [o0] "v"(o0), [rs] "s"(rs), [T] "s"(T), [M] "s"(M), [stag] "n"(PSTAG)
      : PV_CLOB);
  a = u32x4{r0.x, r0.y, r1.x, r1.y}; b = u32x4{r2.x, r2.y, r3.x, r3.y};
}
#define PV_LD3(r0, r1, r2, r3, r4) \
  "buffer_load_dwordx4 v[" r0 ":" r1 "], %[o0], %[rs], 0 offen sc1\n\t" "buffer_load_dwordx4 v[" r2 ":" r3 "], %[o0], %[rs], 0 offen offset:16 sc1\n\t" \
  "buffer_load_dword v" r4 ", %[o1], %[rs], 0 offen sc1\n\t"
__device__ __forceinline__ void poll3(rsrc_t rs, int o0, int o1, u32 T, u32 M, u32x4& a, u32x4& b, u32& c) {
  u32x2 r0, r1, r2, r3; u32 r4, cnt = SPIN_CAP;
  asm volatile(
      PV_LD3("128", "131", "132", "135", "136")
      "s_sleep %[stag]\n"
      "1:\n\t"
      PV_LD3("144", "147", "148", "151", "152")
      "s_waitcnt vmcnt(3)\n\t"
      PV_X4("v160", "128", "129", "130", "131") PV_X4("v161", "132", "133", "134", "135") "v_xor_b32 v162, %[T], v136\n\t"
      "v_or3_b32 v160, v160, v161, v162\n\t" PV_TEST
      "s_cbranch_scc1 7f\n\t"
      PV_LD3("128", "131", "132", "135", "136")
      "s_waitcnt vmcnt(3)\n\t"
      PV_X4("v160", "144", "145", "146", "147") PV_X4("v161", "148", "149", "150", "151") "v_xor_b32 v162, %[T], v152\n\t"
      "v_or3_b32 v160, v160, v161, v162\n\t" PV_TEST
      "s_cbranch_scc1 8f\n\t"
      "s_sub_u32 %[cnt], %[cnt], 1\n\ts_cbranch_scc0 1b\n\t"
      "s_waitcnt vmcnt(0)\n"
      "7:\n\t"
      PV_MOV2("%[r0]", "128", "129") PV_MOV2("%[r1]", "130", "131") PV_MOV2("%[r2]", "132", "133") PV_MOV2("%[r3]", "134", "135") "v_mov_b32 %[r4], v136\n\t"
      "s_branch 9f\n"
      "8:\n\t"
      PV_MOV2("%[r0]", "144", "145") PV_MOV2("%[r1]", "146", "147") PV_MOV2("%[r2]", "148", "149") PV_MOV2("%[r3]", "150", "151") "v_mov_b32 %[r4], v152\n\t"
      "9:\n\t"
      : [r0] "=&v"(r0), [r1] "=&v"(r1), [r2] "=&v"(r2), [r3] "=&v"(r3), [r4] "=&v"(r4), [cnt] "+s"(cnt)
      : [o0] "v"(o0), [o1] "v"(o1), [rs] "s"(rs), [T] "s"(T), [M] "s"(M), [stag] "n"(PSTAG)
      : PV_CLOB);
  a = u32x4{r0.x, r0.y, r1.x, r1.y}; b = u32x4{r2.x, r2.y, r3.x, r3.y}; c = r4;
}

__device__ __forceinline__ unsigned long long now() { return __builtin_amdgcn_s_memrealtime(); }
__device__ __forceinline__ void not_before(unsigned long long t0, unsigned ticks) {
  while (now() - t0 < ticks) __builtin_amdgcn_s_sleep(2);
}
__device__ __forceinline__ u32 lds_ld(u32 addr) {
  u32 v;
  asm volatile("ds_read_b32 %0, %1\n\ts_waitcnt lgkmcnt(0)" : "=v"(v) : "v"(addr) : "memory");
  return v;
}
__device__ __forceinline__ void lds_st(u32 addr, u32 v) { asm volatile("ds_write_b32 %0, %1" ::"v"(addr), "v"(v) : "memory"); }
__device__ __forceinline__ void wait_word(int word, u32 want) {
  unsigned spins = 0;
#pragma clang loop unroll(disable)
  while ((u32)__builtin_amdgcn_readfirstlane(lds_ld(L_FL + 4 * word)) < want)
    if (++spins > LSPIN_CAP) break;
}
__device__ __forceinline__ void wait_word_sleep(int word, u32 want) {
  unsigned spins = 0;
#pragma clang loop unroll(disable)
  while ((u32)__builtin_amdgcn_readfirstlane(lds_ld(L_FL + 4 * word)) < want) {
    __builtin_amdgcn_s_sleep(2);
    if (++spins > LSPIN_CAP) break;
  }
}
__device__ __forceinline__ void wait_all8(int word0, u32 want, int lane) {
  unsigned spins = 0;
#pragma clang loop unroll(disable)
  while (!__all(lds_ld(L_FL + 4 * (word0 + (lane & 7))) >= want)) {
    __builtin_amdgcn_s_sleep(1);
    if (++spins > LSPIN_CAP) break;
  }
}
__device__ __forceinline__ void cbar(u32 n, int lane) {
  asm volatile("s_waitcnt lgkmcnt(0)" ::: "memory");
  if (lane == 0) asm volatile("ds_add_u32 %0, %1" ::"v"((u32)(L_FL + 4 * F_BAR)), "v"(1u) : "memory");
  wait_word(F_BAR, 8u * n);
}
__device__ __forceinline__ void hw_barrier() { __builtin_amdgcn_sched_barrier(0); __builtin_amdgcn_s_barrier(); __builtin_amdgcn_sched_barrier(0); }
__device__ __forceinline__ void wg_barrier() {
  asm volatile("s_waitcnt lgkmcnt(0)" ::: "memory");
  hw_barrier();
  asm volatile("" ::: "memory");
}
__device__ __forceinline__ void glds16(const void* sbase, u32 voff, u32 lds_dst) {
  asm volatile("s_mov_b32 m0, %2\n\ts_nop 0\n\tglobal_load_lds_dwordx4 %0, %1 nt" ::"v"(voff), "s"(sbase), "s"(lds_dst) : "memory");
}
__device__ __forceinline__ int vswz(int row) { return ((row & 3) << 2) | ((row >> 2) & 3); }
__device__ __forceinline__ int swz(int row) { return ((row >> 1) & 1) | ((row >> 2) & 2); }
__device__ __forceinline__ u32 rowoff(int q, int stride, int lane) { const int row = 8 * q + (lane >> 3); return row * stride + (((lane & 7) ^ (swz(row) << 1)) & 7) * 16; }
template <int W> __device__ __forceinline__ void tile_dma(const unsigned char* a, int stride, u32 o0, u32 o1, u32 dst) {
  glds16(a, o0, dst); if (W >= 0) waitvm<W>();
  glds16(a, o1, dst + 1024); if (W >= 0) waitvm<W>();
  glds16(a + (size_t)16 * stride, o0, dst + 2048); if (W >= 0) waitvm<W>();
  glds16(a + (size_t)16 * stride, o1, dst + 3072); if (W >= 0) waitvm<W>();
}
__device__ __forceinline__ void tile_frags_lds(const unsigned char* lds, u32 base, int lane, u32x4 (&f)[4]) {
  const int g = lane >> 4, q = (lane & 15) >> 2, p = lane & 3;
#pragma unroll
  for (int e = 0; e < 4; ++e) {
    u32x2 h[2];
#pragma unroll
    for (int t = 0; t < 2; ++t) {
      const int row = 8 * g + 4 * t + q;
      const u32 a = base + row * 128 + (((2 * e + (p >> 1)) ^ (swz(row) << 1)) << 4) + 8 * (p & 1);
      h[t] = __builtin_bit_cast(u32x2, __builtin_amdgcn_ds_read_tr16_b64_v4i16((LAS s16x4*)(lds + a)));
    }
    f[e] = u32x4{h[0].x, h[0].y, h[1].x, h[1].y};
  }
}

#define ESTAMP(k) do { } while (0)
#define STAMP(k) do { } while (0)
#define STAMP_FLUSH() do { } while (0)

__device__ __forceinline__ u32x4 afrag(u32x4 a, u32x4 b) {
  u32x4 f;
  f.x = __builtin_amdgcn_perm(a.y, a.x, 0x07060302u);
  f.y = __builtin_amdgcn_perm(a.w, a.z, 0x07060302u);
  f.z = __builtin_amdgcn_perm(b.y, b.x, 0x07060302u);
  f.w = __builtin_amdgcn_perm(b.w, b.z, 0x07060302u);
  return f;
}

struct RowStats { float m, rs; };
template <int N> __device__ __forceinline__ RowStats row_stats(const float (&v)[N], const bool (&valid)[N], float pivot) {
  float s1[2] = {0.f, 0.f}, s2[2] = {0.f, 0.f};
#pragma unroll
  for (int e = 0; e < N; ++e) { const float d = valid[e] ? v[e] - pivot : 0.f; s1[e & 1] += d; s2[e & 1] = fmaf(d, d, s2[e & 1]); }
  const float m1 = wave_sum(s1[0] + s1[1]) * (1.0f / DM), m2 = wave_sum(s2[0] + s2[1]) * (1.0f / DM);
  const float var = rbf(fmax_(fmaf(-m1, m1, m2), 0.f));
  RowStats r;
  r.m = rbf(pivot + m1);
  r.rs = __builtin_amdgcn_rcpf(rbf(__builtin_amdgcn_sqrtf(rbf(var + 1e-5f))));
  return r;
}
__device__ __forceinline__ float ln_apply(float v, RowStats s) { return rbf(v - s.m) * s.rs; }

enum { ROLE_W = 0, ROLE_G = 1, ROLE_F = 2 };

__global__ void __launch_bounds__(NTHR, 3) fused_kernel(Params p) {
  extern __shared__ __attribute__((aligned(16))) unsigned char lds[];
  const int tid = threadIdx.x, lane = tid & 63, w = __builtin_amdgcn_readfirstlane(tid >> 6);
  const int wg = blockIdx.x, xcd = wg & 7, s = wg >> 3, j = xcd + 8 * (s >> 4), i = s & 15, b = i >> 1, half = i & 1;
  const size_t ubase = ((size_t)(b * HKV + j) * SK + (SK - WIN)) * DH;
  const int cq = i & 3, kp = i >> 2;
  const int ks0 = kp < 3 ? 4 * kp : 13, nt = kp < 2 ? 4 : 5;
  const u32 PK = 4 * nt, PV = PK + 128, PW = PK + 256;
  const int g4 = lane >> 4, c16 = lane & 15;
#define RSTAMP(k)
  const unsigned long long pt0 = __builtin_amdgcn_s_memrealtime();
  unsigned long long px = 0;
#define PMARK(n) do { if (PROBE == (n)) px = __builtin_amdgcn_s_memrealtime() - pt0; } while (0)
  STAMP(0);
  const int cw = (w + 7) & 7;
  u32 xr[5];
  u32x4 xa;
  {
    const GAS u32* xrow = (const GAS u32*)(p.x + cw * DM);
#pragma unroll
    for (int m = 0; m < 4; ++m) xr[m] = __builtin_nontemporal_load(xrow + lane + 64 * m);
    xr[4] = __builtin_nontemporal_load(xrow + (lane & 31) + 256);
    xa = __builtin_nontemporal_load((const GAS u32x4*)(p.x + (lane & 7) * DM + 32 * (ks0 + (cw < nt ? cw : 0)) + 8 * g4));
  }
  if (w == 0) {
    if (lane < 32) lds_st(L_FL + 4 * lane, 0u);
    asm volatile("s_waitcnt lgkmcnt(0)" ::: "memory");
  }
  hw_barrier();

  if (w == 0) {
    __builtin_amdgcn_s_setprio(3);
    u32 pub = 0;
#define PUBLISH(W) lds_st(L_FL + 4 * F_LANDED, pub - (W))
    {
      const unsigned char* a = (const unsigned char*)(p.wq + (size_t)32 * ks0 * NQ + 256 * j + 64 * cq);
      const u32 o0 = rowoff(0, NQ * 2, lane), o1 = rowoff(1, NQ * 2, lane);
#pragma unroll
      for (int t = 0; t < 4; ++t) tile_dma<-1>(a + (size_t)t * 32 * NQ * 2, NQ * 2, o0, o1, L_EXT + t * 4096);
      if (nt == 5) tile_dma<-1>(a + (size_t)4 * 32 * NQ * 2, NQ * 2, o0, o1, L_QSUM);
      pub = PK;
    }
    ESTAMP(8);
    {
      const unsigned char* kb = (const unsigned char*)(p.kc + ubase + (size_t)half * 512 * DH);
      u32 koff[4];
#pragma unroll
      for (int q = 0; q < 4; ++q) { const int key = 4 * q + g4; koff[q] = key * 256 + ((c16 ^ key) & 15) * 16; }
#pragma clang loop unroll(disable)
      for (int t = 0; t < 32; ++t) {
#pragma unroll
        for (int q = 0; q < 4; ++q) { glds16(kb + t * 4096, koff[q], (t * 4 + q) * 1024); waitvm<LWIN>(); }
        pub += 4;
        if (pub > LWIN) PUBLISH(LWIN);
      }
    }
    STAMP(1);
    {
      const unsigned char* vb = (const unsigned char*)(p.vc + ubase + (size_t)half * 512 * DH);
      u32 voff[4];
#pragma unroll
      for (int q = 0; q < 4; ++q) { const int key = 4 * q + g4; voff[q] = key * 256 + ((c16 ^ vswz(key)) & 15) * 16; }
      wait_word(F_QPUB, 1u);
#pragma clang loop unroll(disable)
      for (int t = 0; t < 32; ++t) {
        if ((t & 7) == 4) wait_all8(F_KDONE, (t >> 3) + 1, lane);
        const u32 dst = t < 4 ? L_EXT + t * 4096 : (t - 4) * 4096;
#pragma unroll
        for (int q = 0; q < 4; ++q) { glds16(vb + t * 4096, voff[q], dst + q * 1024); waitvm<LWIN>(); }
        pub += 4; PUBLISH(LWIN);
      }
    }
    STAMP(2);
    wait_word_sleep(F_RANK, 1u);
    const int rank = (int)__builtin_amdgcn_readfirstlane(lds_ld(L_FL + 4 * F_RANK)) - 1;
    const int role = rank < 72 ? ROLE_G : (rank < 216 ? ROLE_W : ROLE_F);
    if (role == ROLE_W) {
      const int jw = (rank - 72) / 9, cg = (rank - 72) % 9;
      const unsigned char* a = (const unsigned char*)(p.wo + (size_t)256 * jw * DM + 64 * cg);
      const u32 o0 = rowoff(0, DM * 2, lane), o1 = rowoff(1, DM * 2, lane);
      wait_all8(F_VDONE, 1, lane);
#pragma clang loop unroll(disable)
      for (int t = 0; t < 8; ++t) { tile_dma<LWIN>(a + (size_t)t * 32 * DM * 2, DM * 2, o0, o1, t * 4096); pub += 4; PUBLISH(LWIN); }
    } else if (role == ROLE_G) {
      const int gx = rank;
      const u32 o0 = rowoff(0, FF * 2, lane), o1 = rowoff(1, FF * 2, lane);
#pragma clang loop unroll(disable)
      for (int t = 0; t < 36; ++t) {
        if (t == 0 || t == 16) wait_all8(F_VDONE, (t >> 4) + 1, lane);
        const unsigned char* a = (const unsigned char*)(((t & 1) ? p.wu : p.wg) + (size_t)(t >> 1) * 32 * FF + 64 * gx);
        tile_dma<GWIN>(a, FF * 2, o0, o1, t < 4 ? L_EXT + t * 4096 : (t - 4) * 4096);
        pub += 4;
        if (4 * t + 4 > GWIN - LWIN) PUBLISH(GWIN);
      }
    }
    STAMP(3);
#define DR(N) if (GWIN > N && (role == ROLE_G || LWIN > N)) { waitvm<N>(); PUBLISH(N); }
    DR(44) DR(40) DR(36) DR(32) DR(28) DR(24) DR(20) DR(16) DR(12) DR(8) DR(4) DR(0)
#undef DR
#undef PUBLISH
    STAMP(14);
    STAMP_FLUSH();
    return;
  }

  const int ct = tid - 64;
  const rsrc_t rs = __builtin_amdgcn_make_buffer_rsrc((void*)p.ws, 0, WS_END, 0x00020000);
  const u32 T = *(const __attribute__((address_space(4))) u32*)(p.ws + WS_EPOCH) + 1u;
  const u32 T16 = T & 0xFFFFu, T8 = T & 0xFFu, T4 = T & 0xFu;
  const u32 tstart = (u32)now();
  if (ct == 448) __builtin_amdgcn_raw_buffer_store_b64(u32x2{tstart, T}, rs, WS_RANK + wg * 8, 0, AUX_SC1);
  float* const lf = (float*)lds;
  u32* const lu = (u32*)lds;
  {
    ESTAMP(8);
    asm volatile("" :: "v"(xr[0]), "v"(xr[4]));
    ESTAMP(9);
    float v[10]; bool valid[10];
#pragma unroll
    for (int m = 0; m < 5; ++m) { valid[2 * m] = valid[2 * m + 1] = (m < 4) || (lane < 32); v[2 * m] = bflo(xr[m]); v[2 * m + 1] = bfhi(xr[m]); }
    const float pivot = __uint_as_float(__builtin_amdgcn_readfirstlane(xr[0]) << 16);
    const RowStats st = row_stats<10>(v, valid, pivot);
    if (lane == 0) *(f32x2*)(lf + L_ST / 4 + 2 * cw) = f32x2{st.m, st.rs};
    ESTAMP(10);
    cbar(1, lane);
    STAMP(1);
    PMARK(1);
  }
  if (cw < nt) {
    const f32x2 sv = *(const f32x2*)(lf + L_ST / 4 + 2 * (lane & 7));
    const RowStats st = {sv.x, sv.y};
    u32x4 a;
#pragma unroll
    for (int q = 0; q < 4; ++q) a[q] = pack2(ln_apply(bflo(xa[q]), st), ln_apply(bfhi(xa[q]), st));
    const u32 base = cw < 4 ? L_EXT + cw * 4096 : L_QSUM;
    wait_word(F_LANDED, 4 * (cw + 1));
    u32x4 f[4]; tile_frags_lds(lds, base, lane, f);
    f32x4 d[4];
#pragma unroll
    for (int e = 0; e < 4; ++e) d[e] = mfma(f[e], a, f32x4{0.f, 0.f, 0.f, 0.f});
    if (c16 < 8) {
#pragma unroll
      for (int e = 0; e < 4; ++e) *(f32x4*)(lf + base / 4 + c16 * 64 + 16 * e + 4 * g4) = d[e];
    }
  }
  ESTAMP(11);
  cbar(2, lane);
  ESTAMP(12);
  {
    float sum = (lf[L_EXT / 4 + cw * 64 + lane] + lf[L_EXT / 4 + 1024 + cw * 64 + lane]) + (lf[L_EXT / 4 + 2048 + cw * 64 + lane] + lf[L_EXT / 4 + 3072 + cw * 64 + lane]);
    if (nt == 5) sum += lf[L_QSUM / 4 + cw * 64 + lane];
    const u32 v = put_tag<0xFu>(sum, T4);
    const u32x4 o = {v, (u32)__builtin_amdgcn_mov_dpp((int)v, 0x55, 0xf, 0xf, true), (u32)__builtin_amdgcn_mov_dpp((int)v, 0xAA, 0xf, 0xf, true),
                     (u32)__builtin_amdgcn_mov_dpp((int)v, 0xFF, 0xf, 0xf, true)};
    if ((lane & 3) == 0) st16c(rs, WS_QS + ((((j * 8 + cw) * 4 + kp) * 256) + 64 * cq + lane) * 4, o);
  }
  cbar(3, lane);
  if (ct == 0) lds_st(L_FL + 4 * F_QPUB, 1u);
  STAMP(2);
  PMARK(2);
  if (cw >= 2 && cw < 6) {
    u32x4 qg;
    __builtin_amdgcn_s_sleep(QDELAY);
    {
      unsigned spins = 0;
#pragma clang loop unroll(disable)
      for (;;) {
        qg = ld16c(rs, WS_QS + (((j * 8 + b) * 4 + (cw - 2)) * 256 + 4 * lane) * 4);
        if (__all(tag_ok<0xFu>(qg, T4)) || ++spins > SPIN_CAP) break;
        __builtin_amdgcn_s_sleep(4);
      }
    }
    *(f32x4*)(lf + L_QSUM / 4 + (cw - 2) * 256 + 4 * lane) = f32x4{get_tag<0xFu>(qg.x), get_tag<0xFu>(qg.y), get_tag<0xFu>(qg.z), get_tag<0xFu>(qg.w)};
  }
  cbar(4, lane);
  if (cw == 0) {
    f32x4 sv[4];
#pragma unroll
    for (int q = 0; q < 4; ++q) sv[q] = *(const f32x4*)(lf + L_QSUM / 4 + q * 256 + 4 * lane);
    const f32x4 sum = (sv[0] + sv[1]) + (sv[2] + sv[3]);
    *(u32x2*)(lu + L_Q / 4 + 2 * lane) = u32x2{pack2(sum.x, sum.y), pack2(sum.z, sum.w)};
    asm volatile("s_waitcnt lgkmcnt(0)" ::: "memory");
    if (lane == 0) lds_st(L_FL + 4 * F_QRDY, 1u);
  } else {
    wait_word(F_QRDY, 1u);
  }
  STAMP(3);
  PMARK(3);
  float s0, s1;
  {
    u32x4 qa[4];
#pragma unroll
    for (int ks = 0; ks < 4; ++ks) qa[ks] = *(const u32x4*)(lu + L_Q / 4 + (lane & 1) * 64 + 16 * ks + 4 * g4);
    f32x4 d[4];
#pragma unroll
    for (int m = 0; m < 4; ++m) {
      const int tile = cw + 8 * m;
      wait_word_sleep(F_LANDED, PK + 4 * (tile + 1));
      u32x4 kf[4];
#pragma unroll
      for (int ks = 0; ks < 4; ++ks) kf[ks] = *(const u32x4*)(lds + tile * 4096 + c16 * 256 + (((4 * ks + g4) ^ c16) << 4));
      f32x4 acc = mfma(qa[0], kf[0], f32x4{0.f, 0.f, 0.f, 0.f});
      acc = mfma(qa[1], kf[1], acc); acc = mfma(qa[2], kf[2], acc); acc = mfma(qa[3], kf[3], acc);
      d[m] = acc;
      asm volatile("s_waitcnt lgkmcnt(0)" ::: "memory");
      if (lane == 0) lds_st(L_FL + 4 * (F_KDONE + cw), m + 1);
    }
    const float x0 = g4 == 0 ? d[0][0] : g4 == 1 ? d[1][0] : g4 == 2 ? d[2][0] : d[3][0];
    const float x1 = g4 == 0 ? d[0][1] : g4 == 1 ? d[1][1] : g4 == 2 ? d[2][1] : d[3][1];
    constexpr float SCALE = 0.08838834764831845f;
    s0 = rbf(rbf(x0) * SCALE); s1 = rbf(rbf(x1) * SCALE);
  }
  STAMP(4);
  PMARK(4);
  u32x4 mine;
  {
    constexpr float L2E = 1.4426950408889634f;
    const float m0 = wave_max(s0), m1 = wave_max(s1);
    const float l0 = wave_sum(__builtin_amdgcn_exp2f((s0 - m0) * L2E)), l1 = wave_sum(__builtin_amdgcn_exp2f((s1 - m1) * L2E));
    if (lane == 0) *(f32x4*)(lf + L_ST / 4 + 16 + 4 * cw) = f32x4{m0, m1, l0, l1};
    cbar(5, lane);
    float M0 = -INFINITY, M1 = -INFINITY;
    f32x4 st[8];
#pragma unroll
    for (int ww = 0; ww < 8; ++ww) { st[ww] = *(const f32x4*)(lf + L_ST / 4 + 16 + 4 * ww); M0 = fmax_(M0, st[ww].x); M1 = fmax_(M1, st[ww].y); }
    float L0 = 0.f, L1 = 0.f;
#pragma unroll
    for (int ww = 0; ww < 8; ++ww) {
      L0 = fmaf(st[ww].z, __builtin_amdgcn_exp2f((st[ww].x - M0) * L2E), L0);
      L1 = fmaf(st[ww].w, __builtin_amdgcn_exp2f((st[ww].y - M1) * L2E), L1);
    }
    const int kl = 16 * (cw + 8 * g4) + c16;
    __bf16* P = (__bf16*)(lds + L_P);
    P[kl] = (__bf16)__builtin_amdgcn_exp2f((s0 - M0) * L2E);
    P[512 + kl] = (__bf16)__builtin_amdgcn_exp2f((s1 - M1) * L2E);
    mine = u32x4{__float_as_uint(M0) | T16, __float_as_uint(M1) | T16, put_tag<0xFFu>(L0, T8), put_tag<0xFFu>(L1, T8)};
    if (ct == 64) st16c(rs, WS_SC + wg * 128, mine);
  }
  cbar(6, lane);
  STAMP(6);
  if (cw == 7) {
    u32x4 e0, e1; unsigned spins = 0;
#pragma clang loop unroll(disable)
    for (;;) {
      e0 = ld16c(rs, WS_RANK + lane * 32); e1 = ld16c(rs, WS_RANK + lane * 32 + 16);
      if (__all((e0.y == T) & (e0.w == T) & (e1.y == T) & (e1.w == T)) || ++spins > SPIN_CAP) break;
      __builtin_amdgcn_s_sleep(4);
    }
    auto before = [&](u32 t, int g) -> float { const int d = (int)(t - tstart); return (d < 0 || (d == 0 && g < wg)) ? 1.f : 0.f; };
    const float n = wave_sum((before(e0.x, 4 * lane) + before(e0.z, 4 * lane + 1)) + (before(e1.x, 4 * lane + 2) + before(e1.z, 4 * lane + 3)));
    if (lane == 0) lds_st(L_FL + 4 * F_RANK, (u32)n + 1u);
  }
  if (cw == 1) {
    constexpr float L2E = 1.4426950408889634f;
    u32x4 oth; unsigned spins = 0;
#pragma clang loop unroll(disable)
    for (;;) {
      oth = ld16c(rs, WS_SC + (wg ^ 8) * 128);
      if (__all(((((oth.x ^ T16) | (oth.y ^ T16)) & 0xFFFFu) | (((oth.z ^ T8) | (oth.w ^ T8)) & 0xFFu)) == 0u) || ++spins > SPIN_CAP) break;
      __builtin_amdgcn_s_sleep(4);
    }
    const float M0 = bfhi(mine.x), M1 = bfhi(mine.y), om0 = bfhi(oth.x), om1 = bfhi(oth.y);
    const float G0 = fmax_(M0, om0), G1 = fmax_(M1, om1);
    const float e0 = __builtin_amdgcn_exp2f((M0 - G0) * L2E), e1 = __builtin_amdgcn_exp2f((M1 - G1) * L2E);
    const float S0 = get_tag<0xFFu>(mine.z) * e0 + get_tag<0xFFu>(oth.z) * __builtin_amdgcn_exp2f((om0 - G0) * L2E);
    const float S1 = get_tag<0xFFu>(mine.w) * e1 + get_tag<0xFFu>(oth.w) * __builtin_amdgcn_exp2f((om1 - G1) * L2E);
    if (lane == 0) *(f32x2*)(lf + L_ST / 4 + 48) = f32x2{e0 / S0, e1 / S1};
    STAMP(5);
  }
  {
    f32x4 d[8];
#pragma unroll
    for (int e = 0; e < 8; ++e) d[e] = f32x4{0.f, 0.f, 0.f, 0.f};
    const int q4 = (lane & 15) >> 2, p4 = lane & 3;
#pragma unroll
    for (int m = 0; m < 2; ++m) {
      const int ks = cw + 8 * m;
      const u32x4 pf = *(const u32x4*)(lds + L_P + (lane & 1) * 1024 + (32 * ks + 8 * g4) * 2);
      const u32 base = ks < 2 ? L_EXT + ks * 8192 : (ks - 2) * 8192;
      wait_word(F_LANDED, PV + 8 * (ks + 1));
      u32x4 f[8];
#pragma unroll
      for (int e = 0; e < 8; ++e) {
        u32x2 h[2];
#pragma unroll
        for (int t = 0; t < 2; ++t) {
          const int row = 8 * g4 + 4 * t + q4;
          const u32 a = base + row * 256 + (((2 * e + (p4 >> 1)) ^ vswz(row)) << 4) + 8 * (p4 & 1);
          h[t] = __builtin_bit_cast(u32x2, __builtin_amdgcn_ds_read_tr16_b64_v4i16((LAS s16x4*)(lds + a)));
        }
        f[e] = u32x4{h[0].x, h[0].y, h[1].x, h[1].y};
      }
      SB();
#pragma unroll
      for (int e = 0; e < 8; ++e) d[e] = mfma(f[e], pf, d[e]);
      asm volatile("s_waitcnt lgkmcnt(0)" ::: "memory");
      if (lane == 0) lds_st(L_FL + 4 * (F_VDONE + cw), m + 1);
    }
    if (c16 < 2) {
#pragma unroll
      for (int e = 0; e < 8; ++e) *(f32x4*)(lf + L_QSUM / 4 + cw * 256 + c16 * 128 + 16 * e + 4 * g4) = d[e];
    }
  }
  cbar(7, lane);
  auto gather4 = [&](u32 v) -> u32x4 {
    return u32x4{v, (u32)__builtin_amdgcn_mov_dpp((int)v, 0x55, 0xf, 0xf, true), (u32)__builtin_amdgcn_mov_dpp((int)v, 0xAA, 0xf, 0xf, true),
                 (u32)__builtin_amdgcn_mov_dpp((int)v, 0xFF, 0xf, 0xf, true)};
  };
  if (cw < 4) {
    float sv[8];
#pragma unroll
    for (int ww = 0; ww < 8; ++ww) sv[ww] = lf[L_QSUM / 4 + ww * 256 + ct];
    const float sum = (((sv[0] + sv[1]) + (sv[2] + sv[3])) + ((sv[4] + sv[5]) + (sv[6] + sv[7]))) * lf[L_ST / 4 + 48 + (cw >> 1)];
    const u32x4 o = gather4(put_tag<0xFFu>(sum, T8));
    if ((lane & 3) == 0) st16c(rs, WS_OS + (((j * 8 + b) * 2 + half) * 256 + ct) * 4, o);
  }
  const unsigned long long to = now();
  wait_word(F_RANK, 1u);
  const int rank = (int)__builtin_amdgcn_readfirstlane(lds_ld(L_FL + 4 * F_RANK)) - 1;
  const int role = rank < 72 ? ROLE_G : (rank < 216 ? ROLE_W : ROLE_F);
  STAMP(7);
  PMARK(7);

  auto acc_to_lds = [&](int base, const f32x4 (&d)[4]) {
    if (c16 < 8) {
#pragma unroll
      for (int e = 0; e < 4; ++e) *(f32x4*)(lf + base / 4 + (cw * 8 + c16) * 64 + 16 * e + 4 * g4) = d[e];
    }
  };
  auto publish_partial = [&](int base, int jw, int cg) {
    float sv[8];
#pragma unroll
    for (int ww = 0; ww < 8; ++ww) sv[ww] = lf[L_EXT / 4 + ww * 512 + cw * 64 + lane];
    const float sum = ((sv[0] + sv[1]) + (sv[2] + sv[3])) + ((sv[4] + sv[5]) + (sv[6] + sv[7]));
    const u32x4 o = gather4(put_tag<0xFFu>(sum, T8));
    if ((lane & 3) == 0) st16c(rs, base + (((cw * 9 + cg) * 16 + jw) * 64 + lane) * 4, o);
  };

  if (role == ROLE_W) {
    const int jw = (rank - 72) / 9, cg = (rank - 72) % 9;
    u32x4 f[4];
    wait_word(F_LANDED, PW + 32);
    tile_frags_lds(lds, cw * 4096, lane, f);
    {
      const int off = WS_OS + ((jw * 8 + (lane & 7)) * 512 + 32 * cw + 8 * g4) * 4;
      u32x4 a0 = {0u, 0u, 0u, 0u}, a1 = a0, b0 = a0, b1 = a0;
      if (c16 < 8)
        POLL((a0 = ld16c(rs, off), a1 = ld16c(rs, off + 16), b0 = ld16c(rs, off + 1024), b1 = ld16c(rs, off + 1040),
              tag_ok<0xFFu>(a0, T8) & tag_ok<0xFFu>(a1, T8) & tag_ok<0xFFu>(b0, T8) & tag_ok<0xFFu>(b1, T8)));
      STAMP(8);
      RSTAMP(0);
#define S2(x, y) (get_tag<0xFFu>(x) + get_tag<0xFFu>(y))
      const u32x4 a = {pack2(S2(a0.x, b0.x), S2(a0.y, b0.y)), pack2(S2(a0.z, b0.z), S2(a0.w, b0.w)), pack2(S2(a1.x, b1.x), S2(a1.y, b1.y)), pack2(S2(a1.z, b1.z), S2(a1.w, b1.w))};
#undef S2
      f32x4 d[4];
#pragma unroll
      for (int e = 0; e < 4; ++e) d[e] = mfma(f[e], a, f32x4{0.f, 0.f, 0.f, 0.f});
      acc_to_lds(L_EXT, d);
    }
    wg_barrier();
    publish_partial(WS_PW, jw, cg);
    const unsigned long long tw = now();
    STAMP(9);
    u32x4 f2[4];
    {
      const unsigned char* a = (const unsigned char*)(p.wd + (size_t)(288 * jw + 32 * cw) * DM + 64 * cg);
      const u32 o0 = rowoff(0, DM * 2, lane), o1 = rowoff(1, DM * 2, lane);
      tile_dma<-1>(a, DM * 2, o0, o1, (8 + cw) * 4096);
      if (cw == 0) tile_dma<-1>(a + 256 * DM * 2, DM * 2, o0, o1, 16 * 4096);
      waitvm<0>();
      tile_frags_lds(lds, (8 + cw) * 4096, lane, f);
      if (cw == 0) tile_frags_lds(lds, 16 * 4096, lane, f2);
    }
    {
      const int off = WS_AS + ((lane & 7) * FF + 288 * jw + 32 * cw + 8 * g4) * 4;
      u32x4 R[4] = {};
      not_before(tw, GATE_ACT);
      if (c16 < 8) {
        if (cw == 0) poll4(rs, off, off + 16, off + 1024, off + 1040, T16, 0xFFFFu, R);
        else poll2(rs, off, T16, 0xFFFFu, R[0], R[1]);
      }
      STAMP(12);
      const u32x4 a = afrag(R[0], R[1]);
      f32x4 d[4];
#pragma unroll
      for (int e = 0; e < 4; ++e) d[e] = mfma(f[e], a, f32x4{0.f, 0.f, 0.f, 0.f});
      if (cw == 0) {
        const u32x4 ab = afrag(R[2], R[3]);
#pragma unroll
        for (int e = 0; e < 4; ++e) d[e] = mfma(f2[e], ab, d[e]);
      }
      acc_to_lds(L_EXT, d);
    }
    wg_barrier();
    publish_partial(WS_PD, jw, cg);
    STAMP(13);
  } else if (role == ROLE_G) {
    const int gx = rank;
    u32x4 f[5][4];
#pragma unroll
    for (int tl = 0; tl < 5; ++tl) {
      if (tl < 4 || cw < 4) {
        const int tau = cw + 8 * tl;
        wait_word(F_LANDED, PW + 4 * (tau + 1));
        tile_frags_lds(lds, tau < 4 ? L_EXT + tau * 4096 : (tau - 4) * 4096, lane, f[tl]);
      }
    }
    wait_word(F_LANDED, PW + 144);
    STAMP(8);
    {
      const int off = WS_HS + (xcd * 8 + cw) * DM * 4;
      u32x4 h0, h1; u32 h2;
      not_before(to, GATE_H);
      poll3(rs, off + lane * 32, off + 2048 + lane * 4, T16, 0xFFFFu, h0, h1, h2);
      STAMP(10);
      float v[9]; bool valid[9];
#pragma unroll
      for (int e = 0; e < 4; ++e) { v[e] = bfhi(h0[e]); v[4 + e] = bfhi(h1[e]); }
      v[8] = bfhi(h2);
#pragma unroll
      for (int e = 0; e < 9; ++e) valid[e] = true;
      const float pivot = __uint_as_float(__builtin_amdgcn_readfirstlane(h0.x) & 0xFFFF0000u);
      const RowStats st = row_stats<9>(v, valid, pivot);
      u32* hn = lu + L_HN / 4 + cw * HN_STRIDE;
      *(u32x4*)(hn + 4 * lane) = u32x4{pack2(ln_apply(v[0], st), ln_apply(v[1], st)), pack2(ln_apply(v[2], st), ln_apply(v[3], st)),
                                       pack2(ln_apply(v[4], st), ln_apply(v[5], st)), pack2(ln_apply(v[6], st), ln_apply(v[7], st))};
      ((__bf16*)(hn + 256))[lane] = (__bf16)ln_apply(v[8], st);
    }
    wg_barrier();
    {
      f32x4 d[4];
      const u32* hn = lu + L_HN / 4 + (lane & 7) * HN_STRIDE + 16 * (cw >> 1) + 4 * g4;
      u32x4 a[5];
#pragma unroll
      for (int tl = 0; tl < 5; ++tl) a[tl] = *(const u32x4*)(hn + (tl < 4 || cw < 4 ? 64 * tl : 0));
      SB();
#pragma unroll
      for (int e = 0; e < 4; ++e) d[e] = mfma(f[0][e], a[0], f32x4{0.f, 0.f, 0.f, 0.f});
#pragma unroll
      for (int tl = 1; tl < 5; ++tl) {
        if (tl < 4 || cw < 4) {
#pragma unroll
          for (int e = 0; e < 4; ++e) d[e] = mfma(f[tl][e], a[tl], d[e]);
        }
      }
      acc_to_lds(0, d);
    }
    wg_barrier();
    {
      float gv[4], uv[4];
#pragma unroll
      for (int ww = 0; ww < 4; ++ww) { gv[ww] = lf[(2 * ww) * 512 + cw * 64 + lane]; uv[ww] = lf[(2 * ww + 1) * 512 + cw * 64 + lane]; }
      const float gg = rbf((gv[0] + gv[1]) + (gv[2] + gv[3])), uu = rbf((uv[0] + uv[1]) + (uv[2] + uv[3]));
      const float sl = rbf(gg * __builtin_amdgcn_rcpf(1.0f + __builtin_amdgcn_exp2f(gg * -1.4426950408889634f)));
      const u32x4 o = gather4(bfbits_hi(sl * uu) | T16);
      if ((lane & 3) == 0) st16c(rs, WS_AS + (cw * FF + 64 * gx + lane) * 4, o);
    }
    STAMP(11);
  } else {
    const int f = rank - 216;
    const int job = 2 * f + (cw >> 2);
    if ((cw & 3) == 0 && job < 72) {
      const int jb = job / 9, cg = job % 9;
      const u32x2 xq = *(const GAS u32x2*)(p.x + jb * DM + 64 * cg + 4 * c16);
      const u32x4 wr0 = *(const GAS u32x4*)(p.wr + 8 * lane);
      const u16 wr1 = ((const GAS u16*)p.wr)[512 + lane];
      auto gather16 = [&](int base) -> f32x4 {
        const int off = base + (((jb * 9 + cg) * 16 + g4) * 64 + 4 * c16) * 4;
        u32x4 R[4];
        poll4(rs, off, off + 1024, off + 2048, off + 3072, T8, 0xFFu, R);
        const u32x4 q0 = R[0], q1 = R[1], q2 = R[2], q3 = R[3];
        f32x4 sum;
#pragma unroll
        for (int e = 0; e < 4; ++e) {
          float t = (get_tag<0xFFu>(q0[e]) + get_tag<0xFFu>(q1[e])) + (get_tag<0xFFu>(q2[e]) + get_tag<0xFFu>(q3[e]));
          t = swap16_add(t, t); t = swap32_add(t, t);
          sum[e] = t;
        }
        return sum;
      };
      STAMP(3);
      not_before(to, GATE_PW);
      const f32x4 at = gather16(WS_PW);
      STAMP(8);
      PMARK(8);
      RSTAMP(0);
      f32x4 hv;
      hv[0] = rbf(bflo(xq.x) + rbf(at[0])); hv[1] = rbf(bfhi(xq.x) + rbf(at[1]));
      hv[2] = rbf(bflo(xq.y) + rbf(at[2])); hv[3] = rbf(bfhi(xq.y) + rbf(at[3]));
      {
        const u32x4 hq = {__float_as_uint(hv[0]) | T16, __float_as_uint(hv[1]) | T16, __float_as_uint(hv[2]) | T16, __float_as_uint(hv[3]) | T16};
        const int o = WS_HS + ((g4 * 8 + jb) * DM + 64 * cg + 4 * c16) * 4;
        st16c(rs, o, hq); st16c(rs, o + 4 * 8 * DM * 4, hq);
      }
      const unsigned long long th = now();
      STAMP(9);
      RowStats st; float sig;
      {
        const int off = WS_HS + (xcd * 8 + jb) * DM * 4;
        u32x4 h0, h1; u32 h2;
        {
          unsigned spins = 0;
#pragma clang loop unroll(disable)
          for (;;) {
            h0 = ld16c(rs, off + lane * 32); h1 = ld16c(rs, off + lane * 32 + 16); h2 = ld4c(rs, off + 2048 + lane * 4);
            if (__all(tag_ok<0xFFFFu>(h0, T16) & tag_ok<0xFFFFu>(h1, T16) & ((h2 & 0xFFFFu) == T16)) || ++spins > SPIN_CAP) break;
            __builtin_amdgcn_s_sleep(2);
          }
        }
        float v[9]; bool valid[9];
#pragma unroll
        for (int e = 0; e < 4; ++e) { v[e] = bfhi(h0[e]); v[4 + e] = bfhi(h1[e]); }
        v[8] = bfhi(h2);
#pragma unroll
        for (int e = 0; e < 9; ++e) valid[e] = true;
        const float pivot = __uint_as_float(__builtin_amdgcn_readfirstlane(h0.x) & 0xFFFF0000u);
        st = row_stats<9>(v, valid, pivot);
        float r = rbf(ln_apply(v[8], st)) * bflo((u32)wr1);
#pragma unroll
        for (int e = 0; e < 8; ++e) r = fmaf(rbf(ln_apply(v[e], st)), (e & 1) ? bfhi(wr0[e >> 1]) : bflo(wr0[e >> 1]), r);
        r = rbf(wave_sum(r));
        sig = rbf(__builtin_amdgcn_rcpf(1.0f + __builtin_amdgcn_exp2f(r * -1.4426950408889634f)));
      }
      float hn[4];
#pragma unroll
      for (int e = 0; e < 4; ++e) hn[e] = rbf(ln_apply(hv[e], st));
      STAMP(10);
      not_before(th, GATE_PD);
      const f32x4 y = gather16(WS_PD);
      STAMP(12);
      RSTAMP(1);
      float o[4];
#pragma unroll
      for (int e = 0; e < 4; ++e) o[e] = hv[e] + rbf(hn[e] + rbf(rbf(y[e]) * sig));
      if (lane < 16) *(GAS u32x2*)(p.out + jb * DM + 64 * cg + 4 * lane) = u32x2{pack2(o[0], o[1]), pack2(o[2], o[3])};
      PMARK(9);
      if (job == 0) { const unsigned long long until = 6000ull + (PROBE ? 10ull * px : 0ull); while (__builtin_amdgcn_s_memrealtime() - pt0 < until) __builtin_amdgcn_s_sleep(1); }
      if (job == 0 && lane == 0) __hip_atomic_store((GAS u32*)(p.ws + WS_EPOCH), T, __ATOMIC_RELAXED, __HIP_MEMORY_SCOPE_AGENT);
      STAMP(13);
    }
  }
  STAMP(14);
  STAMP_FLUSH();
}

}

extern "C" void kernel_launch(void* const* d_in, const int* in_sizes, int n_in, void* d_out, int out_size, void* d_ws, size_t ws_size,
                              hipStream_t stream) {
  Params p;
  p.x = (const u16*)d_in[0];
  p.kc = (const u16*)d_in[1];
  p.vc = (const u16*)d_in[2];
  p.wq = (const u16*)d_in[3];
  p.wo = (const u16*)d_in[4];
  p.wr = (const u16*)d_in[6];
  p.wg = (const u16*)d_in[7];
  p.wu = (const u16*)d_in[8];
  p.wd = (const u16*)d_in[9];
  p.out = (u16*)d_out;
  p.ws = (unsigned char*)d_ws;
  (void)hipFuncSetAttribute((const void*)fused_kernel, hipFuncAttributeMaxDynamicSharedMemorySize, LDS_BYTES);
  hipLaunchKernelGGL(fused_kernel, dim3(NWG), dim3(NTHR), LDS_BYTES, stream, p);
}
```

```cpp
#include <hip/hip_runtime.h>
#include <stdint.h>

typedef unsigned u32;
typedef unsigned short u16;
typedef u32 u32x4 __attribute__((ext_vector_type(4)));
typedef u32 u32x2 __attribute__((ext_vector_type(2)));
typedef float f32x4 __attribute__((ext_vector_type(4)));
typedef float f32x2 __attribute__((ext_vector_type(2)));
typedef short s16x4 __attribute__((ext_vector_type(4)));
typedef __bf16 bf16x8 __attribute__((ext_vector_type(8)));
typedef __bf16 bf16x2 __attribute__((ext_vector_type(2)));
#define GAS __attribute__((address_space(1)))
#define LAS __attribute__((address_space(3)))

#define STAMPS 0
#define LWIN 20
#define GWIN 40
#define QDELAY 8
#define GEARLY 4
#define GATE_PW 90
#define GATE_H 150
#define GATE_ACT 220
#define GATE_PD 270
#define PSTAG 6
#define PSLEEP 1
#define QS_T4 1
#define DYNROLE 1
#define OS_B 1

namespace {

constexpr int DM = 576, HKV = 16, SK = 4096, DH = 128, WIN = 1024, NQ = 4096, FF = 4608;
constexpr int NWG = 256, NTHR = 576;

constexpr int WS_EPOCH = 0;
constexpr int WS_RANK = 2048;
constexpr int WS_QS = 4096;
constexpr int WS_SC = WS_QS + 16 * 8 * 4 * 256 * 4;
constexpr int WS_OS = WS_SC + 256 * 128;
constexpr int WS_PW = WS_OS + 16 * 8 * 2 * 256 * 4;
constexpr int HREP = 8;
constexpr int WS_HS = WS_PW + 8 * 9 * 16 * 64 * 4;
constexpr int WS_AS = WS_HS + HREP * 8 * 576 * 4;
constexpr int WS_PD = WS_AS + 8 * 4608 * 4;
constexpr int WS_END = WS_PD + 8 * 9 * 16 * 64 * 4;
constexpr int WS_STAMP = (WS_END + 4095) / 4096 * 4096;
constexpr int AUX_SC1 = 16;
constexpr unsigned SPIN_CAP = 1u << 16, LSPIN_CAP = 1u << 22;

constexpr int L_EXT = 128 * 1024;
constexpr int L_FL = 144 * 1024;
constexpr int L_ST = L_FL + 128;
constexpr int L_P = L_ST + 256;
constexpr int L_QSUM = L_P + 2048;
constexpr int L_Q = L_QSUM + 8192;
constexpr int L_HN = L_P;
constexpr int HN_STRIDE = 292;
constexpr int RS = 68;
constexpr int L_RED_W = 112 * 1024;
constexpr int LDS_BYTES = 160 * 1024;
static_assert(L_Q + 512 <= LDS_BYTES && L_HN + 8 * HN_STRIDE * 4 <= LDS_BYTES, "LDS");
enum { F_LANDED = 0, F_QPUB = 1, F_BAR = 2, F_QRDY = 3, F_RANK = 4, F_FAC = 5, F_KDONE = 8, F_VDONE = 16 };

struct Params {
  const u16 *x, *kc, *vc, *wq, *wo, *wr, *wg, *wu, *wd;
  u16* out;
  unsigned char* ws;
};

__device__ __forceinline__ float bflo(u32 d) { return __uint_as_float(d << 16); }
__device__ __forceinline__ float bfhi(u32 d) { return __uint_as_float(d & 0xFFFF0000u); }
__device__ __forceinline__ float rbf(float f) { return (float)(__bf16)f; }
__device__ __forceinline__ u32 bfbits_hi(float f) { return __float_as_uint(rbf(f)); }
__device__ __forceinline__ u32 pack2(float lo, float hi) {
  bf16x2 v = {(__bf16)lo, (__bf16)hi};
  return __builtin_bit_cast(u32, v);
}
template <int CTRL> __device__ __forceinline__ float dpp(float x) {
  return __uint_as_float((u32)__builtin_amdgcn_mov_dpp((int)__float_as_uint(x), CTRL, 0xf, 0xf, true));
}
constexpr int XOR1 = 0xB1, XOR2 = 0x4E, XOR7 = 0x141, XOR8 = 0x128;
__device__ __forceinline__ float swap32_add(float a, float b) {
  auto r = __builtin_amdgcn_permlane32_swap(__float_as_uint(a), __float_as_uint(b), false, false);
  return __uint_as_float(r[0]) + __uint_as_float(r[1]);
}
__device__ __forceinline__ float swap16_add(float a, float b) {
  auto r = __builtin_amdgcn_permlane16_swap(__float_as_uint(a), __float_as_uint(b), false, false);
  return __uint_as_float(r[0]) + __uint_as_float(r[1]);
}
__device__ __forceinline__ float wave_sum(float x) {
  x += dpp<XOR1>(x); x += dpp<XOR2>(x); x += dpp<XOR7>(x); x += dpp<XOR8>(x);
  x = swap16_add(x, x); x = swap32_add(x, x);
  return x;
}
__device__ __forceinline__ float fmax_(float a, float b) { return __builtin_amdgcn_fmed3f(a, b, INFINITY); }
__device__ __forceinline__ float wave_max(float x) {
  x = fmax_(x, dpp<XOR1>(x)); x = fmax_(x, dpp<XOR2>(x)); x = fmax_(x, dpp<XOR7>(x)); x = fmax_(x, dpp<XOR8>(x));
  auto r = __builtin_amdgcn_permlane16_swap(__float_as_uint(x), __float_as_uint(x), false, false);
  x = fmax_(__uint_as_float(r[0]), __uint_as_float(r[1]));
  auto t = __builtin_amdgcn_permlane32_swap(__float_as_uint(x), __float_as_uint(x), false, false);
  return fmax_(__uint_as_float(t[0]), __uint_as_float(t[1]));
}
__device__ __forceinline__ f32x4 mfma(u32x4 a, u32x4 b, f32x4 c) {
  return __builtin_amdgcn_mfma_f32_16x16x32_bf16(__builtin_bit_cast(bf16x8, a), __builtin_bit_cast(bf16x8, b), c, 0, 0, 0);
}
#define SB() __builtin_amdgcn_sched_barrier(0)
template <int N> __device__ __forceinline__ void waitvm() { asm volatile("s_waitcnt vmcnt(%0)" ::"n"(N) : "memory"); }
typedef __amdgpu_buffer_rsrc_t rsrc_t;
__device__ __forceinline__ u32x4 ld16c(rsrc_t rs, int off) { return __builtin_amdgcn_raw_buffer_load_b128(rs, off, 0, AUX_SC1); }
__device__ __forceinline__ u32 ld4c(rsrc_t rs, int off) { return __builtin_amdgcn_raw_buffer_load_b32(rs, off, 0, AUX_SC1); }
__device__ __forceinline__ void st16c(rsrc_t rs, int off, u32x4 v) { __builtin_amdgcn_raw_buffer_store_b128(v, rs, off, 0, AUX_SC1); }
template <u32 M> __device__ __forceinline__ bool tag_ok(u32x4 v, u32 t) { return ((((v.x ^ t) | (v.y ^ t)) | ((v.z ^ t) | (v.w ^ t))) & M) == 0u; }
template <u32 M> __device__ __forceinline__ u32 put_tag(float f, u32 t) { return ((__float_as_uint(f) + (M + 1) / 2) & ~M) | t; }
template <u32 M> __device__ __forceinline__ float get_tag(u32 d) { return __uint_as_float(d & ~M); }
#define POLL_COUNT(n)
#define POLL(cond_load) do { unsigned spins_ = 0; _Pragma("clang loop unroll(disable)") while (!(cond_load) && ++spins_ < SPIN_CAP) __builtin_amdgcn_s_sleep(PSLEEP); POLL_COUNT(spins_); } while (0)

#define PV_CLOB "v128","v129","v130","v131","v132","v133","v134","v135","v136","v137","v138","v139","v140","v141","v142","v143", \
                "v144","v145","v146","v147","v148","v149","v150","v151","v152","v153","v154","v155","v156","v157","v158","v159","v160","v161","v162","v163", "vcc", "scc", "memory"
#define PV_LD4(r0, r1, r2, r3, r4, r5, r6, r7) \
  "buffer_load_dwordx4 v[" r0 ":" r1 "], %[o0], %[rs], 0 offen sc1\n\t" "buffer_load_dwordx4 v[" r2 ":" r3 "], %[o1], %[rs], 0 offen sc1\n\t" \
  "buffer_load_dwordx4 v[" r4 ":" r5 "], %[o2], %[rs], 0 offen sc1\n\t" "buffer_load_dwordx4 v[" r6 ":" r7 "], %[o3], %[rs], 0 offen sc1\n\t"
#define PV_X4(t, a, b, c, d) "v_xor_b32 " t ", %[T], v" a "\n\t" "v_bitop3_b32 " t ", " t ", v" b ", %[T] bitop3:0xf6\n\t" "v_bitop3_b32 " t ", " t ", v" c ", %[T] bitop3:0xf6\n\t" "v_bitop3_b32 " t ", " t ", v" d ", %[T] bitop3:0xf6\n\t"
#define PV_TEST "v_and_b32 v160, %[M], v160\n\tv_cmp_ne_u32 vcc, 0, v160\n\ts_cmp_eq_u64 vcc, 0\n\t"
#define PV_MOV2(o, a, b) "v_mov_b64 " o ", v[" a ":" b "]\n\t"
__device__ __forceinline__ void poll4(rsrc_t rs, int o0, int o1, int o2, int o3, u32 T, u32 M, u32x4 (&R)[4]) {
  u32x2 r0, r1, r2, r3, r4, r5, r6, r7; u32 cnt = SPIN_CAP;
  asm volatile(
      PV_LD4("128", "131", "132", "135", "136", "139", "140", "143")
      "s_sleep %[stag]\n"
      "1:\n\t"
      PV_LD4("144", "147", "148", "151", "152", "155", "156", "159")
      "s_waitcnt vmcnt(4)\n\t"
      PV_X4("v160", "128", "129", "130", "131") PV_X4("v161", "132", "133", "134", "135") PV_X4("v162", "136", "137", "138", "139") PV_X4("v163", "140", "141", "142", "143")
      "v_or3_b32 v160, v160, v161, v162\n\tv_or_b32 v160, v160, v163\n\t" PV_TEST
      "s_cbranch_scc1 7f\n\t"
      PV_LD4("128", "131", "132", "135", "136", "139", "140", "143")
      "s_waitcnt vmcnt(4)\n\t"
      PV_X4("v160", "144", "145", "146", "147") PV_X4("v161", "148", "149", "150", "151") PV_X4("v162", "152", "153", "154", "155") PV_X4("v163", "156", "157", "158", "159")
      "v_or3_b32 v160, v160, v161, v162\n\tv_or_b32 v160, v160, v163\n\t" PV_TEST
      "s_cbranch_scc1 8f\n\t"
      "s_sub_u32 %[cnt], %[cnt], 1\n\ts_cbranch_scc0 1b\n\t"
      "s_waitcnt vmcnt(0)\n"
      "7:\n\t"
      PV_MOV2("%[r0]", "128", "129") PV_MOV2("%[r1]", "130", "131") PV_MOV2("%[r2]", "132", "133") PV_MOV2("%[r3]", "134", "135")
      PV_MOV2("%[r4]", "136", "137") PV_MOV2("%[r5]", "138", "139") PV_MOV2("%[r6]", "140", "141") PV_MOV2("%[r7]", "142", "143")
      "s_branch 9f\n"
      "8:\n\t"
      PV_MOV2("%[r0]", "144", "145") PV_MOV2("%[r1]", "146", "147") PV_MOV2("%[r2]", "148", "149") PV_MOV2("%[r3]", "150", "151")
      PV_MOV2("%[r4]", "152", "153") PV_MOV2("%[r5]", "154", "155") PV_MOV2("%[r6]", "156", "157") PV_MOV2("%[r7]", "158", "159")
      "9:\n\t"
      : [r0] "=&v"(r0), [r1] "=&v"(r1), [r2] "=&v"(r2), [r3] "=&v"(r3), [r4] "=&v"(r4), [r5] "=&v"(r5), [r6] "=&v"(r6), [r7] "=&v"(r7), [cnt] "+s"(cnt)
      : [o0] "v"(o0), [o1] "v"(o1), [o2] "v"(o2), [o3] "v"(o3), [rs] "s"(rs), [T] "s"(T), [M] "s"(M), [stag] "n"(PSTAG)
      : PV_CLOB);
  R[0] = u32x4{r0.x, r0.y, r1.x, r1.y}; R[1] = u32x4{r2.x, r2.y, r3.x, r3.y}; R[2] = u32x4{r4.x, r4.y, r5.x, r5.y}; R[3] = u32x4{r6.x, r6.y, r7.x, r7.y};
}
#define PV_LD2(r0, r1, r2, r3) \
  "buffer_load_dwordx4 v[" r0 ":" r1 "], %[o0], %[rs], 0 offen sc1\n\t" "buffer_load_dwordx4 v[" r2 ":" r3 "], %[o0], %[rs], 0 offen offset:16 sc1\n\t"
__device__ __forceinline__ void poll2(rsrc_t rs, int o0, u32 T, u32 M, u32x4& a, u32x4& b) {
  u32x2 r0, r1, r2, r3; u32 cnt = SPIN_CAP;
  asm volatile(
      PV_LD2("128", "131", "132", "135")
      "s_sleep %[stag]\n"
      "1:\n\t"
      PV_LD2("144", "147", "148", "151")
      "s_waitcnt vmcnt(2)\n\t"
      PV_X4("v160", "128", "129", "130", "131") PV_X4("v161", "132", "133", "134", "135") "v_or_b32 v160, v160, v161\n\t" PV_TEST
      "s_cbranch_scc1 7f\n\t"
      PV_LD2("128", "131", "132", "135")
      "s_waitcnt vmcnt(2)\n\t"
      PV_X4("v160", "144", "145", "146", "147") PV_X4("v161", "148", "149", "150", "151") "v_or_b32 v160, v160, v161\n\t" PV_TEST
      "s_cbranch_scc1 8f\n\t"
      "s_sub_u32 %[cnt], %[cnt], 1\n\ts_cbranch_scc0 1b\n\t"
      "s_waitcnt vmcnt(0)\n"
      "7:\n\t"
      PV_MOV2("%[r0]", "128", "129") PV_MOV2("%[r1]", "130", "131") PV_MOV2("%[r2]", "132", "133") PV_MOV2("%[r3]", "134", "135")
      "s_branch 9f\n"
      "8:\n\t"
      PV_MOV2("%[r0]", "144", "145") PV_MOV2("%[r1]", "146", "147") PV_MOV2("%[r2]", "148", "149") PV_MOV2("%[r3]", "150", "151")
      "9:\n\t"
      : [r0] "=&v"(r0), [r1] "=&v"(r1), [r2] "=&v"(r2), [r3] "=&v"(r3), [cnt] "+s"(cnt)
      : [o0] "v"(o0), [rs] "s"(rs), [T] "s"(T), [M] "s"(M), [stag] "n"(PSTAG)
      : PV_CLOB);
  a = u32x4{r0.x, r0.y, r1.x, r1.y}; b = u32x4{r2.x, r2.y, r3.x, r3.y};
}
#define PV_LD3(r0, r1, r2, r3, r4) \
  "buffer_load_dwordx4 v[" r0 ":" r1 "], %[o0], %[rs], 0 offen sc1\n\t" "buffer_load_dwordx4 v[" r2 ":" r3 "], %[o0], %[rs], 0 offen offset:16 sc1\n\t" \
  "buffer_load_dword v" r4 ", %[o1], %[rs], 0 offen sc1\n\t"
__device__ __forceinline__ void poll3(rsrc_t rs, int o0, int o1, u32 T, u32 M, u32x4& a, u32x4& b, u32& c) {
  u32x2 r0, r1, r2, r3; u32 r4, cnt = SPIN_CAP;
  asm volatile(
      PV_LD3("128", "131", "132", "135", "136")
      "s_sleep %[stag]\n"
      "1:\n\t"
      PV_LD3("144", "147", "148", "151", "152")
      "s_waitcnt vmcnt(3)\n\t"
      PV_X4("v160", "128", "129", "130", "131") PV_X4("v161", "132", "133", "134", "135") "v_xor_b32 v162, %[T], v136\n\t"
      "v_or3_b32 v160, v160, v161, v162\n\t" PV_TEST
      "s_cbranch_scc1 7f\n\t"
      PV_LD3("128", "131", "132", "135", "136")
      "s_waitcnt vmcnt(3)\n\t"
      PV_X4("v160", "144", "145", "146", "147") PV_X4("v161", "148", "149", "150", "151") "v_xor_b32 v162, %[T], v152\n\t"
      "v_or3_b32 v160, v160, v161, v162\n\t" PV_TEST
      "s_cbranch_scc1 8f\n\t"
      "s_sub_u32 %[cnt], %[cnt], 1\n\ts_cbranch_scc0 1b\n\t"
      "s_waitcnt vmcnt(0)\n"
      "7:\n\t"
      PV_MOV2("%[r0]", "128", "129") PV_MOV2("%[r1]", "130", "131") PV_MOV2("%[r2]", "132", "133") PV_MOV2("%[r3]", "134", "135") "v_mov_b32 %[r4], v136\n\t"
      "s_branch 9f\n"
      "8:\n\t"
      PV_MOV2("%[r0]", "144", "145") PV_MOV2("%[r1]", "146", "147") PV_MOV2("%[r2]", "148", "149") PV_MOV2("%[r3]", "150", "151") "v_mov_b32 %[r4], v152\n\t"
      "9:\n\t"
      : [r0] "=&v"(r0), [r1] "=&v"(r1), [r2] "=&v"(r2), [r3] "=&v"(r3), [r4] "=&v"(r4), [cnt] "+s"(cnt)
      : [o0] "v"(o0), [o1] "v"(o1), [rs] "s"(rs), [T] "s"(T), [M] "s"(M), [stag] "n"(PSTAG)
      : PV_CLOB);
  a = u32x4{r0.x, r0.y, r1.x, r1.y}; b = u32x4{r2.x, r2.y, r3.x, r3.y}; c = r4;
}

__device__ __forceinline__ unsigned long long now() { return __builtin_amdgcn_s_memrealtime(); }
__device__ __forceinline__ void not_before(unsigned long long t0, unsigned ticks) {
  while (now() - t0 < ticks) __builtin_amdgcn_s_sleep(2);
}
__device__ __forceinline__ u32 lds_ld(u32 addr) {
  u32 v;
  asm volatile("ds_read_b32 %0, %1\n\ts_waitcnt lgkmcnt(0)" : "=v"(v) : "v"(addr) : "memory");
  return v;
}
__device__ __forceinline__ void lds_st(u32 addr, u32 v) { asm volatile("ds_write_b32 %0, %1" ::"v"(addr), "v"(v) : "memory"); }
__device__ __forceinline__ void wait_word(int word, u32 want) {
  unsigned spins = 0;
#pragma clang loop unroll(disable)
  while ((u32)__builtin_amdgcn_readfirstlane(lds_ld(L_FL + 4 * word)) < want)
    if (++spins > LSPIN_CAP) break;
}
__device__ __forceinline__ void wait_word_sleep(int word, u32 want) {
  unsigned spins = 0;
#pragma clang loop unroll(disable)
  while ((u32)__builtin_amdgcn_readfirstlane(lds_ld(L_FL + 4 * word)) < want) {
    __builtin_amdgcn_s_sleep(2);
    if (++spins > LSPIN_CAP) break;
  }
}
__device__ __forceinline__ void wait_all8(int word0, u32 want, int lane) {
  unsigned spins = 0;
#pragma clang loop unroll(disable)
  while (!__all(lds_ld(L_FL + 4 * (word0 + (lane & 7))) >= want)) {
    __builtin_amdgcn_s_sleep(1);
    if (++spins > LSPIN_CAP) break;
  }
}
__device__ __forceinline__ void cbar(u32 n, int lane) {
  asm volatile("s_waitcnt lgkmcnt(0)" ::: "memory");
  if (lane == 0) asm volatile("ds_add_u32 %0, %1" ::"v"((u32)(L_FL + 4 * F_BAR)), "v"(1u) : "memory");
  wait_word(F_BAR, 8u * n);
}
__device__ __forceinline__ void hw_barrier() { __builtin_amdgcn_sched_barrier(0); __builtin_amdgcn_s_barrier(); __builtin_amdgcn_sched_barrier(0); }
__device__ __forceinline__ void wg_barrier() {
  asm volatile("s_waitcnt lgkmcnt(0)" ::: "memory");
  hw_barrier();
  asm volatile("" ::: "memory");
}
__device__ __forceinline__ void glds16(const void* sbase, u32 voff, u32 lds_dst) {
  asm volatile("s_mov_b32 m0, %2\n\ts_nop 0\n\tglobal_load_lds_dwordx4 %0, %1 nt" ::"v"(voff), "s"(sbase), "s"(lds_dst) : "memory");
}
__device__ __forceinline__ int vswz(int row) { return ((row & 3) << 2) | ((row >> 2) & 3); }
__device__ __forceinline__ int swz(int row) { return ((row >> 1) & 1) | ((row >> 2) & 2); }
__device__ __forceinline__ u32 rowoff(int q, int stride, int lane) { const int row = 8 * q + (lane >> 3); return row * stride + (((lane & 7) ^ (swz(row) << 1)) & 7) * 16; }
template <int W> __device__ __forceinline__ void tile_dma(const unsigned char* a, int stride, u32 o0, u32 o1, u32 dst) {
  glds16(a, o0, dst); if (W >= 0) waitvm<W>();
  glds16(a, o1, dst + 1024); if (W >= 0) waitvm<W>();
  glds16(a + (size_t)16 * stride, o0, dst + 2048); if (W >= 0) waitvm<W>();
  glds16(a + (size_t)16 * stride, o1, dst + 3072); if (W >= 0) waitvm<W>();
}
__device__ __forceinline__ void tile_frags_lds(const unsigned char* lds, u32 base, int lane, u32x4 (&f)[4]) {
  const int g = lane >> 4, q = (lane & 15) >> 2, p = lane & 3;
#pragma unroll
  for (int e = 0; e < 4; ++e) {
    u32x2 h[2];
#pragma unroll
    for (int t = 0; t < 2; ++t) {
      const int row = 8 * g + 4 * t + q;
      const u32 a = base + row * 128 + (((2 * e + (p >> 1)) ^ (swz(row) << 1)) << 4) + 8 * (p & 1);
      h[t] = __builtin_bit_cast(u32x2, __builtin_amdgcn_ds_read_tr16_b64_v4i16((LAS s16x4*)(lds + a)));
    }
    f[e] = u32x4{h[0].x, h[0].y, h[1].x, h[1].y};
  }
}

#define ESTAMP(k) do { } while (0)
#define STAMP(k) do { } while (0)
#define STAMP_FLUSH() do { } while (0)

__device__ __forceinline__ u32x4 afrag(u32x4 a, u32x4 b) {
  u32x4 f;
  f.x = __builtin_amdgcn_perm(a.y, a.x, 0x07060302u);
  f.y = __builtin_amdgcn_perm(a.w, a.z, 0x07060302u);
  f.z = __builtin_amdgcn_perm(b.y, b.x, 0x07060302u);
  f.w = __builtin_amdgcn_perm(b.w, b.z, 0x07060302u);
  return f;
}

struct RowStats { float m, rs; };
template <int N> __device__ __forceinline__ RowStats row_stats(const float (&v)[N], const bool (&valid)[N], float pivot) {
  float s1[2] = {0.f, 0.f}, s2[2] = {0.f, 0.f};
#pragma unroll
  for (int e = 0; e < N; ++e) { const float d = valid[e] ? v[e] - pivot : 0.f; s1[e & 1] += d; s2[e & 1] = fmaf(d, d, s2[e & 1]); }
  const float m1 = wave_sum(s1[0] + s1[1]) * (1.0f / DM), m2 = wave_sum(s2[0] + s2[1]) * (1.0f / DM);
  const float var = rbf(fmax_(fmaf(-m1, m1, m2), 0.f));
  RowStats r;
  r.m = rbf(pivot + m1);
  r.rs = __builtin_amdgcn_rcpf(rbf(__builtin_amdgcn_sqrtf(rbf(var + 1e-5f))));
  return r;
}
__device__ __forceinline__ float ln_apply(float v, RowStats s) { return rbf(v - s.m) * s.rs; }

enum { ROLE_W = 0, ROLE_G = 1, ROLE_F = 2 };

__global__ void __launch_bounds__(NTHR, 3) fused_kernel(Params p) {
  extern __shared__ __attribute__((aligned(16))) unsigned char lds[];
  const int tid = threadIdx.x, lane = tid & 63, w = __builtin_amdgcn_readfirstlane(tid >> 6);
  const int wg = blockIdx.x, xcd = wg & 7, s = wg >> 3, j = xcd + 8 * (s >> 4), i = s & 15, b = i >> 1, half = i & 1;
  const size_t ubase = ((size_t)(b * HKV + j) * SK + (SK - WIN)) * DH;
  const int cq = i & 3, kp = i >> 2;
  const int ks0 = kp < 3 ? 4 * kp : 13, nt = kp < 2 ? 4 : 5;
  const u32 PK = 4 * nt, PV = PK + 128, PW = PK + 256;
  const int g4 = lane >> 4, c16 = lane & 15;
#define RSTAMP(k)
#define PMARK(n)
  STAMP(0);
  const int cw = (w + 7) & 7;
  u32 xr[5];
  u32x4 xa;
  {
    const GAS u32* xrow = (const GAS u32*)(p.x + cw * DM);
#pragma unroll
    for (int m = 0; m < 4; ++m) xr[m] = __builtin_nontemporal_load(xrow + lane + 64 * m);
    xr[4] = __builtin_nontemporal_load(xrow + (lane & 31) + 256);
    xa = __builtin_nontemporal_load((const GAS u32x4*)(p.x + (lane & 7) * DM + 32 * (ks0 + (cw < nt ? cw : 0)) + 8 * g4));
  }
  if (w == 0) {
    if (lane < 32) lds_st(L_FL + 4 * lane, 0u);
    asm volatile("s_waitcnt lgkmcnt(0)" ::: "memory");
  }
  hw_barrier();

  if (w == 0) {
    __builtin_amdgcn_s_setprio(3);
    u32 pub = 0;
#define PUBLISH(W) lds_st(L_FL + 4 * F_LANDED, pub - (W))
    {
      const unsigned char* a = (const unsigned char*)(p.wq + (size_t)32 * ks0 * NQ + 256 * j + 64 * cq);
      const u32 o0 = rowoff(0, NQ * 2, lane), o1 = rowoff(1, NQ * 2, lane);
#pragma unroll
      for (int t = 0; t < 4; ++t) tile_dma<-1>(a + (size_t)t * 32 * NQ * 2, NQ * 2, o0, o1, L_EXT + t * 4096);
      if (nt == 5) tile_dma<-1>(a + (size_t)4 * 32 * NQ * 2, NQ * 2, o0, o1, L_QSUM);
      pub = PK;
    }
    ESTAMP(8);
    {
      const unsigned char* kb = (const unsigned char*)(p.kc + ubase + (size_t)half * 512 * DH);
      u32 koff[4];
#pragma unroll
      for (int q = 0; q < 4; ++q) { const int key = 4 * q + g4; koff[q] = key * 256 + ((c16 ^ key) & 15) * 16; }
#pragma clang loop unroll(disable)
      for (int t = 0; t < 32; ++t) {
#pragma unroll
        for (int q = 0; q < 4; ++q) { glds16(kb + t * 4096, koff[q], (t * 4 + q) * 1024); waitvm<LWIN>(); }
        pub += 4;
        if (pub > LWIN) PUBLISH(LWIN);
      }
    }
    STAMP(1);
    wait_word_sleep(F_RANK, 1u);
    const int rank = (int)__builtin_amdgcn_readfirstlane(lds_ld(L_FL + 4 * F_RANK)) - 1;
    const int role = rank < 72 ? ROLE_G : (rank < 216 ? ROLE_W : ROLE_F);
    const u32 go0 = rowoff(0, FF * 2, lane), go1 = rowoff(1, FF * 2, lane);
    auto gtile = [&](int t) -> const unsigned char* { return (const unsigned char*)(((t & 1) ? p.wu : p.wg) + (size_t)(t >> 1) * 32 * FF + 64 * rank); };
    {
      const unsigned char* vb = (const unsigned char*)(p.vc + ubase + (size_t)half * 512 * DH);
      u32 voff[4];
#pragma unroll
      for (int q = 0; q < 4; ++q) { const int key = 4 * q + g4; voff[q] = key * 256 + ((c16 ^ vswz(key)) & 15) * 16; }
      wait_word(F_QPUB, 1u);
#pragma clang loop unroll(disable)
      for (int t = 0; t < 32; ++t) {
        if ((t & 7) == 4) wait_all8(F_KDONE, (t >> 3) + 1, lane);
        const u32 dst = t < 4 ? L_EXT + t * 4096 : (t - 4) * 4096;
#pragma unroll
        for (int q = 0; q < 4; ++q) { glds16(vb + t * 4096, voff[q], dst + q * 1024); waitvm<LWIN>(); }
        pub += 4; PUBLISH(LWIN);
        if (role == ROLE_G && t >= 32 - GEARLY) {
          const int tau = t - (32 - GEARLY);
          if (tau == 0) wait_all8(F_VDONE, 1, lane);
          tile_dma<LWIN>(gtile(tau), FF * 2, go0, go1, tau < 4 ? L_EXT + tau * 4096 : (tau - 4) * 4096);
          pub += 4; PUBLISH(LWIN);
        }
      }
    }
    STAMP(2);
    if (role == ROLE_W) {
      const int jw = (rank - 72) / 9, cg = (rank - 72) % 9;
      const unsigned char* a = (const unsigned char*)(p.wo + (size_t)256 * jw * DM + 64 * cg);
      const u32 o0 = rowoff(0, DM * 2, lane), o1 = rowoff(1, DM * 2, lane);
      wait_all8(F_VDONE, 1, lane);
#pragma clang loop unroll(disable)
      for (int t = 0; t < 8; ++t) { tile_dma<-1>(a + (size_t)t * 32 * DM * 2, DM * 2, o0, o1, t * 4096); pub += 4; }
    } else if (role == ROLE_G) {
      static_assert(GEARLY >= 1 && 80 - GWIN >= 4 * GEARLY - 4, "the last tile of V is published before the loader waits for its consumption");
#pragma clang loop unroll(disable)
      for (int n = GEARLY; n < 36; ++n) {
        if (n == 20) wait_all8(F_VDONE, 2, lane);
        const int t = n < 16 ? n : (n < 20 ? n + 16 : n - 4);
        tile_dma<GWIN>(gtile(t), FF * 2, go0, go1, t < 4 ? L_EXT + t * 4096 : (t - 4) * 4096);
        pub += 4;
        if (4 * (n - GEARLY) + 4 > GWIN - LWIN) PUBLISH(GWIN);
      }
    }
    STAMP(3);
#define DR(N) if ((role == ROLE_G ? GWIN : (role == ROLE_W ? LWIN + 32 : LWIN)) > N) { waitvm<N>(); PUBLISH(N); }
    static_assert(LWIN + 32 <= 60 && GWIN <= 60, "vmcnt");
    DR(56) DR(52) DR(48) DR(44) DR(40) DR(36) DR(32) DR(28) DR(24) DR(20) DR(16) DR(12) DR(8) DR(4) DR(0)
#undef DR
#undef PUBLISH
    STAMP(14);
    STAMP_FLUSH();
    return;
  }

  const int ct = tid - 64;
  const rsrc_t rs = __builtin_amdgcn_make_buffer_rsrc((void*)p.ws, 0, WS_END, 0x00020000);
  const u32 T = *(const __attribute__((address_space(4))) u32*)(p.ws + WS_EPOCH) + 1u;
  const u32 T16 = T & 0xFFFFu, T8 = T & 0xFFu, T4 = T & 0xFu;
  const u32 tstart = (u32)now();
  if (ct == 448) __builtin_amdgcn_raw_buffer_store_b64(u32x2{tstart, T}, rs, WS_RANK + wg * 8, 0, AUX_SC1);
  float* const lf = (float*)lds;
  u32* const lu = (u32*)lds;
  {
    ESTAMP(8);
    asm volatile("" :: "v"(xr[0]), "v"(xr[4]));
    ESTAMP(9);
    float v[10]; bool valid[10];
#pragma unroll
    for (int m = 0; m < 5; ++m) { valid[2 * m] = valid[2 * m + 1] = (m < 4) || (lane < 32); v[2 * m] = bflo(xr[m]); v[2 * m + 1] = bfhi(xr[m]); }
    const float pivot = __uint_as_float(__builtin_amdgcn_readfirstlane(xr[0]) << 16);
    const RowStats st = row_stats<10>(v, valid, pivot);
    if (lane == 0) *(f32x2*)(lf + L_ST / 4 + 2 * cw) = f32x2{st.m, st.rs};
    ESTAMP(10);
    cbar(1, lane);
    STAMP(1);
    PMARK(1);
  }
  if (cw < nt) {
    const f32x2 sv = *(const f32x2*)(lf + L_ST / 4 + 2 * (lane & 7));
    const RowStats st = {sv.x, sv.y};
    u32x4 a;
#pragma unroll
    for (int q = 0; q < 4; ++q) a[q] = pack2(ln_apply(bflo(xa[q]), st), ln_apply(bfhi(xa[q]), st));
    const u32 base = cw < 4 ? L_EXT + cw * 4096 : L_QSUM;
    wait_word(F_LANDED, 4 * (cw + 1));
    u32x4 f[4]; tile_frags_lds(lds, base, lane, f);
    f32x4 d[4];
#pragma unroll
    for (int e = 0; e < 4; ++e) d[e] = mfma(f[e], a, f32x4{0.f, 0.f, 0.f, 0.f});
    if (c16 < 8) {
#pragma unroll
      for (int e = 0; e < 4; ++e) *(f32x4*)(lf + base / 4 + c16 * RS + 16 * e + 4 * g4) = d[e];
    }
  }
  ESTAMP(11);
  cbar(2, lane);
  ESTAMP(12);
  {
    float sum = (lf[L_EXT / 4 + cw * RS + lane] + lf[L_EXT / 4 + 1024 + cw * RS + lane]) + (lf[L_EXT / 4 + 2048 + cw * RS + lane] + lf[L_EXT / 4 + 3072 + cw * RS + lane]);
    if (nt == 5) sum += lf[L_QSUM / 4 + cw * RS + lane];
    const u32 v = put_tag<0xFu>(sum, T4);
    const u32x4 o = {v, (u32)__builtin_amdgcn_mov_dpp((int)v, 0x55, 0xf, 0xf, true), (u32)__builtin_amdgcn_mov_dpp((int)v, 0xAA, 0xf, 0xf, true),
                     (u32)__builtin_amdgcn_mov_dpp((int)v, 0xFF, 0xf, 0xf, true)};
    if ((lane & 3) == 0) st16c(rs, WS_QS + ((((j * 8 + cw) * 4 + kp) * 256) + 64 * cq + lane) * 4, o);
  }
  cbar(3, lane);
  if (ct == 0) lds_st(L_FL + 4 * F_QPUB, 1u);
  STAMP(2);
  PMARK(2);
  if (cw >= 2 && cw < 6) {
    u32x4 qg;
    __builtin_amdgcn_s_sleep(QDELAY);
    {
      unsigned spins = 0;
#pragma clang loop unroll(disable)
      for (;;) {
        qg = ld16c(rs, WS_QS + (((j * 8 + b) * 4 + (cw - 2)) * 256 + 4 * lane) * 4);
        if (__all(tag_ok<0xFu>(qg, T4)) || ++spins > SPIN_CAP) break;
        __builtin_amdgcn_s_sleep(4);
      }
    }
    *(f32x4*)(lf + L_QSUM / 4 + (cw - 2) * 256 + 4 * lane) = f32x4{get_tag<0xFu>(qg.x), get_tag<0xFu>(qg.y), get_tag<0xFu>(qg.z), get_tag<0xFu>(qg.w)};
  }
  if (cw == 7) {
    u32x4 e0, e1; unsigned spins = 0;
#pragma clang loop unroll(disable)
    for (;;) {
      e0 = ld16c(rs, WS_RANK + lane * 32); e1 = ld16c(rs, WS_RANK + lane * 32 + 16);
      if (__all((e0.y == T) & (e0.w == T) & (e1.y == T) & (e1.w == T)) || ++spins > SPIN_CAP) break;
      __builtin_amdgcn_s_sleep(4);
    }
    auto before = [&](u32 t, int g) -> float { const int d = (int)(t - tstart); return (d < 0 || (d == 0 && g < wg)) ? 1.f : 0.f; };
    const float n = wave_sum((before(e0.x, 4 * lane) + before(e0.z, 4 * lane + 1)) + (before(e1.x, 4 * lane + 2) + before(e1.z, 4 * lane + 3)));
    if (lane == 0) lds_st(L_FL + 4 * F_RANK, (u32)n + 1u);
  }
  cbar(4, lane);
  if (cw == 0) {
    f32x4 sv[4];
#pragma unroll
    for (int q = 0; q < 4; ++q) sv[q] = *(const f32x4*)(lf + L_QSUM / 4 + q * 256 + 4 * lane);
    const f32x4 sum = (sv[0] + sv[1]) + (sv[2] + sv[3]);
    *(u32x2*)(lu + L_Q / 4 + 2 * lane) = u32x2{pack2(sum.x, sum.y), pack2(sum.z, sum.w)};
    asm volatile("s_waitcnt lgkmcnt(0)" ::: "memory");
    if (lane == 0) lds_st(L_FL + 4 * F_QRDY, 1u);
  } else {
    wait_word(F_QRDY, 1u);
  }
  STAMP(3);
  PMARK(3);
  float s0, s1;
  {
    u32x4 qa[4];
#pragma unroll
    for (int ks = 0; ks < 4; ++ks) qa[ks] = *(const u32x4*)(lu + L_Q / 4 + (lane & 1) * 64 + 16 * ks + 4 * g4);
    f32x4 d[4];
#pragma unroll
    for (int m = 0; m < 4; ++m) {
      const int tile = cw + 8 * m;
      wait_word_sleep(F_LANDED, PK + 4 * (tile + 1));
      u32x4 kf[4];
#pragma unroll
      for (int ks = 0; ks < 4; ++ks) kf[ks] = *(const u32x4*)(lds + tile * 4096 + c16 * 256 + (((4 * ks + g4) ^ c16) << 4));
      f32x4 acc = mfma(qa[0], kf[0], f32x4{0.f, 0.f, 0.f, 0.f});
      acc = mfma(qa[1], kf[1], acc); acc = mfma(qa[2], kf[2], acc); acc = mfma(qa[3], kf[3], acc);
      d[m] = acc;
      asm volatile("s_waitcnt lgkmcnt(0)" ::: "memory");
      if (lane == 0) lds_st(L_FL + 4 * (F_KDONE + cw), m + 1);
    }
    const float x0 = g4 == 0 ? d[0][0] : g4 == 1 ? d[1][0] : g4 == 2 ? d[2][0] : d[3][0];
    const float x1 = g4 == 0 ? d[0][1] : g4 == 1 ? d[1][1] : g4 == 2 ? d[2][1] : d[3][1];
    constexpr float SCALE = 0.08838834764831845f;
    s0 = rbf(rbf(x0) * SCALE); s1 = rbf(rbf(x1) * SCALE);
  }
  STAMP(4);
  PMARK(4);
  u32x4 mine;
  {
    constexpr float L2E = 1.4426950408889634f;
    const float m0 = wave_max(s0), m1 = wave_max(s1);
    const float l0 = wave_sum(__builtin_amdgcn_exp2f((s0 - m0) * L2E)), l1 = wave_sum(__builtin_amdgcn_exp2f((s1 - m1) * L2E));
    if (lane == 0) *(f32x4*)(lf + L_ST / 4 + 16 + 4 * cw) = f32x4{m0, m1, l0, l1};
    cbar(5, lane);
    float M0 = -INFINITY, M1 = -INFINITY;
    f32x4 st[8];
#pragma unroll
    for (int ww = 0; ww < 8; ++ww) { st[ww] = *(const f32x4*)(lf + L_ST / 4 + 16 + 4 * ww); M0 = fmax_(M0, st[ww].x); M1 = fmax_(M1, st[ww].y); }
    float L0 = 0.f, L1 = 0.f;
#pragma unroll
    for (int ww = 0; ww < 8; ++ww) {
      L0 = fmaf(st[ww].z, __builtin_amdgcn_exp2f((st[ww].x - M0) * L2E), L0);
      L1 = fmaf(st[ww].w, __builtin_amdgcn_exp2f((st[ww].y - M1) * L2E), L1);
    }
    const int kl = 16 * (cw + 8 * g4) + c16;
    __bf16* P = (__bf16*)(lds + L_P);
    P[kl] = (__bf16)__builtin_amdgcn_exp2f((s0 - M0) * L2E);
    P[512 + kl] = (__bf16)__builtin_amdgcn_exp2f((s1 - M1) * L2E);
    mine = u32x4{__float_as_uint(M0) | T16, __float_as_uint(M1) | T16, put_tag<0xFFu>(L0, T8), put_tag<0xFFu>(L1, T8)};
    if (ct == 64) st16c(rs, WS_SC + wg * 128, mine);
  }
  cbar(6, lane);
  STAMP(6);
  wait_word(F_RANK, 1u);
  const int rank = (int)__builtin_amdgcn_readfirstlane(lds_ld(L_FL + 4 * F_RANK)) - 1;
  const int role = rank < 72 ? ROLE_G : (rank < 216 ? ROLE_W : ROLE_F);
  {
    const int q4 = (lane & 15) >> 2, p4 = lane & 3;
    auto vfrag = [&](u32 base, int e) -> u32x4 {
      u32x2 h[2];
#pragma unroll
      for (int t = 0; t < 2; ++t) {
        const int row = 8 * g4 + 4 * t + q4;
        const u32 a = base + row * 256 + (((2 * e + (p4 >> 1)) ^ vswz(row)) << 4) + 8 * (p4 & 1);
        h[t] = __builtin_bit_cast(u32x2, __builtin_amdgcn_ds_read_tr16_b64_v4i16((LAS s16x4*)(lds + a)));
      }
      return u32x4{h[0].x, h[0].y, h[1].x, h[1].y};
    };
    auto pfrag = [&](int ks) -> u32x4 { return *(const u32x4*)(lds + L_P + (lane & 1) * 1024 + (32 * ks + 8 * g4) * 2); };
    {
      const u32x4 pf = pfrag(cw);
      const u32 base = cw < 2 ? L_EXT + cw * 8192 : (cw - 2) * 8192;
      wait_word(F_LANDED, PV + 8 * (cw + 1));
      u32x4 f[8];
#pragma unroll
      for (int e = 0; e < 8; ++e) f[e] = vfrag(base, e);
      SB();
      f32x4 d[8];
#pragma unroll
      for (int e = 0; e < 8; ++e) d[e] = mfma(f[e], pf, f32x4{0.f, 0.f, 0.f, 0.f});
      asm volatile("s_waitcnt lgkmcnt(0)" ::: "memory");
      if (lane == 0) lds_st(L_FL + 4 * (F_VDONE + cw), 1u);
      if (c16 < 2) {
#pragma unroll
        for (int e = 0; e < 8; ++e) *(f32x4*)(lf + L_QSUM / 4 + cw * 256 + c16 * 128 + 16 * e + 4 * g4) = d[e];
      }
    }
    cbar(7, lane);
    f32x4 acc;
    {
      f32x4 sv[8];
#pragma unroll
      for (int ww = 0; ww < 8; ++ww) sv[ww] = *(const f32x4*)(lf + L_QSUM / 4 + ww * 256 + (lane & 1) * 128 + 16 * cw + 4 * g4);
      acc = ((sv[0] + sv[1]) + (sv[2] + sv[3])) + ((sv[4] + sv[5]) + (sv[6] + sv[7]));
    }
    u32x4 oth = {0u, 0u, 0u, 0u};
    bool got = cw != 1;
    if (cw == 1) oth = ld16c(rs, WS_SC + (wg ^ 8) * 128);
    auto try_stats = [&](bool last) {
      if (got) return;
      unsigned spins = 0;
#pragma clang loop unroll(disable)
      for (;;) {
        if (__all(((((oth.x ^ T16) | (oth.y ^ T16)) & 0xFFFFu) | (((oth.z ^ T8) | (oth.w ^ T8)) & 0xFFu)) == 0u) || ++spins > SPIN_CAP) { got = true; break; }
        oth = ld16c(rs, WS_SC + (wg ^ 8) * 128);
        if (!last) return;
      }
      constexpr float L2E = 1.4426950408889634f;
      const float M0 = bfhi(mine.x), M1 = bfhi(mine.y), om0 = bfhi(oth.x), om1 = bfhi(oth.y);
      const float G0 = fmax_(M0, om0), G1 = fmax_(M1, om1);
      const float e0 = __builtin_amdgcn_exp2f((M0 - G0) * L2E), e1 = __builtin_amdgcn_exp2f((M1 - G1) * L2E);
      const float S0 = get_tag<0xFFu>(mine.z) * e0 + get_tag<0xFFu>(oth.z) * __builtin_amdgcn_exp2f((om0 - G0) * L2E);
      const float S1 = get_tag<0xFFu>(mine.w) * e1 + get_tag<0xFFu>(oth.w) * __builtin_amdgcn_exp2f((om1 - G1) * L2E);
      if (lane == 0) { *(f32x2*)(lf + L_ST / 4 + 48) = f32x2{e0 / S0, e1 / S1}; asm volatile("s_waitcnt lgkmcnt(0)" ::: "memory"); lds_st(L_FL + 4 * F_FAC, 1u); }
      STAMP(5);
    };
#pragma unroll
    for (int ks = 8; ks < 16; ++ks) {
      const u32x4 pf = pfrag(ks);
      const int tl = 2 * ks + 1;
      wait_word(F_LANDED, PV + 4 * (tl + 1) + ((role == ROLE_G && tl > 32 - GEARLY) ? 4 * (tl - (32 - GEARLY)) : 0));
      acc = mfma(vfrag((ks - 2) * 8192, cw), pf, acc);
      if (ks >= 9 && ks <= 13) try_stats(false);
      if (ks == 14) { try_stats(true); wait_word(F_FAC, 1u); }
    }
    const float fac = lf[L_ST / 4 + 48 + (lane & 1)];
    asm volatile("s_waitcnt lgkmcnt(0)" ::: "memory");
    if (lane == 0) lds_st(L_FL + 4 * (F_VDONE + cw), 2u);
    if (c16 < 2)
      st16c(rs, WS_OS + (((j * 8 + b) * 2 + half) * 256 + c16 * 128 + 16 * cw + 4 * g4) * 4,
            u32x4{put_tag<0xFFu>(acc.x * fac, T8), put_tag<0xFFu>(acc.y * fac, T8), put_tag<0xFFu>(acc.z * fac, T8), put_tag<0xFFu>(acc.w * fac, T8)});
  }
  auto gather4 = [&](u32 v) -> u32x4 {
    return u32x4{v, (u32)__builtin_amdgcn_mov_dpp((int)v, 0x55, 0xf, 0xf, true), (u32)__builtin_amdgcn_mov_dpp((int)v, 0xAA, 0xf, 0xf, true),
                 (u32)__builtin_amdgcn_mov_dpp((int)v, 0xFF, 0xf, 0xf, true)};
  };
  const unsigned long long to = now();
  STAMP(7);
  PMARK(7);

  auto acc_to_lds = [&](int base, const f32x4 (&d)[4]) {
    if (c16 < 8) {
#pragma unroll
      for (int e = 0; e < 4; ++e) *(f32x4*)(lf + base / 4 + (cw * 8 + c16) * RS + 16 * e + 4 * g4) = d[e];
    }
  };
  auto publish_partial = [&](int base, int jw, int cg) {
    float sv[8];
#pragma unroll
    for (int ww = 0; ww < 8; ++ww) sv[ww] = lf[L_RED_W / 4 + (ww * 8 + cw) * RS + lane];
    const float sum = ((sv[0] + sv[1]) + (sv[2] + sv[3])) + ((sv[4] + sv[5]) + (sv[6] + sv[7]));
    const u32x4 o = gather4(put_tag<0xFFu>(sum, T8));
    if ((lane & 3) == 0) st16c(rs, base + (((cw * 9 + cg) * 16 + jw) * 64 + lane) * 4, o);
  };

  if (role == ROLE_W) {
    const int jw = (rank - 72) / 9, cg = (rank - 72) % 9;
    u32x4 f[4];
    wait_word(F_LANDED, PW + 32);
    tile_frags_lds(lds, cw * 4096, lane, f);
    {
      const int off = WS_OS + ((jw * 8 + (lane & 7)) * 512 + 32 * cw + 8 * g4) * 4;
      u32x4 a0 = {0u, 0u, 0u, 0u}, a1 = a0, b0 = a0, b1 = a0;
      if (c16 < 8)
        POLL((a0 = ld16c(rs, off), a1 = ld16c(rs, off + 16), b0 = ld16c(rs, off + 1024), b1 = ld16c(rs, off + 1040),
              tag_ok<0xFFu>(a0, T8) & tag_ok<0xFFu>(a1, T8) & tag_ok<0xFFu>(b0, T8) & tag_ok<0xFFu>(b1, T8)));
      STAMP(8);
      RSTAMP(0);
#define S2(x, y) (get_tag<0xFFu>(x) + get_tag<0xFFu>(y))
      const u32x4 a = {pack2(S2(a0.x, b0.x), S2(a0.y, b0.y)), pack2(S2(a0.z, b0.z), S2(a0.w, b0.w)), pack2(S2(a1.x, b1.x), S2(a1.y, b1.y)), pack2(S2(a1.z, b1.z), S2(a1.w, b1.w))};
#undef S2
      f32x4 d[4];
#pragma unroll
      for (int e = 0; e < 4; ++e) d[e] = mfma(f[e], a, f32x4{0.f, 0.f, 0.f, 0.f});
      acc_to_lds(L_RED_W, d);
    }
    wg_barrier();
    publish_partial(WS_PW, jw, cg);
    const unsigned long long tw = now();
    STAMP(9);
    u32x4 f2[4];
    {
      const unsigned char* a = (const unsigned char*)(p.wd + (size_t)(288 * jw + 32 * cw) * DM + 64 * cg);
      const u32 o0 = rowoff(0, DM * 2, lane), o1 = rowoff(1, DM * 2, lane);
      tile_dma<-1>(a, DM * 2, o0, o1, (8 + cw) * 4096);
      if (cw == 0) tile_dma<-1>(a + 256 * DM * 2, DM * 2, o0, o1, 16 * 4096);
      waitvm<0>();
      tile_frags_lds(lds, (8 + cw) * 4096, lane, f);
      if (cw == 0) tile_frags_lds(lds, 16 * 4096, lane, f2);
    }
    {
      const int off = WS_AS + ((lane & 7) * FF + 288 * jw + 32 * cw + 8 * g4) * 4;
      u32x4 R[4] = {};
      not_before(tw, GATE_ACT);
      if (c16 < 8) {
        if (cw == 0) poll4(rs, off, off + 16, off + 1024, off + 1040, T16, 0xFFFFu, R);
        else poll2(rs, off, T16, 0xFFFFu, R[0], R[1]);
      }
      STAMP(12);
      const u32x4 a = afrag(R[0], R[1]);
      f32x4 d[4];
#pragma unroll
      for (int e = 0; e < 4; ++e) d[e] = mfma(f[e], a, f32x4{0.f, 0.f, 0.f, 0.f});
      if (cw == 0) {
        const u32x4 ab = afrag(R[2], R[3]);
#pragma unroll
        for (int e = 0; e < 4; ++e) d[e] = mfma(f2[e], ab, d[e]);
      }
      acc_to_lds(L_RED_W, d);
    }
    wg_barrier();
    publish_partial(WS_PD, jw, cg);
    STAMP(13);
  } else if (role == ROLE_G) {
    const int gx = rank;
    u32x4 f[5][4];
#pragma unroll
    for (int tl = 0; tl < 5; ++tl) {
      if (tl < 4 || cw < 4) {
        const int tau = cw + 8 * tl;
        const int n = tau < 16 ? tau : (tau >= 32 ? tau - 16 : tau + 4);
        wait_word(F_LANDED, tau < GEARLY ? PV + 4 * (32 - GEARLY + tau + 1) + 4 * (tau + 1) : PW + 4 * (n + 1));
        tile_frags_lds(lds, tau < 4 ? L_EXT + tau * 4096 : (tau - 4) * 4096, lane, f[tl]);
      }
    }
    wait_word(F_LANDED, PW + 144);
    STAMP(8);
    {
      const int off = WS_HS + (xcd * 8 + cw) * DM * 4;
      u32x4 h0, h1; u32 h2;
      not_before(to, GATE_H);
      poll3(rs, off + lane * 32, off + 2048 + lane * 4, T16, 0xFFFFu, h0, h1, h2);
      STAMP(10);
      float v[9]; bool valid[9];
#pragma unroll
      for (int e = 0; e < 4; ++e) { v[e] = bfhi(h0[e]); v[4 + e] = bfhi(h1[e]); }
      v[8] = bfhi(h2);
#pragma unroll
      for (int e = 0; e < 9; ++e) valid[e] = true;
      const float pivot = __uint_as_float(__builtin_amdgcn_readfirstlane(h0.x) & 0xFFFF0000u);
      const RowStats st = row_stats<9>(v, valid, pivot);
      u32* hn = lu + L_HN / 4 + cw * HN_STRIDE;
      *(u32x4*)(hn + 4 * lane) = u32x4{pack2(ln_apply(v[0], st), ln_apply(v[1], st)), pack2(ln_apply(v[2], st), ln_apply(v[3], st)),
                                       pack2(ln_apply(v[4], st), ln_apply(v[5], st)), pack2(ln_apply(v[6], st), ln_apply(v[7], st))};
      ((__bf16*)(hn + 256))[lane] = (__bf16)ln_apply(v[8], st);
    }
    STAMP(9);
    wg_barrier();
    STAMP(12);
    {
      f32x4 d[4];
      const u32* hn = lu + L_HN / 4 + (lane & 7) * HN_STRIDE + 16 * (cw >> 1) + 4 * g4;
      u32x4 a[5];
#pragma unroll
      for (int tl = 0; tl < 5; ++tl) a[tl] = *(const u32x4*)(hn + (tl < 4 || cw < 4 ? 64 * tl : 0));
      SB();
#pragma unroll
      for (int e = 0; e < 4; ++e) d[e] = mfma(f[0][e], a[0], f32x4{0.f, 0.f, 0.f, 0.f});
#pragma unroll
      for (int tl = 1; tl < 5; ++tl) {
        if (tl < 4 || cw < 4) {
#pragma unroll
          for (int e = 0; e < 4; ++e) d[e] = mfma(f[tl][e], a[tl], d[e]);
        }
      }
      STAMP(13);
      acc_to_lds(0, d);
    }
    wg_barrier();
    STAMP(3);
    {
      float gv[4], uv[4];
#pragma unroll
      for (int ww = 0; ww < 4; ++ww) { gv[ww] = lf[(2 * ww * 8 + cw) * RS + lane]; uv[ww] = lf[((2 * ww + 1) * 8 + cw) * RS + lane]; }
      const float gg = rbf((gv[0] + gv[1]) + (gv[2] + gv[3])), uu = rbf((uv[0] + uv[1]) + (uv[2] + uv[3]));
      const float sl = rbf(gg * __builtin_amdgcn_rcpf(1.0f + __builtin_amdgcn_exp2f(gg * -1.4426950408889634f)));
      const u32x4 o = gather4(bfbits_hi(sl * uu) | T16);
      if ((lane & 3) == 0) st16c(rs, WS_AS + (cw * FF + 64 * gx + lane) * 4, o);
    }
    STAMP(11);
  } else {
    const int f = rank - 216;
    const int job = 2 * f + (cw >> 2);
    if ((cw & 3) == 0 && job < 72) {
      const int jb = job / 9, cg = job % 9;
      const u32x2 xq = *(const GAS u32x2*)(p.x + jb * DM + 64 * cg + 4 * c16);
      const u32x4 wr0 = *(const GAS u32x4*)(p.wr + 8 * lane);
      const u16 wr1 = ((const GAS u16*)p.wr)[512 + lane];
      auto gather16 = [&](int base) -> f32x4 {
        const int off = base + (((jb * 9 + cg) * 16 + g4) * 64 + 4 * c16) * 4;
        u32x4 R[4];
        poll4(rs, off, off + 1024, off + 2048, off + 3072, T8, 0xFFu, R);
        const u32x4 q0 = R[0], q1 = R[1], q2 = R[2], q3 = R[3];
        f32x4 sum;
#pragma unroll
        for (int e = 0; e < 4; ++e) {
          float t = (get_tag<0xFFu>(q0[e]) + get_tag<0xFFu>(q1[e])) + (get_tag<0xFFu>(q2[e]) + get_tag<0xFFu>(q3[e]));
          t = swap16_add(t, t); t = swap32_add(t, t);
          sum[e] = t;
        }
        return sum;
      };
      STAMP(3);
      not_before(to, GATE_PW);
      const f32x4 at = gather16(WS_PW);
      STAMP(8);
      PMARK(8);
      RSTAMP(0);
      f32x4 hv;
      hv[0] = rbf(bflo(xq.x) + rbf(at[0])); hv[1] = rbf(bfhi(xq.x) + rbf(at[1]));
      hv[2] = rbf(bflo(xq.y) + rbf(at[2])); hv[3] = rbf(bfhi(xq.y) + rbf(at[3]));
      {
        const u32x4 hq = {__float_as_uint(hv[0]) | T16, __float_as_uint(hv[1]) | T16, __float_as_uint(hv[2]) | T16, __float_as_uint(hv[3]) | T16};
        const int o = WS_HS + ((g4 * 8 + jb) * DM + 64 * cg + 4 * c16) * 4;
        st16c(rs, o, hq); st16c(rs, o + 4 * 8 * DM * 4, hq);
      }
      const unsigned long long th = now();
      STAMP(9);
      RowStats st; float sig;
      {
        const int off = WS_HS + (xcd * 8 + jb) * DM * 4;
        u32x4 h0, h1; u32 h2;
        {
          unsigned spins = 0;
#pragma clang loop unroll(disable)
          for (;;) {
            h0 = ld16c(rs, off + lane * 32); h1 = ld16c(rs, off + lane * 32 + 16); h2 = ld4c(rs, off + 2048 + lane * 4);
            if (__all(tag_ok<0xFFFFu>(h0, T16) & tag_ok<0xFFFFu>(h1, T16) & ((h2 & 0xFFFFu) == T16)) || ++spins > SPIN_CAP) break;
            __builtin_amdgcn_s_sleep(2);
          }
        }
        float v[9]; bool valid[9];
#pragma unroll
        for (int e = 0; e < 4; ++e) { v[e] = bfhi(h0[e]); v[4 + e] = bfhi(h1[e]); }
        v[8] = bfhi(h2);
#pragma unroll
        for (int e = 0; e < 9; ++e) valid[e] = true;
        const float pivot = __uint_as_float(__builtin_amdgcn_readfirstlane(h0.x) & 0xFFFF0000u);
        st = row_stats<9>(v, valid, pivot);
        float r = rbf(ln_apply(v[8], st)) * bflo((u32)wr1);
#pragma unroll
        for (int e = 0; e < 8; ++e) r = fmaf(rbf(ln_apply(v[e], st)), (e & 1) ? bfhi(wr0[e >> 1]) : bflo(wr0[e >> 1]), r);
        r = rbf(wave_sum(r));
        sig = rbf(__builtin_amdgcn_rcpf(1.0f + __builtin_amdgcn_exp2f(r * -1.4426950408889634f)));
      }
      float hn[4];
#pragma unroll
      for (int e = 0; e < 4; ++e) hn[e] = rbf(ln_apply(hv[e], st));
      STAMP(10);
      not_before(th, GATE_PD);
      const f32x4 y = gather16(WS_PD);
      STAMP(12);
      RSTAMP(1);
      float o[4];
#pragma unroll
      for (int e = 0; e < 4; ++e) o[e] = hv[e] + rbf(hn[e] + rbf(rbf(y[e]) * sig));
      if (lane < 16) *(GAS u32x2*)(p.out + jb * DM + 64 * cg + 4 * lane) = u32x2{pack2(o[0], o[1]), pack2(o[2], o[3])};
      PMARK(9);
      if (job == 0 && lane == 0) __hip_atomic_store((GAS u32*)(p.ws + WS_EPOCH), T, __ATOMIC_RELAXED, __HIP_MEMORY_SCOPE_AGENT);
      STAMP(13);
    }
  }
  STAMP(14);
  STAMP_FLUSH();
}

}

extern "C" void kernel_launch(void* const* d_in, const int* in_sizes, int n_in, void* d_out, int out_size, void* d_ws, size_t ws_size,
                              hipStream_t stream) {
  Params p;
  p.x = (const u16*)d_in[0];
  p.kc = (const u16*)d_in[1];
  p.vc = (const u16*)d_in[2];
  p.wq = (const u16*)d_in[3];
  p.wo = (const u16*)d_in[4];
  p.wr = (const u16*)d_in[6];
  p.wg = (const u16*)d_in[7];
  p.wu = (const u16*)d_in[8];
  p.wd = (const u16*)d_in[9];
  p.out = (u16*)d_out;
  p.ws = (unsigned char*)d_ws;
  (void)hipFuncSetAttribute((const void*)fused_kernel, hipFuncAttributeMaxDynamicSharedMemorySize, LDS_BYTES);
  hipLaunchKernelGGL(fused_kernel, dim3(NWG), dim3(NTHR), LDS_BYTES, stream, p);
}
```

```cpp
#include <hip/hip_runtime.h>
#include <stdint.h>

typedef unsigned u32;
typedef unsigned short u16;
typedef u32 u32x4 __attribute__((ext_vector_type(4)));
typedef u32 u32x2 __attribute__((ext_vector_type(2)));
typedef float f32x4 __attribute__((ext_vector_type(4)));
typedef float f32x2 __attribute__((ext_vector_type(2)));
typedef short s16x4 __attribute__((ext_vector_type(4)));
typedef __bf16 bf16x8 __attribute__((ext_vector_type(8)));
typedef __bf16 bf16x2 __attribute__((ext_vector_type(2)));
#define GAS __attribute__((address_space(1)))
#define LAS __attribute__((address_space(3)))

#define STAMPS 0
#define LWIN 24
#define GWIN 40
#define QDELAY 32
#define XFIRST 0
#define GEARLY 4
#define GATE_PW 90
#define GATE_H 150
#define GATE_ACT 220
#define GATE_PD 270
#define PSTAG 6
#define PSLEEP 1
#define QS_T4 1
#define DYNROLE 1
#define OS_B 1

namespace {

constexpr int DM = 576, HKV = 16, SK = 4096, DH = 128, WIN = 1024, NQ = 4096, FF = 4608;
constexpr int NWG = 256, NTHR = 576;

constexpr int WS_EPOCH = 0;
constexpr int WS_RANK = 2048;
constexpr int WS_QS = 4096;
constexpr int WS_SC = WS_QS + 16 * 8 * 4 * 256 * 4;
constexpr int WS_OS = WS_SC + 256 * 128;
constexpr int WS_PW = WS_OS + 16 * 8 * 2 * 256 * 4;
constexpr int HREP = 8;
constexpr int WS_HS = WS_PW + 8 * 9 * 16 * 64 * 4;
constexpr int WS_AS = WS_HS + HREP * 8 * 576 * 4;
constexpr int WS_PD = WS_AS + 8 * 4608 * 4;
constexpr int WS_END = WS_PD + 8 * 9 * 16 * 64 * 4;
constexpr int WS_STAMP = (WS_END + 4095) / 4096 * 4096;
constexpr int AUX_SC1 = 16;
constexpr unsigned SPIN_CAP = 1u << 16, LSPIN_CAP = 1u << 22;

constexpr int L_EXT = 128 * 1024;
constexpr int L_FL = 144 * 1024;
constexpr int L_ST = L_FL + 128;
constexpr int L_P = L_ST + 256;
constexpr int L_QSUM = L_P + 2048;
constexpr int L_Q = L_QSUM + 8192;
constexpr int L_HN = L_P;
constexpr int HN_STRIDE = 292;
constexpr int RS = 68;
constexpr int L_RED_W = 112 * 1024;
constexpr int LDS_BYTES = 160 * 1024;
static_assert(L_Q + 512 <= LDS_BYTES && L_HN + 8 * HN_STRIDE * 4 <= LDS_BYTES, "LDS");
enum { F_LANDED = 0, F_QPUB = 1, F_BAR = 2, F_QRDY = 3, F_RANK = 4, F_FAC = 5, F_KDONE = 8, F_VDONE = 16 };

struct Params {
  const u16 *x, *kc, *vc, *wq, *wo, *wr, *wg, *wu, *wd;
  u16* out;
  unsigned char* ws;
};

__device__ __forceinline__ float bflo(u32 d) { return __uint_as_float(d << 16); }
__device__ __forceinline__ float bfhi(u32 d) { return __uint_as_float(d & 0xFFFF0000u); }
__device__ __forceinline__ float rbf(float f) { return (float)(__bf16)f; }
__device__ __forceinline__ u32 bfbits_hi(float f) { return __float_as_uint(rbf(f)); }
__device__ __forceinline__ u32 pack2(float lo, float hi) {
  bf16x2 v = {(__bf16)lo, (__bf16)hi};
  return __builtin_bit_cast(u32, v);
}
template <int CTRL> __device__ __forceinline__ float dpp(float x) {
  return __uint_as_float((u32)__builtin_amdgcn_mov_dpp((int)__float_as_uint(x), CTRL, 0xf, 0xf, true));
}
constexpr int XOR1 = 0xB1, XOR2 = 0x4E, XOR7 = 0x141, XOR8 = 0x128;
__device__ __forceinline__ float swap32_add(float a, float b) {
  auto r = __builtin_amdgcn_permlane32_swap(__float_as_uint(a), __float_as_uint(b), false, false);
  return __uint_as_float(r[0]) + __uint_as_float(r[1]);
}
__device__ __forceinline__ float swap16_add(float a, float b) {
  auto r = __builtin_amdgcn_permlane16_swap(__float_as_uint(a), __float_as_uint(b), false, false);
  return __uint_as_float(r[0]) + __uint_as_float(r[1]);
}
__device__ __forceinline__ float wave_sum(float x) {
  x += dpp<XOR1>(x); x += dpp<XOR2>(x); x += dpp<XOR7>(x); x += dpp<XOR8>(x);
  x = swap16_add(x, x); x = swap32_add(x, x);
  return x;
}
__device__ __forceinline__ float fmax_(float a, float b) { return __builtin_amdgcn_fmed3f(a, b, INFINITY); }
__device__ __forceinline__ float wave_max(float x) {
  x = fmax_(x, dpp<XOR1>(x)); x = fmax_(x, dpp<XOR2>(x)); x = fmax_(x, dpp<XOR7>(x)); x = fmax_(x, dpp<XOR8>(x));
  auto r = __builtin_amdgcn_permlane16_swap(__float_as_uint(x), __float_as_uint(x), false, false);
  x = fmax_(__uint_as_float(r[0]), __uint_as_float(r[1]));
  auto t = __builtin_amdgcn_permlane32_swap(__float_as_uint(x), __float_as_uint(x), false, false);
  return fmax_(__uint_as_float(t[0]), __uint_as_float(t[1]));
}
__device__ __forceinline__ f32x4 mfma(u32x4 a, u32x4 b, f32x4 c) {
  return __builtin_amdgcn_mfma_f32_16x16x32_bf16(__builtin_bit_cast(bf16x8, a), __builtin_bit_cast(bf16x8, b), c, 0, 0, 0);
}
#define SB() __builtin_amdgcn_sched_barrier(0)
template <int N> __device__ __forceinline__ void waitvm() { asm volatile("s_waitcnt vmcnt(%0)" ::"n"(N) : "memory"); }
typedef __amdgpu_buffer_rsrc_t rsrc_t;
__device__ __forceinline__ u32x4 ld16c(rsrc_t rs, int off) { return __builtin_amdgcn_raw_buffer_load_b128(rs, off, 0, AUX_SC1); }
__device__ __forceinline__ u32 ld4c(rsrc_t rs, int off) { return __builtin_amdgcn_raw_buffer_load_b32(rs, off, 0, AUX_SC1); }
__device__ __forceinline__ void st16c(rsrc_t rs, int off, u32x4 v) { __builtin_amdgcn_raw_buffer_store_b128(v, rs, off, 0, AUX_SC1); }
template <u32 M> __device__ __forceinline__ bool tag_ok(u32x4 v, u32 t) { return ((((v.x ^ t) | (v.y ^ t)) | ((v.z ^ t) | (v.w ^ t))) & M) == 0u; }
template <u32 M> __device__ __forceinline__ u32 put_tag(float f, u32 t) { return ((__float_as_uint(f) + (M + 1) / 2) & ~M) | t; }
template <u32 M> __device__ __forceinline__ float get_tag(u32 d) { return __uint_as_float(d & ~M); }
#define POLL_COUNT(n)
#define POLL(cond_load) do { unsigned spins_ = 0; _Pragma("clang loop unroll(disable)") while (!(cond_load) && ++spins_ < SPIN_CAP) __builtin_amdgcn_s_sleep(PSLEEP); POLL_COUNT(spins_); } while (0)

#define PV_CLOB "v128","v129","v130","v131","v132","v133","v134","v135","v136","v137","v138","v139","v140","v141","v142","v143", \
                "v144","v145","v146","v147","v148","v149","v150","v151","v152","v153","v154","v155","v156","v157","v158","v159","v160","v161","v162","v163", "vcc", "scc", "memory"
#define PV_LD4(r0, r1, r2, r3, r4, r5, r6, r7) \
  "buffer_load_dwordx4 v[" r0 ":" r1 "], %[o0], %[rs], 0 offen sc1\n\t" "buffer_load_dwordx4 v[" r2 ":" r3 "], %[o1], %[rs], 0 offen sc1\n\t" \
  "buffer_load_dwordx4 v[" r4 ":" r5 "], %[o2], %[rs], 0 offen sc1\n\t" "buffer_load_dwordx4 v[" r6 ":" r7 "], %[o3], %[rs], 0 offen sc1\n\t"
#define PV_X4(t, a, b, c, d) "v_xor_b32 " t ", %[T], v" a "\n\t" "v_bitop3_b32 " t ", " t ", v" b ", %[T] bitop3:0xf6\n\t" "v_bitop3_b32 " t ", " t ", v" c ", %[T] bitop3:0xf6\n\t" "v_bitop3_b32 " t ", " t ", v" d ", %[T] bitop3:0xf6\n\t"
#define PV_TEST "v_and_b32 v160, %[M], v160\n\tv_cmp_ne_u32 vcc, 0, v160\n\ts_cmp_eq_u64 vcc, 0\n\t"
#define PV_MOV2(o, a, b) "v_mov_b64 " o ", v[" a ":" b "]\n\t"
__device__ __forceinline__ void poll4(rsrc_t rs, int o0, int o1, int o2, int o3, u32 T, u32 M, u32x4 (&R)[4]) {
  u32x2 r0, r1, r2, r3, r4, r5, r6, r7; u32 cnt = SPIN_CAP;
  asm volatile(
      PV_LD4("128", "131", "132", "135", "136", "139", "140", "143")
      "s_sleep %[stag]\n"
      "1:\n\t"
      PV_LD4("144", "147", "148", "151", "152", "155", "156", "159")
      "s_waitcnt vmcnt(4)\n\t"
      PV_X4("v160", "128", "129", "130", "131") PV_X4("v161", "132", "133", "134", "135") PV_X4("v162", "136", "137", "138", "139") PV_X4("v163", "140", "141", "142", "143")
      "v_or3_b32 v160, v160, v161, v162\n\tv_or_b32 v160, v160, v163\n\t" PV_TEST
      "s_cbranch_scc1 7f\n\t"
      PV_LD4("128", "131", "132", "135", "136", "139", "140", "143")
      "s_waitcnt vmcnt(4)\n\t"
      PV_X4("v160", "144", "145", "146", "147") PV_X4("v161", "148", "149", "150", "151") PV_X4("v162", "152", "153", "154", "155") PV_X4("v163", "156", "157", "158", "159")
      "v_or3_b32 v160, v160, v161, v162\n\tv_or_b32 v160, v160, v163\n\t" PV_TEST
      "s_cbranch_scc1 8f\n\t"
      "s_sub_u32 %[cnt], %[cnt], 1\n\ts_cbranch_scc0 1b\n\t"
      "s_waitcnt vmcnt(0)\n"
      "7:\n\t"
      PV_MOV2("%[r0]", "128", "129") PV_MOV2("%[r1]", "130", "131") PV_MOV2("%[r2]", "132", "133") PV_MOV2("%[r3]", "134", "135")
      PV_MOV2("%[r4]", "136", "137") PV_MOV2("%[r5]", "138", "139") PV_MOV2("%[r6]", "140", "141") PV_MOV2("%[r7]", "142", "143")
      "s_branch 9f\n"
      "8:\n\t"
      PV_MOV2("%[r0]", "144", "145") PV_MOV2("%[r1]", "146", "147") PV_MOV2("%[r2]", "148", "149") PV_MOV2("%[r3]", "150", "151")
      PV_MOV2("%[r4]", "152", "153") PV_MOV2("%[r5]", "154", "155") PV_MOV2("%[r6]", "156", "157") PV_MOV2("%[r7]", "158", "159")
      "9:\n\t"
      : [r0] "=&v"(r0), [r1] "=&v"(r1), [r2] "=&v"(r2), [r3] "=&v"(r3), [r4] "=&v"(r4), [r5] "=&v"(r5), [r6] "=&v"(r6), [r7] "=&v"(r7), [cnt] "+s"(cnt)
      : [o0] "v"(o0), [o1] "v"(o1), [o2] "v"(o2), [o3] "v"(o3), [rs] "s"(rs), [T] "s"(T), [M] "s"(M), [stag] "n"(PSTAG)
      : PV_CLOB);
  R[0] = u32x4{r0.x, r0.y, r1.x, r1.y}; R[1] = u32x4{r2.x, r2.y, r3.x, r3.y}; R[2] = u32x4{r4.x, r4.y, r5.x, r5.y}; R[3] = u32x4{r6.x, r6.y, r7.x, r7.y};
}
#define PV_LD2(r0, r1, r2, r3) \
  "buffer_load_dwordx4 v[" r0 ":" r1 "], %[o0], %[rs], 0 offen sc1\n\t" "buffer_load_dwordx4 v[" r2 ":" r3 "], %[o0], %[rs], 0 offen offset:16 sc1\n\t"
__device__ __forceinline__ void poll2(rsrc_t rs, int o0, u32 T, u32 M, u32x4& a, u32x4& b) {
  u32x2 r0, r1, r2, r3; u32 cnt = SPIN_CAP;
  asm volatile(
      PV_LD2("128", "131", "132", "135")
      "s_sleep %[stag]\n"
      "1:\n\t"
      PV_LD2("144", "147", "148", "151")
      "s_waitcnt vmcnt(2)\n\t"
      PV_X4("v160", "128", "129", "130", "131") PV_X4("v161", "132", "133", "134", "135") "v_or_b32 v160, v160, v161\n\t" PV_TEST
      "s_cbranch_scc1 7f\n\t"
      PV_LD2("128", "131", "132", "135")
      "s_waitcnt vmcnt(2)\n\t"
      PV_X4("v160", "144", "145", "146", "147") PV_X4("v161", "148", "149", "150", "151") "v_or_b32 v160, v160, v161\n\t" PV_TEST
      "s_cbranch_scc1 8f\n\t"
      "s_sub_u32 %[cnt], %[cnt], 1\n\ts_cbranch_scc0 1b\n\t"
      "s_waitcnt vmcnt(0)\n"
      "7:\n\t"
      PV_MOV2("%[r0]", "128", "129") PV_MOV2("%[r1]", "130", "131") PV_MOV2("%[r2]", "132", "133") PV_MOV2("%[r3]", "134", "135")
      "s_branch 9f\n"
      "8:\n\t"
      PV_MOV2("%[r0]", "144", "145") PV_MOV2("%[r1]", "146", "147") PV_MOV2("%[r2]", "148", "149") PV_MOV2("%[r3]", "150", "151")
      "9:\n\t"
      : [r0] "=&v"(r0), [r1] "=&v"(r1), [r2] "=&v"(r2), [r3] "=&v"(r3), [cnt] "+s"(cnt)
      : [o0] "v"(o0), [rs] "s"(rs), [T] "s"(T), [M] "s"(M), [stag] "n"(PSTAG)
      : PV_CLOB);
  a = u32x4{r0.x, r0.y, r1.x, r1.y}; b = u32x4{r2.x, r2.y, r3.x, r3.y};
}
#define PV_LD3(r0, r1, r2, r3, r4) \
  "buffer_load_dwordx4 v[" r0 ":" r1 "], %[o0], %[rs], 0 offen sc1\n\t" "buffer_load_dwordx4 v[" r2 ":" r3 "], %[o0], %[rs], 0 offen offset:16 sc1\n\t" \
  "buffer_load_dword v" r4 ", %[o1], %[rs], 0 offen sc1\n\t"
__device__ __forceinline__ void poll3(rsrc_t rs, int o0, int o1, u32 T, u32 M, u32x4& a, u32x4& b, u32& c) {
  u32x2 r0, r1, r2, r3; u32 r4, cnt = SPIN_CAP;
  asm volatile(
      PV_LD3("128", "131", "132", "135", "136")
      "s_sleep %[stag]\n"
      "1:\n\t"
      PV_LD3("144", "147", "148", "151", "152")
      "s_waitcnt vmcnt(3)\n\t"
      PV_X4("v160", "128", "129", "130", "131") PV_X4("v161", "132", "133", "134", "135") "v_xor_b32 v162, %[T], v136\n\t"
      "v_or3_b32 v160, v160, v161, v162\n\t" PV_TEST
      "s_cbranch_scc1 7f\n\t"
      PV_LD3("128", "131", "132", "135", "136")
      "s_waitcnt vmcnt(3)\n\t"
      PV_X4("v160", "144", "145", "146", "147") PV_X4("v161", "148", "149", "150", "151") "v_xor_b32 v162, %[T], v152\n\t"
      "v_or3_b32 v160, v160, v161, v162\n\t" PV_TEST
      "s_cbranch_scc1 8f\n\t"
      "s_sub_u32 %[cnt], %[cnt], 1\n\ts_cbranch_scc0 1b\n\t"
      "s_waitcnt vmcnt(0)\n"
      "7:\n\t"
      PV_MOV2("%[r0]", "128", "129") PV_MOV2("%[r1]", "130", "131") PV_MOV2("%[r2]", "132", "133") PV_MOV2("%[r3]", "134", "135") "v_mov_b32 %[r4], v136\n\t"
      "s_branch 9f\n"
      "8:\n\t"
      PV_MOV2("%[r0]", "144", "145") PV_MOV2("%[r1]", "146", "147") PV_MOV2("%[r2]", "148", "149") PV_MOV2("%[r3]", "150", "151") "v_mov_b32 %[r4], v152\n\t"
      "9:\n\t"
      : [r0] "=&v"(r0), [r1] "=&v"(r1), [r2] "=&v"(r2), [r3] "=&v"(r3), [r4] "=&v"(r4), [cnt] "+s"(cnt)
      : [o0] "v"(o0), [o1] "v"(o1), [rs] "s"(rs), [T] "s"(T), [M] "s"(M), [stag] "n"(PSTAG)
      : PV_CLOB);
  a = u32x4{r0.x, r0.y, r1.x, r1.y}; b = u32x4{r2.x, r2.y, r3.x, r3.y}; c = r4;
}

__device__ __forceinline__ unsigned long long now() { return __builtin_amdgcn_s_memrealtime(); }
__device__ __forceinline__ void not_before(unsigned long long t0, unsigned ticks) {
  while (now() - t0 < ticks) __builtin_amdgcn_s_sleep(2);
}
__device__ __forceinline__ u32 lds_ld(u32 addr) {
  u32 v;
  asm volatile("ds_read_b32 %0, %1\n\ts_waitcnt lgkmcnt(0)" : "=v"(v) : "v"(addr) : "memory");
  return v;
}
__device__ __forceinline__ void lds_st(u32 addr, u32 v) { asm volatile("ds_write_b32 %0, %1" ::"v"(addr), "v"(v) : "memory"); }
__device__ __forceinline__ void wait_word(int word, u32 want) {
  unsigned spins = 0;
#pragma clang loop unroll(disable)
  while ((u32)__builtin_amdgcn_readfirstlane(lds_ld(L_FL + 4 * word)) < want)
    if (++spins > LSPIN_CAP) break;
}
__device__ __forceinline__ void wait_word_sleep(int word, u32 want) {
  unsigned spins = 0;
#pragma clang loop unroll(disable)
  while ((u32)__builtin_amdgcn_readfirstlane(lds_ld(L_FL + 4 * word)) < want) {
    __builtin_amdgcn_s_sleep(2);
    if (++spins > LSPIN_CAP) break;
  }
}
__device__ __forceinline__ void wait_all8(int word0, u32 want, int lane) {
  unsigned spins = 0;
#pragma clang loop unroll(disable)
  while (!__all(lds_ld(L_FL + 4 * (word0 + (lane & 7))) >= want)) {
    __builtin_amdgcn_s_sleep(1);
    if (++spins > LSPIN_CAP) break;
  }
}
__device__ __forceinline__ void cbar(u32 n, int lane) {
  asm volatile("s_waitcnt lgkmcnt(0)" ::: "memory");
  if (lane == 0) asm volatile("ds_add_u32 %0, %1" ::"v"((u32)(L_FL + 4 * F_BAR)), "v"(1u) : "memory");
  wait_word(F_BAR, 8u * n);
}
__device__ __forceinline__ void hw_barrier() { __builtin_amdgcn_sched_barrier(0); __builtin_amdgcn_s_barrier(); __builtin_amdgcn_sched_barrier(0); }
__device__ __forceinline__ void wg_barrier() {
  asm volatile("s_waitcnt lgkmcnt(0)" ::: "memory");
  hw_barrier();
  asm volatile("" ::: "memory");
}
__device__ __forceinline__ void glds16(const void* sbase, u32 voff, u32 lds_dst) {
  asm volatile("s_mov_b32 m0, %2\n\ts_nop 0\n\tglobal_load_lds_dwordx4 %0, %1 nt" ::"v"(voff), "s"(sbase), "s"(lds_dst) : "memory");
}
__device__ __forceinline__ int vswz(int row) { return ((row & 3) << 2) | ((row >> 2) & 3); }
__device__ __forceinline__ int swz(int row) { return ((row >> 1) & 1) | ((row >> 2) & 2); }
__device__ __forceinline__ u32 rowoff(int q, int stride, int lane) { const int row = 8 * q + (lane >> 3); return row * stride + (((lane & 7) ^ (swz(row) << 1)) & 7) * 16; }
template <int W> __device__ __forceinline__ void tile_dma(const unsigned char* a, int stride, u32 o0, u32 o1, u32 dst) {
  glds16(a, o0, dst); if (W >= 0) waitvm<W>();
  glds16(a, o1, dst + 1024); if (W >= 0) waitvm<W>();
  glds16(a + (size_t)16 * stride, o0, dst + 2048); if (W >= 0) waitvm<W>();
  glds16(a + (size_t)16 * stride, o1, dst + 3072); if (W >= 0) waitvm<W>();
}
__device__ __forceinline__ void tile_frags_lds(const unsigned char* lds, u32 base, int lane, u32x4 (&f)[4]) {
  const int g = lane >> 4, q = (lane & 15) >> 2, p = lane & 3;
#pragma unroll
  for (int e = 0; e < 4; ++e) {
    u32x2 h[2];
#pragma unroll
    for (int t = 0; t < 2; ++t) {
      const int row = 8 * g + 4 * t + q;
      const u32 a = base + row * 128 + (((2 * e + (p >> 1)) ^ (swz(row) << 1)) << 4) + 8 * (p & 1);
      h[t] = __builtin_bit_cast(u32x2, __builtin_amdgcn_ds_read_tr16_b64_v4i16((LAS s16x4*)(lds + a)));
    }
    f[e] = u32x4{h[0].x, h[0].y, h[1].x, h[1].y};
  }
}

#define ESTAMP(k) do { } while (0)
#define STAMP(k) do { } while (0)
#define STAMP_FLUSH() do { } while (0)

__device__ __forceinline__ u32x4 afrag(u32x4 a, u32x4 b) {
  u32x4 f;
  f.x = __builtin_amdgcn_perm(a.y, a.x, 0x07060302u);
  f.y = __builtin_amdgcn_perm(a.w, a.z, 0x07060302u);
  f.z = __builtin_amdgcn_perm(b.y, b.x, 0x07060302u);
  f.w = __builtin_amdgcn_perm(b.w, b.z, 0x07060302u);
  return f;
}

struct RowStats { float m, rs; };
template <int N> __device__ __forceinline__ RowStats row_stats(const float (&v)[N], const bool (&valid)[N], float pivot) {
  float s1[2] = {0.f, 0.f}, s2[2] = {0.f, 0.f};
#pragma unroll
  for (int e = 0; e < N; ++e) { const float d = valid[e] ? v[e] - pivot : 0.f; s1[e & 1] += d; s2[e & 1] = fmaf(d, d, s2[e & 1]); }
  const float m1 = wave_sum(s1[0] + s1[1]) * (1.0f / DM), m2 = wave_sum(s2[0] + s2[1]) * (1.0f / DM);
  const float var = rbf(fmax_(fmaf(-m1, m1, m2), 0.f));
  RowStats r;
  r.m = rbf(pivot + m1);
  r.rs = __builtin_amdgcn_rcpf(rbf(__builtin_amdgcn_sqrtf(rbf(var + 1e-5f))));
  return r;
}
__device__ __forceinline__ float ln_apply(float v, RowStats s) { return rbf(v - s.m) * s.rs; }

enum { ROLE_W = 0, ROLE_G = 1, ROLE_F = 2 };

__global__ void __launch_bounds__(NTHR, 3) fused_kernel(Params p) {
  extern __shared__ __attribute__((aligned(16))) unsigned char lds[];
  const int tid = threadIdx.x, lane = tid & 63, w = __builtin_amdgcn_readfirstlane(tid >> 6);
  const int wg = blockIdx.x, xcd = wg & 7, s = wg >> 3, j = xcd + 8 * (s >> 4), i = s & 15, b = i >> 1, half = i & 1;
  const size_t ubase = ((size_t)(b * HKV + j) * SK + (SK - WIN)) * DH;
  const int cq = i & 3, kp = i >> 2;
  const int ks0 = kp < 3 ? 4 * kp : 13, nt = kp < 2 ? 4 : 5;
  const u32 PK = 4 * nt, PV = PK + 128, PW = PK + 256;
  const int g4 = lane >> 4, c16 = lane & 15;
#define RSTAMP(k)
#define PMARK(n)
  STAMP(0);
  const int cw = (w + 7) & 7;
  u32 xr[5];
  u32x4 xa;
  auto load_x = [&]() {
    const GAS u32* xrow = (const GAS u32*)(p.x + cw * DM);
#pragma unroll
    for (int m = 0; m < 4; ++m) xr[m] = __builtin_nontemporal_load(xrow + lane + 64 * m);
    xr[4] = __builtin_nontemporal_load(xrow + (lane & 31) + 256);
    xa = __builtin_nontemporal_load((const GAS u32x4*)(p.x + (lane & 7) * DM + 32 * (ks0 + (cw < nt ? cw : 0)) + 8 * g4));
  };
  if (w == 0) {
    if (lane < 32) lds_st(L_FL + 4 * lane, 0u);
    asm volatile("s_waitcnt lgkmcnt(0)" ::: "memory");
  }
  hw_barrier();

  if (w == 0) {
    __builtin_amdgcn_s_setprio(3);
    u32 pub = 0;
#define PUBLISH(W) lds_st(L_FL + 4 * F_LANDED, pub - (W))
    {
      const unsigned char* a = (const unsigned char*)(p.wq + (size_t)32 * ks0 * NQ + 256 * j + 64 * cq);
      const u32 o0 = rowoff(0, NQ * 2, lane), o1 = rowoff(1, NQ * 2, lane);
#pragma unroll
      for (int t = 0; t < 4; ++t) tile_dma<-1>(a + (size_t)t * 32 * NQ * 2, NQ * 2, o0, o1, L_EXT + t * 4096);
      if (nt == 5) tile_dma<-1>(a + (size_t)4 * 32 * NQ * 2, NQ * 2, o0, o1, L_QSUM);
      pub = PK;
    }
    ESTAMP(8);
    {
      const unsigned char* kb = (const unsigned char*)(p.kc + ubase + (size_t)half * 512 * DH);
      u32 koff[4];
#pragma unroll
      for (int q = 0; q < 4; ++q) { const int key = 4 * q + g4; koff[q] = key * 256 + ((c16 ^ key) & 15) * 16; }
      auto kseg = [&](int t0, int t1) {
#pragma clang loop unroll(disable)
        for (int t = t0; t < t1; ++t) {
#pragma unroll
          for (int q = 0; q < 4; ++q) { glds16(kb + t * 4096, koff[q], (t * 4 + q) * 1024); waitvm<LWIN>(); }
          pub += 4;
          if (pub > LWIN) PUBLISH(LWIN);
        }
      };
      kseg(0, 32);
    }
    STAMP(1);
    wait_word_sleep(F_RANK, 1u);
    const int rank = (int)__builtin_amdgcn_readfirstlane(lds_ld(L_FL + 4 * F_RANK)) - 1;
    const int role = rank < 72 ? ROLE_G : (rank < 216 ? ROLE_W : ROLE_F);
    const u32 go0 = rowoff(0, FF * 2, lane), go1 = rowoff(1, FF * 2, lane);
    auto gtile = [&](int t) -> const unsigned char* { return (const unsigned char*)(((t & 1) ? p.wu : p.wg) + (size_t)(t >> 1) * 32 * FF + 64 * rank); };
    {
      const unsigned char* vb = (const unsigned char*)(p.vc + ubase + (size_t)half * 512 * DH);
      u32 voff[4];
#pragma unroll
      for (int q = 0; q < 4; ++q) { const int key = 4 * q + g4; voff[q] = key * 256 + ((c16 ^ vswz(key)) & 15) * 16; }
      wait_word(F_QPUB, 1u);
#pragma clang loop unroll(disable)
      for (int t = 0; t < 32; ++t) {
        if ((t & 7) == 4) wait_all8(F_KDONE, (t >> 3) + 1, lane);
        const u32 dst = t < 4 ? L_EXT + t * 4096 : (t - 4) * 4096;
#pragma unroll
        for (int q = 0; q < 4; ++q) { glds16(vb + t * 4096, voff[q], dst + q * 1024); waitvm<LWIN>(); }
        pub += 4; PUBLISH(LWIN);
        if (role == ROLE_G && t >= 32 - GEARLY) {
          const int tau = t - (32 - GEARLY);
          if (tau == 0) wait_all8(F_VDONE, 1, lane);
          tile_dma<LWIN>(gtile(tau), FF * 2, go0, go1, tau < 4 ? L_EXT + tau * 4096 : (tau - 4) * 4096);
          pub += 4; PUBLISH(LWIN);
        }
      }
    }
    STAMP(2);
    if (role == ROLE_W) {
      const int jw = (rank - 72) / 9, cg = (rank - 72) % 9;
      const unsigned char* a = (const unsigned char*)(p.wo + (size_t)256 * jw * DM + 64 * cg);
      const u32 o0 = rowoff(0, DM * 2, lane), o1 = rowoff(1, DM * 2, lane);
      wait_all8(F_VDONE, 1, lane);
#pragma clang loop unroll(disable)
      for (int t = 0; t < 8; ++t) { tile_dma<-1>(a + (size_t)t * 32 * DM * 2, DM * 2, o0, o1, t * 4096); pub += 4; }
    } else if (role == ROLE_G) {
      static_assert(GEARLY >= 1 && 80 - GWIN >= 4 * GEARLY - 4, "the last tile of V is published before the loader waits for its consumption");
#pragma clang loop unroll(disable)
      for (int n = GEARLY; n < 36; ++n) {
        if (n == 20) wait_all8(F_VDONE, 2, lane);
        const int t = n < 16 ? n : (n < 20 ? n + 16 : n - 4);
        tile_dma<GWIN>(gtile(t), FF * 2, go0, go1, t < 4 ? L_EXT + t * 4096 : (t - 4) * 4096);
        pub += 4;
        if (4 * (n - GEARLY) + 4 > GWIN - LWIN) PUBLISH(GWIN);
      }
    }
    STAMP(3);
#define DR(N) if ((role == ROLE_G ? GWIN : (role == ROLE_W ? LWIN + 32 : LWIN)) > N) { waitvm<N>(); PUBLISH(N); }
    static_assert(LWIN + 32 <= 60 && GWIN <= 60, "vmcnt");
    DR(56) DR(52) DR(48) DR(44) DR(40) DR(36) DR(32) DR(28) DR(24) DR(20) DR(16) DR(12) DR(8) DR(4) DR(0)
#undef DR
#undef PUBLISH
    STAMP(14);
    STAMP_FLUSH();
    return;
  }

  load_x();
  const int ct = tid - 64;
  const rsrc_t rs = __builtin_amdgcn_make_buffer_rsrc((void*)p.ws, 0, WS_END, 0x00020000);
  const u32 T = *(const __attribute__((address_space(4))) u32*)(p.ws + WS_EPOCH) + 1u;
  const u32 T16 = T & 0xFFFFu, T8 = T & 0xFFu, T4 = T & 0xFu;
  const u32 tstart = (u32)now();
  if (ct == 448) __builtin_amdgcn_raw_buffer_store_b64(u32x2{tstart, T}, rs, WS_RANK + wg * 8, 0, AUX_SC1);
  float* const lf = (float*)lds;
  u32* const lu = (u32*)lds;
  {
    ESTAMP(8);
    asm volatile("" :: "v"(xr[0]), "v"(xr[4]));
    ESTAMP(9);
    float v[10]; bool valid[10];
#pragma unroll
    for (int m = 0; m < 5; ++m) { valid[2 * m] = valid[2 * m + 1] = (m < 4) || (lane < 32); v[2 * m] = bflo(xr[m]); v[2 * m + 1] = bfhi(xr[m]); }
    const float pivot = __uint_as_float(__builtin_amdgcn_readfirstlane(xr[0]) << 16);
    const RowStats st = row_stats<10>(v, valid, pivot);
    if (lane == 0) *(f32x2*)(lf + L_ST / 4 + 2 * cw) = f32x2{st.m, st.rs};
    ESTAMP(10);
    cbar(1, lane);
    STAMP(1);
    PMARK(1);
  }
  if (cw < nt) {
    const f32x2 sv = *(const f32x2*)(lf + L_ST / 4 + 2 * (lane & 7));
    const RowStats st = {sv.x, sv.y};
    u32x4 a;
#pragma unroll
    for (int q = 0; q < 4; ++q) a[q] = pack2(ln_apply(bflo(xa[q]), st), ln_apply(bfhi(xa[q]), st));
    const u32 base = cw < 4 ? L_EXT + cw * 4096 : L_QSUM;
    wait_word(F_LANDED, 4 * (cw + 1));
    u32x4 f[4]; tile_frags_lds(lds, base, lane, f);
    f32x4 d[4];
#pragma unroll
    for (int e = 0; e < 4; ++e) d[e] = mfma(f[e], a, f32x4{0.f, 0.f, 0.f, 0.f});
    if (c16 < 8) {
#pragma unroll
      for (int e = 0; e < 4; ++e) *(f32x4*)(lf + base / 4 + c16 * RS + 16 * e + 4 * g4) = d[e];
    }
  }
  ESTAMP(11);
  cbar(2, lane);
  ESTAMP(12);
  {
    float sum = (lf[L_EXT / 4 + cw * RS + lane] + lf[L_EXT / 4 + 1024 + cw * RS + lane]) + (lf[L_EXT / 4 + 2048 + cw * RS + lane] + lf[L_EXT / 4 + 3072 + cw * RS + lane]);
    if (nt == 5) sum += lf[L_QSUM / 4 + cw * RS + lane];
    const u32 v = put_tag<0xFu>(sum, T4);
    const u32x4 o = {v, (u32)__builtin_amdgcn_mov_dpp((int)v, 0x55, 0xf, 0xf, true), (u32)__builtin_amdgcn_mov_dpp((int)v, 0xAA, 0xf, 0xf, true),
                     (u32)__builtin_amdgcn_mov_dpp((int)v, 0xFF, 0xf, 0xf, true)};
    if ((lane & 3) == 0) st16c(rs, WS_QS + ((((j * 8 + cw) * 4 + kp) * 256) + 64 * cq + lane) * 4, o);
  }
  cbar(3, lane);
  if (ct == 0) lds_st(L_FL + 4 * F_QPUB, 1u);
  STAMP(2);
  PMARK(2);
  if (cw >= 2 && cw < 6) {
    u32x4 qg;
    __builtin_amdgcn_s_sleep(QDELAY);
    {
      unsigned spins = 0;
#pragma clang loop unroll(disable)
      for (;;) {
        qg = ld16c(rs, WS_QS + (((j * 8 + b) * 4 + (cw - 2)) * 256 + 4 * lane) * 4);
        if (__all(tag_ok<0xFu>(qg, T4)) || ++spins > SPIN_CAP) break;
        __builtin_amdgcn_s_sleep(4);
      }
    }
    *(f32x4*)(lf + L_QSUM / 4 + (cw - 2) * 256 + 4 * lane) = f32x4{get_tag<0xFu>(qg.x), get_tag<0xFu>(qg.y), get_tag<0xFu>(qg.z), get_tag<0xFu>(qg.w)};
  }
  if (cw == 7) {
    u32x4 e0, e1; unsigned spins = 0;
#pragma clang loop unroll(disable)
    for (;;) {
      e0 = ld16c(rs, WS_RANK + lane * 32); e1 = ld16c(rs, WS_RANK + lane * 32 + 16);
      if (__all((e0.y == T) & (e0.w == T) & (e1.y == T) & (e1.w == T)) || ++spins > SPIN_CAP) break;
      __builtin_amdgcn_s_sleep(4);
    }
    auto before = [&](u32 t, int g) -> float { const int d = (int)(t - tstart); return (d < 0 || (d == 0 && g < wg)) ? 1.f : 0.f; };
    const float n = wave_sum((before(e0.x, 4 * lane) + before(e0.z, 4 * lane + 1)) + (before(e1.x, 4 * lane + 2) + before(e1.z, 4 * lane + 3)));
    if (lane == 0) lds_st(L_FL + 4 * F_RANK, (u32)n + 1u);
  }
  cbar(4, lane);
  if (cw == 0) {
    f32x4 sv[4];
#pragma unroll
    for (int q = 0; q < 4; ++q) sv[q] = *(const f32x4*)(lf + L_QSUM / 4 + q * 256 + 4 * lane);
    const f32x4 sum = (sv[0] + sv[1]) + (sv[2] + sv[3]);
    *(u32x2*)(lu + L_Q / 4 + 2 * lane) = u32x2{pack2(sum.x, sum.y), pack2(sum.z, sum.w)};
    asm volatile("s_waitcnt lgkmcnt(0)" ::: "memory");
    if (lane == 0) lds_st(L_FL + 4 * F_QRDY, 1u);
  } else {
    wait_word(F_QRDY, 1u);
  }
  STAMP(3);
  PMARK(3);
  float s0, s1;
  {
    u32x4 qa[4];
#pragma unroll
    for (int ks = 0; ks < 4; ++ks) qa[ks] = *(const u32x4*)(lu + L_Q / 4 + (lane & 1) * 64 + 16 * ks + 4 * g4);
    f32x4 d[4];
#pragma unroll
    for (int m = 0; m < 4; ++m) {
      const int tile = cw + 8 * m;
      wait_word_sleep(F_LANDED, PK + 4 * (tile + 1));
      u32x4 kf[4];
#pragma unroll
      for (int ks = 0; ks < 4; ++ks) kf[ks] = *(const u32x4*)(lds + tile * 4096 + c16 * 256 + (((4 * ks + g4) ^ c16) << 4));
      f32x4 acc = mfma(qa[0], kf[0], f32x4{0.f, 0.f, 0.f, 0.f});
      acc = mfma(qa[1], kf[1], acc); acc = mfma(qa[2], kf[2], acc); acc = mfma(qa[3], kf[3], acc);
      d[m] = acc;
      asm volatile("s_waitcnt lgkmcnt(0)" ::: "memory");
      if (lane == 0) lds_st(L_FL + 4 * (F_KDONE + cw), m + 1);
    }
    const float x0 = g4 == 0 ? d[0][0] : g4 == 1 ? d[1][0] : g4 == 2 ? d[2][0] : d[3][0];
    const float x1 = g4 == 0 ? d[0][1] : g4 == 1 ? d[1][1] : g4 == 2 ? d[2][1] : d[3][1];
    constexpr float SCALE = 0.08838834764831845f;
    s0 = rbf(rbf(x0) * SCALE); s1 = rbf(rbf(x1) * SCALE);
  }
  STAMP(4);
  PMARK(4);
  u32x4 mine;
  {
    constexpr float L2E = 1.4426950408889634f;
    const float m0 = wave_max(s0), m1 = wave_max(s1);
    const float l0 = wave_sum(__builtin_amdgcn_exp2f((s0 - m0) * L2E)), l1 = wave_sum(__builtin_amdgcn_exp2f((s1 - m1) * L2E));
    if (lane == 0) *(f32x4*)(lf + L_ST / 4 + 16 + 4 * cw) = f32x4{m0, m1, l0, l1};
    cbar(5, lane);
    float M0 = -INFINITY, M1 = -INFINITY;
    f32x4 st[8];
#pragma unroll
    for (int ww = 0; ww < 8; ++ww) { st[ww] = *(const f32x4*)(lf + L_ST / 4 + 16 + 4 * ww); M0 = fmax_(M0, st[ww].x); M1 = fmax_(M1, st[ww].y); }
    float L0 = 0.f, L1 = 0.f;
#pragma unroll
    for (int ww = 0; ww < 8; ++ww) {
      L0 = fmaf(st[ww].z, __builtin_amdgcn_exp2f((st[ww].x - M0) * L2E), L0);
      L1 = fmaf(st[ww].w, __builtin_amdgcn_exp2f((st[ww].y - M1) * L2E), L1);
    }
    const int kl = 16 * (cw + 8 * g4) + c16;
    __bf16* P = (__bf16*)(lds + L_P);
    P[kl] = (__bf16)__builtin_amdgcn_exp2f((s0 - M0) * L2E);
    P[512 + kl] = (__bf16)__builtin_amdgcn_exp2f((s1 - M1) * L2E);
    mine = u32x4{__float_as_uint(M0) | T16, __float_as_uint(M1) | T16, put_tag<0xFFu>(L0, T8), put_tag<0xFFu>(L1, T8)};
    if (ct == 64) st16c(rs, WS_SC + wg * 128, mine);
  }
  cbar(6, lane);
  STAMP(6);
  wait_word(F_RANK, 1u);
  const int rank = (int)__builtin_amdgcn_readfirstlane(lds_ld(L_FL + 4 * F_RANK)) - 1;
  const int role = rank < 72 ? ROLE_G : (rank < 216 ? ROLE_W : ROLE_F);
  {
    const int q4 = (lane & 15) >> 2, p4 = lane & 3;
    auto vfrag = [&](u32 base, int e) -> u32x4 {
      u32x2 h[2];
#pragma unroll
      for (int t = 0; t < 2; ++t) {
        const int row = 8 * g4 + 4 * t + q4;
        const u32 a = base + row * 256 + (((2 * e + (p4 >> 1)) ^ vswz(row)) << 4) + 8 * (p4 & 1);
        h[t] = __builtin_bit_cast(u32x2, __builtin_amdgcn_ds_read_tr16_b64_v4i16((LAS s16x4*)(lds + a)));
      }
      return u32x4{h[0].x, h[0].y, h[1].x, h[1].y};
    };
    auto pfrag = [&](int ks) -> u32x4 { return *(const u32x4*)(lds + L_P + (lane & 1) * 1024 + (32 * ks + 8 * g4) * 2); };
    {
      const u32x4 pf = pfrag(cw);
      const u32 base = cw < 2 ? L_EXT + cw * 8192 : (cw - 2) * 8192;
      wait_word(F_LANDED, PV + 8 * (cw + 1));
      u32x4 f[8];
#pragma unroll
      for (int e = 0; e < 8; ++e) f[e] = vfrag(base, e);
      SB();
      f32x4 d[8];
#pragma unroll
      for (int e = 0; e < 8; ++e) d[e] = mfma(f[e], pf, f32x4{0.f, 0.f, 0.f, 0.f});
      asm volatile("s_waitcnt lgkmcnt(0)" ::: "memory");
      if (lane == 0) lds_st(L_FL + 4 * (F_VDONE + cw), 1u);
      if (c16 < 2) {
#pragma unroll
        for (int e = 0; e < 8; ++e) *(f32x4*)(lf + L_QSUM / 4 + cw * 256 + c16 * 128 + 16 * e + 4 * g4) = d[e];
      }
    }
    cbar(7, lane);
    f32x4 acc;
    {
      f32x4 sv[8];
#pragma unroll
      for (int ww = 0; ww < 8; ++ww) sv[ww] = *(const f32x4*)(lf + L_QSUM / 4 + ww * 256 + (lane & 1) * 128 + 16 * cw + 4 * g4);
      acc = ((sv[0] + sv[1]) + (sv[2] + sv[3])) + ((sv[4] + sv[5]) + (sv[6] + sv[7]));
    }
    u32x4 oth = {0u, 0u, 0u, 0u};
    bool got = cw != 1;
    if (cw == 1) oth = ld16c(rs, WS_SC + (wg ^ 8) * 128);
    auto try_stats = [&](bool last) {
      if (got) return;
      unsigned spins = 0;
#pragma clang loop unroll(disable)
      for (;;) {
        if (__all(((((oth.x ^ T16) | (oth.y ^ T16)) & 0xFFFFu) | (((oth.z ^ T8) | (oth.w ^ T8)) & 0xFFu)) == 0u) || ++spins > SPIN_CAP) { got = true; break; }
        oth = ld16c(rs, WS_SC + (wg ^ 8) * 128);
        if (!last) return;
      }
      constexpr float L2E = 1.4426950408889634f;
      const float M0 = bfhi(mine.x), M1 = bfhi(mine.y), om0 = bfhi(oth.x), om1 = bfhi(oth.y);
      const float G0 = fmax_(M0, om0), G1 = fmax_(M1, om1);
      const float e0 = __builtin_amdgcn_exp2f((M0 - G0) * L2E), e1 = __builtin_amdgcn_exp2f((M1 - G1) * L2E);
      const float S0 = get_tag<0xFFu>(mine.z) * e0 + get_tag<0xFFu>(oth.z) * __builtin_amdgcn_exp2f((om0 - G0) * L2E);
      const float S1 = get_tag<0xFFu>(mine.w) * e1 + get_tag<0xFFu>(oth.w) * __builtin_amdgcn_exp2f((om1 - G1) * L2E);
      if (lane == 0) { *(f32x2*)(lf + L_ST / 4 + 48) = f32x2{e0 / S0, e1 / S1}; asm volatile("s_waitcnt lgkmcnt(0)" ::: "memory"); lds_st(L_FL + 4 * F_FAC, 1u); }
      STAMP(5);
    };
#pragma unroll
    for (int ks = 8; ks < 16; ++ks) {
      const u32x4 pf = pfrag(ks);
      const int tl = 2 * ks + 1;
      wait_word(F_LANDED, PV + 4 * (tl + 1) + ((role == ROLE_G && tl > 32 - GEARLY) ? 4 * (tl - (32 - GEARLY)) : 0));
      acc = mfma(vfrag((ks - 2) * 8192, cw), pf, acc);
      if (ks >= 9 && ks <= 13) try_stats(false);
      if (ks == 14) { try_stats(true); wait_word(F_FAC, 1u); }
    }
    const float fac = lf[L_ST / 4 + 48 + (lane & 1)];
    asm volatile("s_waitcnt lgkmcnt(0)" ::: "memory");
    if (lane == 0) lds_st(L_FL + 4 * (F_VDONE + cw), 2u);
    if (c16 < 2)
      st16c(rs, WS_OS + (((j * 8 + b) * 2 + half) * 256 + c16 * 128 + 16 * cw + 4 * g4) * 4,
            u32x4{put_tag<0xFFu>(acc.x * fac, T8), put_tag<0xFFu>(acc.y * fac, T8), put_tag<0xFFu>(acc.z * fac, T8), put_tag<0xFFu>(acc.w * fac, T8)});
  }
  auto gather4 = [&](u32 v) -> u32x4 {
    return u32x4{v, (u32)__builtin_amdgcn_mov_dpp((int)v, 0x55, 0xf, 0xf, true), (u32)__builtin_amdgcn_mov_dpp((int)v, 0xAA, 0xf, 0xf, true),
                 (u32)__builtin_amdgcn_mov_dpp((int)v, 0xFF, 0xf, 0xf, true)};
  };
  const unsigned long long to = now();
  STAMP(7);
  PMARK(7);

  auto acc_to_lds = [&](int base, const f32x4 (&d)[4]) {
    if (c16 < 8) {
#pragma unroll
      for (int e = 0; e < 4; ++e) *(f32x4*)(lf + base / 4 + (cw * 8 + c16) * RS + 16 * e + 4 * g4) = d[e];
    }
  };
  auto publish_partial = [&](int base, int jw, int cg) {
    float sv[8];
#pragma unroll
    for (int ww = 0; ww < 8; ++ww) sv[ww] = lf[L_RED_W / 4 + (ww * 8 + cw) * RS + lane];
    const float sum = ((sv[0] + sv[1]) + (sv[2] + sv[3])) + ((sv[4] + sv[5]) + (sv[6] + sv[7]));
    const u32x4 o = gather4(put_tag<0xFFu>(sum, T8));
    if ((lane & 3) == 0) st16c(rs, base + (((cw * 9 + cg) * 16 + jw) * 64 + lane) * 4, o);
  };

  if (role == ROLE_W) {
    const int jw = (rank - 72) / 9, cg = (rank - 72) % 9;
    u32x4 f[4];
    wait_word(F_LANDED, PW + 32);
    tile_frags_lds(lds, cw * 4096, lane, f);
    {
      const int off = WS_OS + ((jw * 8 + (lane & 7)) * 512 + 32 * cw + 8 * g4) * 4;
      u32x4 a0 = {0u, 0u, 0u, 0u}, a1 = a0, b0 = a0, b1 = a0;
      if (c16 < 8)
        POLL((a0 = ld16c(rs, off), a1 = ld16c(rs, off + 16), b0 = ld16c(rs, off + 1024), b1 = ld16c(rs, off + 1040),
              tag_ok<0xFFu>(a0, T8) & tag_ok<0xFFu>(a1, T8) & tag_ok<0xFFu>(b0, T8) & tag_ok<0xFFu>(b1, T8)));
      STAMP(8);
      RSTAMP(0);
#define S2(x, y) (get_tag<0xFFu>(x) + get_tag<0xFFu>(y))
      const u32x4 a = {pack2(S2(a0.x, b0.x), S2(a0.y, b0.y)), pack2(S2(a0.z, b0.z), S2(a0.w, b0.w)), pack2(S2(a1.x, b1.x), S2(a1.y, b1.y)), pack2(S2(a1.z, b1.z), S2(a1.w, b1.w))};
#undef S2
      f32x4 d[4];
#pragma unroll
      for (int e = 0; e < 4; ++e) d[e] = mfma(f[e], a, f32x4{0.f, 0.f, 0.f, 0.f});
      acc_to_lds(L_RED_W, d);
    }
    wg_barrier();
    publish_partial(WS_PW, jw, cg);
    const unsigned long long tw = now();
    STAMP(9);
    u32x4 f2[4];
    {
      const unsigned char* a = (const unsigned char*)(p.wd + (size_t)(288 * jw + 32 * cw) * DM + 64 * cg);
      const u32 o0 = rowoff(0, DM * 2, lane), o1 = rowoff(1, DM * 2, lane);
      tile_dma<-1>(a, DM * 2, o0, o1, (8 + cw) * 4096);
      if (cw == 0) tile_dma<-1>(a + 256 * DM * 2, DM * 2, o0, o1, 16 * 4096);
      waitvm<0>();
      tile_frags_lds(lds, (8 + cw) * 4096, lane, f);
      if (cw == 0) tile_frags_lds(lds, 16 * 4096, lane, f2);
    }
    {
      const int off = WS_AS + ((lane & 7) * FF + 288 * jw + 32 * cw + 8 * g4) * 4;
      u32x4 R[4] = {};
      not_before(tw, GATE_ACT);
      if (c16 < 8) {
        if (cw == 0) poll4(rs, off, off + 16, off + 1024, off + 1040, T16, 0xFFFFu, R);
        else poll2(rs, off, T16, 0xFFFFu, R[0], R[1]);
      }
      STAMP(12);
      const u32x4 a = afrag(R[0], R[1]);
      f32x4 d[4];
#pragma unroll
      for (int e = 0; e < 4; ++e) d[e] = mfma(f[e], a, f32x4{0.f, 0.f, 0.f, 0.f});
      if (cw == 0) {
        const u32x4 ab = afrag(R[2], R[3]);
#pragma unroll
        for (int e = 0; e < 4; ++e) d[e] = mfma(f2[e], ab, d[e]);
      }
      acc_to_lds(L_RED_W, d);
    }
    wg_barrier();
    publish_partial(WS_PD, jw, cg);
    STAMP(13);
  } else if (role == ROLE_G) {
    const int gx = rank;
    u32x4 f[5][4];
#pragma unroll
    for (int tl = 0; tl < 5; ++tl) {
      if (tl < 4 || cw < 4) {
        const int tau = cw + 8 * tl;
        const int n = tau < 16 ? tau : (tau >= 32 ? tau - 16 : tau + 4);
        wait_word(F_LANDED, tau < GEARLY ? PV + 4 * (32 - GEARLY + tau + 1) + 4 * (tau + 1) : PW + 4 * (n + 1));
        tile_frags_lds(lds, tau < 4 ? L_EXT + tau * 4096 : (tau - 4) * 4096, lane, f[tl]);
      }
    }
    wait_word(F_LANDED, PW + 144);
    STAMP(8);
    {
      const int off = WS_HS + (xcd * 8 + cw) * DM * 4;
      u32x4 h0, h1; u32 h2;
      not_before(to, GATE_H);
      poll3(rs, off + lane * 32, off + 2048 + lane * 4, T16, 0xFFFFu, h0, h1, h2);
      STAMP(10);
      float v[9]; bool valid[9];
#pragma unroll
      for (int e = 0; e < 4; ++e) { v[e] = bfhi(h0[e]); v[4 + e] = bfhi(h1[e]); }
      v[8] = bfhi(h2);
#pragma unroll
      for (int e = 0; e < 9; ++e) valid[e] = true;
      const float pivot = __uint_as_float(__builtin_amdgcn_readfirstlane(h0.x) & 0xFFFF0000u);
      const RowStats st = row_stats<9>(v, valid, pivot);
      u32* hn = lu + L_HN / 4 + cw * HN_STRIDE;
      *(u32x4*)(hn + 4 * lane) = u32x4{pack2(ln_apply(v[0], st), ln_apply(v[1], st)), pack2(ln_apply(v[2], st), ln_apply(v[3], st)),
                                       pack2(ln_apply(v[4], st), ln_apply(v[5], st)), pack2(ln_apply(v[6], st), ln_apply(v[7], st))};
      ((__bf16*)(hn + 256))[lane] = (__bf16)ln_apply(v[8], st);
    }
    STAMP(9);
    wg_barrier();
    STAMP(12);
    {
      f32x4 d[4];
      const u32* hn = lu + L_HN / 4 + (lane & 7) * HN_STRIDE + 16 * (cw >> 1) + 4 * g4;
      u32x4 a[5];
#pragma unroll
      for (int tl = 0; tl < 5; ++tl) a[tl] = *(const u32x4*)(hn + (tl < 4 || cw < 4 ? 64 * tl : 0));
      SB();
#pragma unroll
      for (int e = 0; e < 4; ++e) d[e] = mfma(f[0][e], a[0], f32x4{0.f, 0.f, 0.f, 0.f});
#pragma unroll
      for (int tl = 1; tl < 5; ++tl) {
        if (tl < 4 || cw < 4) {
#pragma unroll
          for (int e = 0; e < 4; ++e) d[e] = mfma(f[tl][e], a[tl], d[e]);
        }
      }
      STAMP(13);
      acc_to_lds(0, d);
    }
    wg_barrier();
    STAMP(3);
    {
      float gv[4], uv[4];
#pragma unroll
      for (int ww = 0; ww < 4; ++ww) { gv[ww] = lf[(2 * ww * 8 + cw) * RS + lane]; uv[ww] = lf[((2 * ww + 1) * 8 + cw) * RS + lane]; }
      const float gg = rbf((gv[0] + gv[1]) + (gv[2] + gv[3])), uu = rbf((uv[0] + uv[1]) + (uv[2] + uv[3]));
      const float sl = rbf(gg * __builtin_amdgcn_rcpf(1.0f + __builtin_amdgcn_exp2f(gg * -1.4426950408889634f)));
      const u32x4 o = gather4(bfbits_hi(sl * uu) | T16);
      if ((lane & 3) == 0) st16c(rs, WS_AS + (cw * FF + 64 * gx + lane) * 4, o);
    }
    STAMP(11);
  } else {
    const int f = rank - 216;
    const int job = 2 * f + (cw >> 2);
    if ((cw & 3) == 0 && job < 72) {
      const int jb = job / 9, cg = job % 9;
      const u32x2 xq = *(const GAS u32x2*)(p.x + jb * DM + 64 * cg + 4 * c16);
      const u32x4 wr0 = *(const GAS u32x4*)(p.wr + 8 * lane);
      const u16 wr1 = ((const GAS u16*)p.wr)[512 + lane];
      auto gather16 = [&](int base) -> f32x4 {
        const int off = base + (((jb * 9 + cg) * 16 + g4) * 64 + 4 * c16) * 4;
        u32x4 R[4];
        poll4(rs, off, off + 1024, off + 2048, off + 3072, T8, 0xFFu, R);
        const u32x4 q0 = R[0], q1 = R[1], q2 = R[2], q3 = R[3];
        f32x4 sum;
#pragma unroll
        for (int e = 0; e < 4; ++e) {
          float t = (get_tag<0xFFu>(q0[e]) + get_tag<0xFFu>(q1[e])) + (get_tag<0xFFu>(q2[e]) + get_tag<0xFFu>(q3[e]));
          t = swap16_add(t, t); t = swap32_add(t, t);
          sum[e] = t;
        }
        return sum;
      };
      STAMP(3);
      not_before(to, GATE_PW);
      const f32x4 at = gather16(WS_PW);
      STAMP(8);
      PMARK(8);
      RSTAMP(0);
      f32x4 hv;
      hv[0] = rbf(bflo(xq.x) + rbf(at[0])); hv[1] = rbf(bfhi(xq.x) + rbf(at[1]));
      hv[2] = rbf(bflo(xq.y) + rbf(at[2])); hv[3] = rbf(bfhi(xq.y) + rbf(at[3]));
      {
        const u32x4 hq = {__float_as_uint(hv[0]) | T16, __float_as_uint(hv[1]) | T16, __float_as_uint(hv[2]) | T16, __float_as_uint(hv[3]) | T16};
        const int o = WS_HS + ((g4 * 8 + jb) * DM + 64 * cg + 4 * c16) * 4;
        st16c(rs, o, hq); st16c(rs, o + 4 * 8 * DM * 4, hq);
      }
      const unsigned long long th = now();
      STAMP(9);
      RowStats st; float sig;
      {
        const int off = WS_HS + (xcd * 8 + jb) * DM * 4;
        u32x4 h0, h1; u32 h2;
        {
          unsigned spins = 0;
#pragma clang loop unroll(disable)
          for (;;) {
            h0 = ld16c(rs, off + lane * 32); h1 = ld16c(rs, off + lane * 32 + 16); h2 = ld4c(rs, off + 2048 + lane * 4);
            if (__all(tag_ok<0xFFFFu>(h0, T16) & tag_ok<0xFFFFu>(h1, T16) & ((h2 & 0xFFFFu) == T16)) || ++spins > SPIN_CAP) break;
            __builtin_amdgcn_s_sleep(2);
          }
        }
        float v[9]; bool valid[9];
#pragma unroll
        for (int e = 0; e < 4; ++e) { v[e] = bfhi(h0[e]); v[4 + e] = bfhi(h1[e]); }
        v[8] = bfhi(h2);
#pragma unroll
        for (int e = 0; e < 9; ++e) valid[e] = true;
        const float pivot = __uint_as_float(__builtin_amdgcn_readfirstlane(h0.x) & 0xFFFF0000u);
        st = row_stats<9>(v, valid, pivot);
        float r = rbf(ln_apply(v[8], st)) * bflo((u32)wr1);
#pragma unroll
        for (int e = 0; e < 8; ++e) r = fmaf(rbf(ln_apply(v[e], st)), (e & 1) ? bfhi(wr0[e >> 1]) : bflo(wr0[e >> 1]), r);
        r = rbf(wave_sum(r));
        sig = rbf(__builtin_amdgcn_rcpf(1.0f + __builtin_amdgcn_exp2f(r * -1.4426950408889634f)));
      }
      float hn[4];
#pragma unroll
      for (int e = 0; e < 4; ++e) hn[e] = rbf(ln_apply(hv[e], st));
      STAMP(10);
      not_before(th, GATE_PD);
      const f32x4 y = gather16(WS_PD);
      STAMP(12);
      RSTAMP(1);
      float o[4];
#pragma unroll
      for (int e = 0; e < 4; ++e) o[e] = hv[e] + rbf(hn[e] + rbf(rbf(y[e]) * sig));
      if (lane < 16) *(GAS u32x2*)(p.out + jb * DM + 64 * cg + 4 * lane) = u32x2{pack2(o[0], o[1]), pack2(o[2], o[3])};
      PMARK(9);
      if (job == 0 && lane == 0) __hip_atomic_store((GAS u32*)(p.ws + WS_EPOCH), T, __ATOMIC_RELAXED, __HIP_MEMORY_SCOPE_AGENT);
      STAMP(13);
    }
  }
  STAMP(14);
  STAMP_FLUSH();
}

}

extern "C" void kernel_launch(void* const* d_in, const int* in_sizes, int n_in, void* d_out, int out_size, void* d_ws, size_t ws_size,
                              hipStream_t stream) {
  Params p;
  p.x = (const u16*)d_in[0];
  p.kc = (const u16*)d_in[1];
  p.vc = (const u16*)d_in[2];
  p.wq = (const u16*)d_in[3];
  p.wo = (const u16*)d_in[4];
  p.wr = (const u16*)d_in[6];
  p.wg = (const u16*)d_in[7];
  p.wu = (const u16*)d_in[8];
  p.wd = (const u16*)d_in[9];
  p.out = (u16*)d_out;
  p.ws = (unsigned char*)d_ws;
  (void)hipFuncSetAttribute((const void*)fused_kernel, hipFuncAttributeMaxDynamicSharedMemorySize, LDS_BYTES);
  hipLaunchKernelGGL(fused_kernel, dim3(NWG), dim3(NTHR), LDS_BYTES, stream, p);
}
```

```cpp
#include <hip/hip_runtime.h>
#include <stdint.h>

typedef unsigned u32;
typedef unsigned short u16;
typedef u32 u32x4 __attribute__((ext_vector_type(4)));
typedef u32 u32x2 __attribute__((ext_vector_type(2)));
typedef float f32x4 __attribute__((ext_vector_type(4)));
typedef float f32x2 __attribute__((ext_vector_type(2)));
typedef short s16x4 __attribute__((ext_vector_type(4)));
typedef __bf16 bf16x8 __attribute__((ext_vector_type(8)));
typedef __bf16 bf16x2 __attribute__((ext_vector_type(2)));
#define GAS __attribute__((address_space(1)))
#define LAS __attribute__((address_space(3)))

#define STAMPS 0
#define LWIN 24
#define GWIN 40
#define QDELAY 32
#define XFIRST 0
#define GEARLY 4
#define GATE_PW 90
#define GATE_H 150
#define GATE_ACT 220
#define GATE_PD 270
#define PSTAG 6
#define PSLEEP 1
#define QS_T4 1
#define DYNROLE 1
#define OS_B 1

namespace {

constexpr int DM = 576, HKV = 16, SK = 4096, DH = 128, WIN = 1024, NQ = 4096, FF = 4608;
constexpr int NWG = 256, NTHR = 576;

constexpr int WS_EPOCH = 0;
constexpr int WS_RANK = 2048;
constexpr int WS_QS = 4096;
constexpr int WS_SC = WS_QS + 16 * 8 * 4 * 256 * 4;
constexpr int WS_OS = WS_SC + 256 * 128;
constexpr int WS_PW = WS_OS + 16 * 8 * 2 * 256 * 4;
constexpr int HREP = 8;
constexpr int WS_HS = WS_PW + 8 * 9 * 16 * 64 * 4;
constexpr int WS_AS = WS_HS + HREP * 8 * 576 * 4;
constexpr int WS_PD = WS_AS + 8 * 4608 * 4;
constexpr int WS_END = WS_PD + 8 * 9 * 16 * 64 * 4;
constexpr int WS_STAMP = (WS_END + 4095) / 4096 * 4096;
constexpr int AUX_SC1 = 16;
constexpr unsigned SPIN_CAP = 1u << 16, LSPIN_CAP = 1u << 22;

constexpr int L_EXT = 128 * 1024;
constexpr int L_FL = 144 * 1024;
constexpr int L_ST = L_FL + 128;
constexpr int L_P = L_ST + 256;
constexpr int L_QSUM = L_P + 2048;
constexpr int L_Q = L_QSUM + 8192;
constexpr int L_HN = L_P;
constexpr int HN_STRIDE = 292;
constexpr int RS = 68;
constexpr int L_RED_W = 112 * 1024;
constexpr int LDS_BYTES = 160 * 1024;
static_assert(L_Q + 512 <= LDS_BYTES && L_HN + 8 * HN_STRIDE * 4 <= LDS_BYTES, "LDS");
enum { F_LANDED = 0, F_QPUB = 1, F_BAR = 2, F_QRDY = 3, F_RANK = 4, F_FAC = 5, F_KDONE = 8, F_VDONE = 16 };

struct Params {
  const u16 *x, *kc, *vc, *wq, *wo, *wr, *wg, *wu, *wd;
  u16* out;
  unsigned char* ws;
};

__device__ __forceinline__ float bflo(u32 d) { return __uint_as_float(d << 16); }
__device__ __forceinline__ float bfhi(u32 d) { return __uint_as_float(d & 0xFFFF0000u); }
__device__ __forceinline__ float rbf(float f) { return (float)(__bf16)f; }
__device__ __forceinline__ u32 bfbits_hi(float f) { return __float_as_uint(rbf(f)); }
__device__ __forceinline__ u32 pack2(float lo, float hi) {
  bf16x2 v = {(__bf16)lo, (__bf16)hi};
  return __builtin_bit_cast(u32, v);
}
template <int CTRL> __device__ __forceinline__ float dpp(float x) {
  return __uint_as_float((u32)__builtin_amdgcn_mov_dpp((int)__float_as_uint(x), CTRL, 0xf, 0xf, true));
}
constexpr int XOR1 = 0xB1, XOR2 = 0x4E, XOR7 = 0x141, XOR8 = 0x128;
__device__ __forceinline__ float swap32_add(float a, float b) {
  auto r = __builtin_amdgcn_permlane32_swap(__float_as_uint(a), __float_as_uint(b), false, false);
  return __uint_as_float(r[0]) + __uint_as_float(r[1]);
}
__device__ __forceinline__ float swap16_add(float a, float b) {
  auto r = __builtin_amdgcn_permlane16_swap(__float_as_uint(a), __float_as_uint(b), false, false);
  return __uint_as_float(r[0]) + __uint_as_float(r[1]);
}
__device__ __forceinline__ float wave_sum(float x) {
  x += dpp<XOR1>(x); x += dpp<XOR2>(x); x += dpp<XOR7>(x); x += dpp<XOR8>(x);
  x = swap16_add(x, x); x = swap32_add(x, x);
  return x;
}
__device__ __forceinline__ float fmax_(float a, float b) { return __builtin_amdgcn_fmed3f(a, b, INFINITY); }
__device__ __forceinline__ float wave_max(float x) {
  x = fmax_(x, dpp<XOR1>(x)); x = fmax_(x, dpp<XOR2>(x)); x = fmax_(x, dpp<XOR7>(x)); x = fmax_(x, dpp<XOR8>(x));
  auto r = __builtin_amdgcn_permlane16_swap(__float_as_uint(x), __float_as_uint(x), false, false);
  x = fmax_(__uint_as_float(r[0]), __uint_as_float(r[1]));
  auto t = __builtin_amdgcn_permlane32_swap(__float_as_uint(x), __float_as_uint(x), false, false);
  return fmax_(__uint_as_float(t[0]), __uint_as_float(t[1]));
}
__device__ __forceinline__ f32x4 mfma(u32x4 a, u32x4 b, f32x4 c) {
  return __builtin_amdgcn_mfma_f32_16x16x32_bf16(__builtin_bit_cast(bf16x8, a), __builtin_bit_cast(bf16x8, b), c, 0, 0, 0);
}
#define SB() __builtin_amdgcn_sched_barrier(0)
template <int N> __device__ __forceinline__ void waitvm() { asm volatile("s_waitcnt vmcnt(%0)" ::"n"(N) : "memory"); }
typedef __amdgpu_buffer_rsrc_t rsrc_t;
__device__ __forceinline__ u32x4 ld16c(rsrc_t rs, int off) { return __builtin_amdgcn_raw_buffer_load_b128(rs, off, 0, AUX_SC1); }
__device__ __forceinline__ u32 ld4c(rsrc_t rs, int off) { return __builtin_amdgcn_raw_buffer_load_b32(rs, off, 0, AUX_SC1); }
__device__ __forceinline__ void st16c(rsrc_t rs, int off, u32x4 v) { __builtin_amdgcn_raw_buffer_store_b128(v, rs, off, 0, AUX_SC1); }
template <u32 M> __device__ __forceinline__ bool tag_ok(u32x4 v, u32 t) { return ((((v.x ^ t) | (v.y ^ t)) | ((v.z ^ t) | (v.w ^ t))) & M) == 0u; }
template <u32 M> __device__ __forceinline__ u32 put_tag(float f, u32 t) { return ((__float_as_uint(f) + (M + 1) / 2) & ~M) | t; }
template <u32 M> __device__ __forceinline__ float get_tag(u32 d) { return __uint_as_float(d & ~M); }
#define POLL_COUNT(n)
#define POLL(cond_load) do { unsigned spins_ = 0; _Pragma("clang loop unroll(disable)") while (!(cond_load) && ++spins_ < SPIN_CAP) __builtin_amdgcn_s_sleep(PSLEEP); POLL_COUNT(spins_); } while (0)

struct PollRegs { u32x4 a0, a1, a2, a3, b0, b1, b2, b3; };
#define PV_OUT(p) "=&{v[128:131]}"(p.a0), "=&{v[132:135]}"(p.a1), "=&{v[136:139]}"(p.a2), "=&{v[140:143]}"(p.a3), \
                  "=&{v[144:147]}"(p.b0), "=&{v[148:151]}"(p.b1), "=&{v[152:155]}"(p.b2), "=&{v[156:159]}"(p.b3)
#define PV_CLOB "v160", "v161", "v162", "v163", "vcc", "scc", "memory"
__device__ __forceinline__ void poll_done(const PollRegs& p) {
  asm volatile("s_waitcnt vmcnt(0)" ::"{v[128:131]}"(p.a0), "{v[132:135]}"(p.a1), "{v[136:139]}"(p.a2), "{v[140:143]}"(p.a3),
               "{v[144:147]}"(p.b0), "{v[148:151]}"(p.b1), "{v[152:155]}"(p.b2), "{v[156:159]}"(p.b3) : "memory");
}
#define PV_LD4(r0, r1, r2, r3, r4, r5, r6, r7) \
  "buffer_load_dwordx4 v[" r0 ":" r1 "], %[o0], %[rs], 0 offen sc1\n\t" "buffer_load_dwordx4 v[" r2 ":" r3 "], %[o1], %[rs], 0 offen sc1\n\t" \
  "buffer_load_dwordx4 v[" r4 ":" r5 "], %[o2], %[rs], 0 offen sc1\n\t" "buffer_load_dwordx4 v[" r6 ":" r7 "], %[o3], %[rs], 0 offen sc1\n\t"
#define PV_X4(t, a, b, c, d) "v_xor_b32 " t ", %[T], v" a "\n\t" "v_bitop3_b32 " t ", " t ", v" b ", %[T] bitop3:0xf6\n\t" "v_bitop3_b32 " t ", " t ", v" c ", %[T] bitop3:0xf6\n\t" "v_bitop3_b32 " t ", " t ", v" d ", %[T] bitop3:0xf6\n\t"
#define PV_TEST "v_and_b32 v160, %[M], v160\n\tv_cmp_ne_u32 vcc, 0, v160\n\ts_cmp_eq_u64 vcc, 0\n\t"
#define PV_MOV2(o, a, b) "v_mov_b64 " o ", v[" a ":" b "]\n\t"
__device__ __forceinline__ void poll4(PollRegs& p, rsrc_t rs, int o0, int o1, int o2, int o3, u32 T, u32 M, u32x4 (&R)[4]) {
  u32x2 r0, r1, r2, r3, r4, r5, r6, r7; u32 cnt = SPIN_CAP;
  asm volatile(
      PV_LD4("128", "131", "132", "135", "136", "139", "140", "143")
      "s_sleep %[stag]\n"
      "1:\n\t"
      PV_LD4("144", "147", "148", "151", "152", "155", "156", "159")
      "s_waitcnt vmcnt(4)\n\t"
      PV_X4("v160", "128", "129", "130", "131") PV_X4("v161", "132", "133", "134", "135") PV_X4("v162", "136", "137", "138", "139") PV_X4("v163", "140", "141", "142", "143")
      "v_or3_b32 v160, v160, v161, v162\n\tv_or_b32 v160, v160, v163\n\t" PV_TEST
      "s_cbranch_scc1 7f\n\t"
      PV_LD4("128", "131", "132", "135", "136", "139", "140", "143")
      "s_waitcnt vmcnt(4)\n\t"
      PV_X4("v160", "144", "145", "146", "147") PV_X4("v161", "148", "149", "150", "151") PV_X4("v162", "152", "153", "154", "155") PV_X4("v163", "156", "157", "158", "159")
      "v_or3_b32 v160, v160, v161, v162\n\tv_or_b32 v160, v160, v163\n\t" PV_TEST
      "s_cbranch_scc1 8f\n\t"
      "s_sub_u32 %[cnt], %[cnt], 1\n\ts_cbranch_scc0 1b\n\t"
      "s_waitcnt vmcnt(0)\n"
      "7:\n\t"
      PV_MOV2("%[r0]", "128", "129") PV_MOV2("%[r1]", "130", "131") PV_MOV2("%[r2]", "132", "133") PV_MOV2("%[r3]", "134", "135")
      PV_MOV2("%[r4]", "136", "137") PV_MOV2("%[r5]", "138", "139") PV_MOV2("%[r6]", "140", "141") PV_MOV2("%[r7]", "142", "143")
      "s_branch 9f\n"
      "8:\n\t"
      PV_MOV2("%[r0]", "144", "145") PV_MOV2("%[r1]", "146", "147") PV_MOV2("%[r2]", "148", "149") PV_MOV2("%[r3]", "150", "151")
      PV_MOV2("%[r4]", "152", "153") PV_MOV2("%[r5]", "154", "155") PV_MOV2("%[r6]", "156", "157") PV_MOV2("%[r7]", "158", "159")
      "9:\n\t"
      : [r0] "=&v"(r0), [r1] "=&v"(r1), [r2] "=&v"(r2), [r3] "=&v"(r3), [r4] "=&v"(r4), [r5] "=&v"(r5), [r6] "=&v"(r6), [r7] "=&v"(r7), [cnt] "+s"(cnt), PV_OUT(p)
      : [o0] "v"(o0), [o1] "v"(o1), [o2] "v"(o2), [o3] "v"(o3), [rs] "s"(rs), [T] "s"(T), [M] "s"(M), [stag] "n"(PSTAG)
      : PV_CLOB);
  R[0] = u32x4{r0.x, r0.y, r1.x, r1.y}; R[1] = u32x4{r2.x, r2.y, r3.x, r3.y}; R[2] = u32x4{r4.x, r4.y, r5.x, r5.y}; R[3] = u32x4{r6.x, r6.y, r7.x, r7.y};
}
#define PV_LD2(r0, r1, r2, r3) \
  "buffer_load_dwordx4 v[" r0 ":" r1 "], %[o0], %[rs], 0 offen sc1\n\t" "buffer_load_dwordx4 v[" r2 ":" r3 "], %[o0], %[rs], 0 offen offset:16 sc1\n\t"
__device__ __forceinline__ void poll2(PollRegs& p, rsrc_t rs, int o0, u32 T, u32 M, u32x4& a, u32x4& b) {
  u32x2 r0, r1, r2, r3; u32 cnt = SPIN_CAP;
  asm volatile(
      PV_LD2("128", "131", "132", "135")
      "s_sleep %[stag]\n"
      "1:\n\t"
      PV_LD2("144", "147", "148", "151")
      "s_waitcnt vmcnt(2)\n\t"
      PV_X4("v160", "128", "129", "130", "131") PV_X4("v161", "132", "133", "134", "135") "v_or_b32 v160, v160, v161\n\t" PV_TEST
      "s_cbranch_scc1 7f\n\t"
      PV_LD2("128", "131", "132", "135")
      "s_waitcnt vmcnt(2)\n\t"
      PV_X4("v160", "144", "145", "146", "147") PV_X4("v161", "148", "149", "150", "151") "v_or_b32 v160, v160, v161\n\t" PV_TEST
      "s_cbranch_scc1 8f\n\t"
      "s_sub_u32 %[cnt], %[cnt], 1\n\ts_cbranch_scc0 1b\n\t"
      "s_waitcnt vmcnt(0)\n"
      "7:\n\t"
      PV_MOV2("%[r0]", "128", "129") PV_MOV2("%[r1]", "130", "131") PV_MOV2("%[r2]", "132", "133") PV_MOV2("%[r3]", "134", "135")
      "s_branch 9f\n"
      "8:\n\t"
      PV_MOV2("%[r0]", "144", "145") PV_MOV2("%[r1]", "146", "147") PV_MOV2("%[r2]", "148", "149") PV_MOV2("%[r3]", "150", "151")
      "9:\n\t"
      : [r0] "=&v"(r0), [r1] "=&v"(r1), [r2] "=&v"(r2), [r3] "=&v"(r3), [cnt] "+s"(cnt), PV_OUT(p)
      : [o0] "v"(o0), [rs] "s"(rs), [T] "s"(T), [M] "s"(M), [stag] "n"(PSTAG)
      : PV_CLOB);
  a = u32x4{r0.x, r0.y, r1.x, r1.y}; b = u32x4{r2.x, r2.y, r3.x, r3.y};
}
#define PV_LD3(r0, r1, r2, r3, r4) \
  "buffer_load_dwordx4 v[" r0 ":" r1 "], %[o0], %[rs], 0 offen sc1\n\t" "buffer_load_dwordx4 v[" r2 ":" r3 "], %[o0], %[rs], 0 offen offset:16 sc1\n\t" \
  "buffer_load_dword v" r4 ", %[o1], %[rs], 0 offen sc1\n\t"
__device__ __forceinline__ void poll3(PollRegs& p, rsrc_t rs, int o0, int o1, u32 T, u32 M, u32x4& a, u32x4& b, u32& c) {
  u32x2 r0, r1, r2, r3; u32 r4, cnt = SPIN_CAP;
  asm volatile(
      PV_LD3("128", "131", "132", "135", "136")
      "s_sleep %[stag]\n"
      "1:\n\t"
      PV_LD3("144", "147", "148", "151", "152")
      "s_waitcnt vmcnt(3)\n\t"
      PV_X4("v160", "128", "129", "130", "131") PV_X4("v161", "132", "133", "134", "135") "v_xor_b32 v162, %[T], v136\n\t"
      "v_or3_b32 v160, v160, v161, v162\n\t" PV_TEST
      "s_cbranch_scc1 7f\n\t"
      PV_LD3("128", "131", "132", "135", "136")
      "s_waitcnt vmcnt(3)\n\t"
      PV_X4("v160", "144", "145", "146", "147") PV_X4("v161", "148", "149", "150", "151") "v_xor_b32 v162, %[T], v152\n\t"
      "v_or3_b32 v160, v160, v161, v162\n\t" PV_TEST
      "s_cbranch_scc1 8f\n\t"
      "s_sub_u32 %[cnt], %[cnt], 1\n\ts_cbranch_scc0 1b\n\t"
      "s_waitcnt vmcnt(0)\n"
      "7:\n\t"
      PV_MOV2("%[r0]", "128", "129") PV_MOV2("%[r1]", "130", "131") PV_MOV2("%[r2]", "132", "133") PV_MOV2("%[r3]", "134", "135") "v_mov_b32 %[r4], v136\n\t"
      "s_branch 9f\n"
      "8:\n\t"
      PV_MOV2("%[r0]", "144", "145") PV_MOV2("%[r1]", "146", "147") PV_MOV2("%[r2]", "148", "149") PV_MOV2("%[r3]", "150", "151") "v_mov_b32 %[r4], v152\n\t"
      "9:\n\t"
      : [r0] "=&v"(r0), [r1] "=&v"(r1), [r2] "=&v"(r2), [r3] "=&v"(r3), [r4] "=&v"(r4), [cnt] "+s"(cnt), PV_OUT(p)
      : [o0] "v"(o0), [o1] "v"(o1), [rs] "s"(rs), [T] "s"(T), [M] "s"(M), [stag] "n"(PSTAG)
      : PV_CLOB);
  a = u32x4{r0.x, r0.y, r1.x, r1.y}; b = u32x4{r2.x, r2.y, r3.x, r3.y}; c = r4;
}

__device__ __forceinline__ unsigned long long now() { return __builtin_amdgcn_s_memrealtime(); }
__device__ __forceinline__ void not_before(unsigned long long t0, unsigned ticks) {
  while (now() - t0 < ticks) __builtin_amdgcn_s_sleep(2);
}
__device__ __forceinline__ u32 lds_ld(u32 addr) {
  u32 v;
  asm volatile("ds_read_b32 %0, %1\n\ts_waitcnt lgkmcnt(0)" : "=v"(v) : "v"(addr) : "memory");
  return v;
}
__device__ __forceinline__ void lds_st(u32 addr, u32 v) { asm volatile("ds_write_b32 %0, %1" ::"v"(addr), "v"(v) : "memory"); }
__device__ __forceinline__ void wait_word(int word, u32 want) {
  unsigned spins = 0;
#pragma clang loop unroll(disable)
  while ((u32)__builtin_amdgcn_readfirstlane(lds_ld(L_FL + 4 * word)) < want)
    if (++spins > LSPIN_CAP) break;
}
__device__ __forceinline__ void wait_word_sleep(int word, u32 want) {
  unsigned spins = 0;
#pragma clang loop unroll(disable)
  while ((u32)__builtin_amdgcn_readfirstlane(lds_ld(L_FL + 4 * word)) < want) {
    __builtin_amdgcn_s_sleep(2);
    if (++spins > LSPIN_CAP) break;
  }
}
__device__ __forceinline__ void wait_all8(int word0, u32 want, int lane) {
  unsigned spins = 0;
#pragma clang loop unroll(disable)
  while (!__all(lds_ld(L_FL + 4 * (word0 + (lane & 7))) >= want)) {
    __builtin_amdgcn_s_sleep(1);
    if (++spins > LSPIN_CAP) break;
  }
}
__device__ __forceinline__ void cbar(u32 n, int lane) {
  asm volatile("s_waitcnt lgkmcnt(0)" ::: "memory");
  if (lane == 0) asm volatile("ds_add_u32 %0, %1" ::"v"((u32)(L_FL + 4 * F_BAR)), "v"(1u) : "memory");
  wait_word(F_BAR, 8u * n);
}
__device__ __forceinline__ void hw_barrier() { __builtin_amdgcn_sched_barrier(0); __builtin_amdgcn_s_barrier(); __builtin_amdgcn_sched_barrier(0); }
__device__ __forceinline__ void wg_barrier() {
  asm volatile("s_waitcnt lgkmcnt(0)" ::: "memory");
  hw_barrier();
  asm volatile("" ::: "memory");
}
__device__ __forceinline__ void glds16(const void* sbase, u32 voff, u32 lds_dst) {
  asm volatile("s_mov_b32 m0, %2\n\ts_nop 0\n\tglobal_load_lds_dwordx4 %0, %1 nt" ::"v"(voff), "s"(sbase), "s"(lds_dst) : "memory");
}
__device__ __forceinline__ int vswz(int row) { return ((row & 3) << 2) | ((row >> 2) & 3); }
__device__ __forceinline__ int swz(int row) { return ((row >> 1) & 1) | ((row >> 2) & 2); }
__device__ __forceinline__ u32 rowoff(int q, int stride, int lane) { const int row = 8 * q + (lane >> 3); return row * stride + (((lane & 7) ^ (swz(row) << 1)) & 7) * 16; }
template <int W> __device__ __forceinline__ void tile_dma(const unsigned char* a, int stride, u32 o0, u32 o1, u32 dst) {
  glds16(a, o0, dst); if (W >= 0) waitvm<W>();
  glds16(a, o1, dst + 1024); if (W >= 0) waitvm<W>();
  glds16(a + (size_t)16 * stride, o0, dst + 2048); if (W >= 0) waitvm<W>();
  glds16(a + (size_t)16 * stride, o1, dst + 3072); if (W >= 0) waitvm<W>();
}
__device__ __forceinline__ void tile_frags_lds(const unsigned char* lds, u32 base, int lane, u32x4 (&f)[4]) {
  const int g = lane >> 4, q = (lane & 15) >> 2, p = lane & 3;
#pragma unroll
  for (int e = 0; e < 4; ++e) {
    u32x2 h[2];
#pragma unroll
    for (int t = 0; t < 2; ++t) {
      const int row = 8 * g + 4 * t + q;
      const u32 a = base + row * 128 + (((2 * e + (p >> 1)) ^ (swz(row) << 1)) << 4) + 8 * (p & 1);
      h[t] = __builtin_bit_cast(u32x2, __builtin_amdgcn_ds_read_tr16_b64_v4i16((LAS s16x4*)(lds + a)));
    }
    f[e] = u32x4{h[0].x, h[0].y, h[1].x, h[1].y};
  }
}

#define ESTAMP(k) do { } while (0)
#define STAMP(k) do { } while (0)
#define STAMP_FLUSH() do { } while (0)

__device__ __forceinline__ u32x4 afrag(u32x4 a, u32x4 b) {
  u32x4 f;
  f.x = __builtin_amdgcn_perm(a.y, a.x, 0x07060302u);
  f.y = __builtin_amdgcn_perm(a.w, a.z, 0x07060302u);
  f.z = __builtin_amdgcn_perm(b.y, b.x, 0x07060302u);
  f.w = __builtin_amdgcn_perm(b.w, b.z, 0x07060302u);
  return f;
}

struct RowStats { float m, rs; };
template <int N> __device__ __forceinline__ RowStats row_stats(const float (&v)[N], const bool (&valid)[N], float pivot) {
  float s1[2] = {0.f, 0.f}, s2[2] = {0.f, 0.f};
#pragma unroll
  for (int e = 0; e < N; ++e) { const float d = valid[e] ? v[e] - pivot : 0.f; s1[e & 1] += d; s2[e & 1] = fmaf(d, d, s2[e & 1]); }
  const float m1 = wave_sum(s1[0] + s1[1]) * (1.0f / DM), m2 = wave_sum(s2[0] + s2[1]) * (1.0f / DM);
  const float var = rbf(fmax_(fmaf(-m1, m1, m2), 0.f));
  RowStats r;
  r.m = rbf(pivot + m1);
  r.rs = __builtin_amdgcn_rcpf(rbf(__builtin_amdgcn_sqrtf(rbf(var + 1e-5f))));
  return r;
}
__device__ __forceinline__ float ln_apply(float v, RowStats s) { return rbf(v - s.m) * s.rs; }

enum { ROLE_W = 0, ROLE_G = 1, ROLE_F = 2 };

__global__ void __launch_bounds__(NTHR, 3) fused_kernel(Params p) {
  extern __shared__ __attribute__((aligned(16))) unsigned char lds[];
  const int tid = threadIdx.x, lane = tid & 63, w = __builtin_amdgcn_readfirstlane(tid >> 6);
  const int wg = blockIdx.x, xcd = wg & 7, s = wg >> 3, j = xcd + 8 * (s >> 4), i = s & 15, b = i >> 1, half = i & 1;
  const size_t ubase = ((size_t)(b * HKV + j) * SK + (SK - WIN)) * DH;
  const int cq = i & 3, kp = i >> 2;
  const int ks0 = kp < 3 ? 4 * kp : 13, nt = kp < 2 ? 4 : 5;
  const u32 PK = 4 * nt, PV = PK + 128, PW = PK + 256;
  const int g4 = lane >> 4, c16 = lane & 15;
#define RSTAMP(k)
#define PMARK(n)
  STAMP(0);
  const int cw = (w + 7) & 7;
  u32 xr[5];
  u32x4 xa;
  auto load_x = [&]() {
    const GAS u32* xrow = (const GAS u32*)(p.x + cw * DM);
#pragma unroll
    for (int m = 0; m < 4; ++m) xr[m] = __builtin_nontemporal_load(xrow + lane + 64 * m);
    xr[4] = __builtin_nontemporal_load(xrow + (lane & 31) + 256);
    xa = __builtin_nontemporal_load((const GAS u32x4*)(p.x + (lane & 7) * DM + 32 * (ks0 + (cw < nt ? cw : 0)) + 8 * g4));
  };
  if (w == 0) {
    if (lane < 32) lds_st(L_FL + 4 * lane, 0u);
    asm volatile("s_waitcnt lgkmcnt(0)" ::: "memory");
  }
  hw_barrier();

  if (w == 0) {
    __builtin_amdgcn_s_setprio(3);
    u32 pub = 0;
#define PUBLISH(W) lds_st(L_FL + 4 * F_LANDED, pub - (W))
    {
      const unsigned char* a = (const unsigned char*)(p.wq + (size_t)32 * ks0 * NQ + 256 * j + 64 * cq);
      const u32 o0 = rowoff(0, NQ * 2, lane), o1 = rowoff(1, NQ * 2, lane);
#pragma unroll
      for (int t = 0; t < 4; ++t) tile_dma<-1>(a + (size_t)t * 32 * NQ * 2, NQ * 2, o0, o1, L_EXT + t * 4096);
      if (nt == 5) tile_dma<-1>(a + (size_t)4 * 32 * NQ * 2, NQ * 2, o0, o1, L_QSUM);
      pub = PK;
    }
    ESTAMP(8);
    {
      const unsigned char* kb = (const unsigned char*)(p.kc + ubase + (size_t)half * 512 * DH);
      u32 koff[4];
#pragma unroll
      for (int q = 0; q < 4; ++q) { const int key = 4 * q + g4; koff[q] = key * 256 + ((c16 ^ key) & 15) * 16; }
      auto kseg = [&](int t0, int t1) {
#pragma clang loop unroll(disable)
        for (int t = t0; t < t1; ++t) {
#pragma unroll
          for (int q = 0; q < 4; ++q) { glds16(kb + t * 4096, koff[q], (t * 4 + q) * 1024); waitvm<LWIN>(); }
          pub += 4;
          if (pub > LWIN) PUBLISH(LWIN);
        }
      };
      kseg(0, 32);
    }
    STAMP(1);
    wait_word_sleep(F_RANK, 1u);
    const int rank = (int)__builtin_amdgcn_readfirstlane(lds_ld(L_FL + 4 * F_RANK)) - 1;
    const int role = rank < 72 ? ROLE_G : (rank < 216 ? ROLE_W : ROLE_F);
    const u32 go0 = rowoff(0, FF * 2, lane), go1 = rowoff(1, FF * 2, lane);
    auto gtile = [&](int t) -> const unsigned char* { return (const unsigned char*)(((t & 1) ? p.wu : p.wg) + (size_t)(t >> 1) * 32 * FF + 64 * rank); };
    {
      const unsigned char* vb = (const unsigned char*)(p.vc + ubase + (size_t)half * 512 * DH);
      u32 voff[4];
#pragma unroll
      for (int q = 0; q < 4; ++q) { const int key = 4 * q + g4; voff[q] = key * 256 + ((c16 ^ vswz(key)) & 15) * 16; }
      wait_word(F_QPUB, 1u);
#pragma clang loop unroll(disable)
      for (int t = 0; t < 32; ++t) {
        if ((t & 7) == 4) wait_all8(F_KDONE, (t >> 3) + 1, lane);
        const u32 dst = t < 4 ? L_EXT + t * 4096 : (t - 4) * 4096;
#pragma unroll
        for (int q = 0; q < 4; ++q) { glds16(vb + t * 4096, voff[q], dst + q * 1024); waitvm<LWIN>(); }
        pub += 4; PUBLISH(LWIN);
        if (role == ROLE_G && t >= 32 - GEARLY) {
          const int tau = t - (32 - GEARLY);
          if (tau == 0) wait_all8(F_VDONE, 1, lane);
          tile_dma<LWIN>(gtile(tau), FF * 2, go0, go1, tau < 4 ? L_EXT + tau * 4096 : (tau - 4) * 4096);
          pub += 4; PUBLISH(LWIN);
        }
      }
    }
    STAMP(2);
    if (role == ROLE_W) {
      const int jw = (rank - 72) / 9, cg = (rank - 72) % 9;
      const unsigned char* a = (const unsigned char*)(p.wo + (size_t)256 * jw * DM + 64 * cg);
      const u32 o0 = rowoff(0, DM * 2, lane), o1 = rowoff(1, DM * 2, lane);
      wait_all8(F_VDONE, 1, lane);
#pragma clang loop unroll(disable)
      for (int t = 0; t < 8; ++t) { tile_dma<-1>(a + (size_t)t * 32 * DM * 2, DM * 2, o0, o1, t * 4096); pub += 4; }
    } else if (role == ROLE_G) {
      static_assert(GEARLY >= 1 && 80 - GWIN >= 4 * GEARLY - 4, "the last tile of V is published before the loader waits for its consumption");
#pragma clang loop unroll(disable)
      for (int n = GEARLY; n < 36; ++n) {
        if (n == 20) wait_all8(F_VDONE, 2, lane);
        const int t = n < 16 ? n : (n < 20 ? n + 16 : n - 4);
        tile_dma<GWIN>(gtile(t), FF * 2, go0, go1, t < 4 ? L_EXT + t * 4096 : (t - 4) * 4096);
        pub += 4;
        if (4 * (n - GEARLY) + 4 > GWIN - LWIN) PUBLISH(GWIN);
      }
    }
    STAMP(3);
#define DR(N) if ((role == ROLE_G ? GWIN : (role == ROLE_W ? LWIN + 32 : LWIN)) > N) { waitvm<N>(); PUBLISH(N); }
    static_assert(LWIN + 32 <= 60 && GWIN <= 60, "vmcnt");
    DR(56) DR(52) DR(48) DR(44) DR(40) DR(36) DR(32) DR(28) DR(24) DR(20) DR(16) DR(12) DR(8) DR(4) DR(0)
#undef DR
#undef PUBLISH
    STAMP(14);
    STAMP_FLUSH();
    return;
  }

  load_x();
  const int ct = tid - 64;
  const rsrc_t rs = __builtin_amdgcn_make_buffer_rsrc((void*)p.ws, 0, WS_END, 0x00020000);
  const u32 T = *(const __attribute__((address_space(4))) u32*)(p.ws + WS_EPOCH) + 1u;
  const u32 T16 = T & 0xFFFFu, T8 = T & 0xFFu, T4 = T & 0xFu;
  const u32 tstart = (u32)now();
  if (ct == 448) __builtin_amdgcn_raw_buffer_store_b64(u32x2{tstart, T}, rs, WS_RANK + wg * 8, 0, AUX_SC1);
  float* const lf = (float*)lds;
  u32* const lu = (u32*)lds;
  {
    ESTAMP(8);
    asm volatile("" :: "v"(xr[0]), "v"(xr[4]));
    ESTAMP(9);
    float v[10]; bool valid[10];
#pragma unroll
    for (int m = 0; m < 5; ++m) { valid[2 * m] = valid[2 * m + 1] = (m < 4) || (lane < 32); v[2 * m] = bflo(xr[m]); v[2 * m + 1] = bfhi(xr[m]); }
    const float pivot = __uint_as_float(__builtin_amdgcn_readfirstlane(xr[0]) << 16);
    const RowStats st = row_stats<10>(v, valid, pivot);
    if (lane == 0) *(f32x2*)(lf + L_ST / 4 + 2 * cw) = f32x2{st.m, st.rs};
    ESTAMP(10);
    cbar(1, lane);
    STAMP(1);
    PMARK(1);
  }
  if (cw < nt) {
    const f32x2 sv = *(const f32x2*)(lf + L_ST / 4 + 2 * (lane & 7));
    const RowStats st = {sv.x, sv.y};
    u32x4 a;
#pragma unroll
    for (int q = 0; q < 4; ++q) a[q] = pack2(ln_apply(bflo(xa[q]), st), ln_apply(bfhi(xa[q]), st));
    const u32 base = cw < 4 ? L_EXT + cw * 4096 : L_QSUM;
    wait_word(F_LANDED, 4 * (cw + 1));
    u32x4 f[4]; tile_frags_lds(lds, base, lane, f);
    f32x4 d[4];
#pragma unroll
    for (int e = 0; e < 4; ++e) d[e] = mfma(f[e], a, f32x4{0.f, 0.f, 0.f, 0.f});
    if (c16 < 8) {
#pragma unroll
      for (int e = 0; e < 4; ++e) *(f32x4*)(lf + base / 4 + c16 * RS + 16 * e + 4 * g4) = d[e];
    }
  }
  ESTAMP(11);
  cbar(2, lane);
  ESTAMP(12);
  {
    float sum = (lf[L_EXT / 4 + cw * RS + lane] + lf[L_EXT / 4 + 1024 + cw * RS + lane]) + (lf[L_EXT / 4 + 2048 + cw * RS + lane] + lf[L_EXT / 4 + 3072 + cw * RS + lane]);
    if (nt == 5) sum += lf[L_QSUM / 4 + cw * RS + lane];
    const u32 v = put_tag<0xFu>(sum, T4);
    const u32x4 o = {v, (u32)__builtin_amdgcn_mov_dpp((int)v, 0x55, 0xf, 0xf, true), (u32)__builtin_amdgcn_mov_dpp((int)v, 0xAA, 0xf, 0xf, true),
                     (u32)__builtin_amdgcn_mov_dpp((int)v, 0xFF, 0xf, 0xf, true)};
    if ((lane & 3) == 0) st16c(rs, WS_QS + ((((j * 8 + cw) * 4 + kp) * 256) + 64 * cq + lane) * 4, o);
  }
  cbar(3, lane);
  if (ct == 0) lds_st(L_FL + 4 * F_QPUB, 1u);
  STAMP(2);
  PMARK(2);
  if (cw >= 2 && cw < 6) {
    u32x4 qg;
    __builtin_amdgcn_s_sleep(QDELAY);
    {
      unsigned spins = 0;
#pragma clang loop unroll(disable)
      for (;;) {
        qg = ld16c(rs, WS_QS + (((j * 8 + b) * 4 + (cw - 2)) * 256 + 4 * lane) * 4);
        if (__all(tag_ok<0xFu>(qg, T4)) || ++spins > SPIN_CAP) break;
        __builtin_amdgcn_s_sleep(4);
      }
    }
    *(f32x4*)(lf + L_QSUM / 4 + (cw - 2) * 256 + 4 * lane) = f32x4{get_tag<0xFu>(qg.x), get_tag<0xFu>(qg.y), get_tag<0xFu>(qg.z), get_tag<0xFu>(qg.w)};
  }
  if (cw == 7) {
    u32x4 e0, e1; unsigned spins = 0;
#pragma clang loop unroll(disable)
    for (;;) {
      e0 = ld16c(rs, WS_RANK + lane * 32); e1 = ld16c(rs, WS_RANK + lane * 32 + 16);
      if (__all((e0.y == T) & (e0.w == T) & (e1.y == T) & (e1.w == T)) || ++spins > SPIN_CAP) break;
      __builtin_amdgcn_s_sleep(4);
    }
    auto before = [&](u32 t, int g) -> float { const int d = (int)(t - tstart); return (d < 0 || (d == 0 && g < wg)) ? 1.f : 0.f; };
    const float n = wave_sum((before(e0.x, 4 * lane) + before(e0.z, 4 * lane + 1)) + (before(e1.x, 4 * lane + 2) + before(e1.z, 4 * lane + 3)));
    if (lane == 0) lds_st(L_FL + 4 * F_RANK, (u32)n + 1u);
  }
  cbar(4, lane);
  if (cw == 0) {
    f32x4 sv[4];
#pragma unroll
    for (int q = 0; q < 4; ++q) sv[q] = *(const f32x4*)(lf + L_QSUM / 4 + q * 256 + 4 * lane);
    const f32x4 sum = (sv[0] + sv[1]) + (sv[2] + sv[3]);
    *(u32x2*)(lu + L_Q / 4 + 2 * lane) = u32x2{pack2(sum.x, sum.y), pack2(sum.z, sum.w)};
    asm volatile("s_waitcnt lgkmcnt(0)" ::: "memory");
    if (lane == 0) lds_st(L_FL + 4 * F_QRDY, 1u);
  } else {
    wait_word(F_QRDY, 1u);
  }
  STAMP(3);
  PMARK(3);
  float s0, s1;
  {
    u32x4 qa[4];
#pragma unroll
    for (int ks = 0; ks < 4; ++ks) qa[ks] = *(const u32x4*)(lu + L_Q / 4 + (lane & 1) * 64 + 16 * ks + 4 * g4);
    f32x4 d[4];
#pragma unroll
    for (int m = 0; m < 4; ++m) {
      const int tile = cw + 8 * m;
      wait_word_sleep(F_LANDED, PK + 4 * (tile + 1));
      u32x4 kf[4];
#pragma unroll
      for (int ks = 0; ks < 4; ++ks) kf[ks] = *(const u32x4*)(lds + tile * 4096 + c16 * 256 + (((4 * ks + g4) ^ c16) << 4));
      f32x4 acc = mfma(qa[0], kf[0], f32x4{0.f, 0.f, 0.f, 0.f});
      acc = mfma(qa[1], kf[1], acc); acc = mfma(qa[2], kf[2], acc); acc = mfma(qa[3], kf[3], acc);
      d[m] = acc;
      asm volatile("s_waitcnt lgkmcnt(0)" ::: "memory");
      if (lane == 0) lds_st(L_FL + 4 * (F_KDONE + cw), m + 1);
    }
    const float x0 = g4 == 0 ? d[0][0] : g4 == 1 ? d[1][0] : g4 == 2 ? d[2][0] : d[3][0];
    const float x1 = g4 == 0 ? d[0][1] : g4 == 1 ? d[1][1] : g4 == 2 ? d[2][1] : d[3][1];
    constexpr float SCALE = 0.08838834764831845f;
    s0 = rbf(rbf(x0) * SCALE); s1 = rbf(rbf(x1) * SCALE);
  }
  STAMP(4);
  PMARK(4);
  u32x4 mine;
  {
    constexpr float L2E = 1.4426950408889634f;
    const float m0 = wave_max(s0), m1 = wave_max(s1);
    const float l0 = wave_sum(__builtin_amdgcn_exp2f((s0 - m0) * L2E)), l1 = wave_sum(__builtin_amdgcn_exp2f((s1 - m1) * L2E));
    if (lane == 0) *(f32x4*)(lf + L_ST / 4 + 16 + 4 * cw) = f32x4{m0, m1, l0, l1};
    cbar(5, lane);
    float M0 = -INFINITY, M1 = -INFINITY;
    f32x4 st[8];
#pragma unroll
    for (int ww = 0; ww < 8; ++ww) { st[ww] = *(const f32x4*)(lf + L_ST / 4 + 16 + 4 * ww); M0 = fmax_(M0, st[ww].x); M1 = fmax_(M1, st[ww].y); }
    float L0 = 0.f, L1 = 0.f;
#pragma unroll
    for (int ww = 0; ww < 8; ++ww) {
      L0 = fmaf(st[ww].z, __builtin_amdgcn_exp2f((st[ww].x - M0) * L2E), L0);
      L1 = fmaf(st[ww].w, __builtin_amdgcn_exp2f((st[ww].y - M1) * L2E), L1);
    }
    const int kl = 16 * (cw + 8 * g4) + c16;
    __bf16* P = (__bf16*)(lds + L_P);
    P[kl] = (__bf16)__builtin_amdgcn_exp2f((s0 - M0) * L2E);
    P[512 + kl] = (__bf16)__builtin_amdgcn_exp2f((s1 - M1) * L2E);
    mine = u32x4{__float_as_uint(M0) | T16, __float_as_uint(M1) | T16, put_tag<0xFFu>(L0, T8), put_tag<0xFFu>(L1, T8)};
    if (ct == 64) st16c(rs, WS_SC + wg * 128, mine);
  }
  cbar(6, lane);
  STAMP(6);
  wait_word(F_RANK, 1u);
  const int rank = (int)__builtin_amdgcn_readfirstlane(lds_ld(L_FL + 4 * F_RANK)) - 1;
  const int role = rank < 72 ? ROLE_G : (rank < 216 ? ROLE_W : ROLE_F);
  {
    const int q4 = (lane & 15) >> 2, p4 = lane & 3;
    auto vfrag = [&](u32 base, int e) -> u32x4 {
      u32x2 h[2];
#pragma unroll
      for (int t = 0; t < 2; ++t) {
        const int row = 8 * g4 + 4 * t + q4;
        const u32 a = base + row * 256 + (((2 * e + (p4 >> 1)) ^ vswz(row)) << 4) + 8 * (p4 & 1);
        h[t] = __builtin_bit_cast(u32x2, __builtin_amdgcn_ds_read_tr16_b64_v4i16((LAS s16x4*)(lds + a)));
      }
      return u32x4{h[0].x, h[0].y, h[1].x, h[1].y};
    };
    auto pfrag = [&](int ks) -> u32x4 { return *(const u32x4*)(lds + L_P + (lane & 1) * 1024 + (32 * ks + 8 * g4) * 2); };
    {
      const u32x4 pf = pfrag(cw);
      const u32 base = cw < 2 ? L_EXT + cw * 8192 : (cw - 2) * 8192;
      wait_word(F_LANDED, PV + 8 * (cw + 1));
      u32x4 f[8];
#pragma unroll
      for (int e = 0; e < 8; ++e) f[e] = vfrag(base, e);
      SB();
      f32x4 d[8];
#pragma unroll
      for (int e = 0; e < 8; ++e) d[e] = mfma(f[e], pf, f32x4{0.f, 0.f, 0.f, 0.f});
      asm volatile("s_waitcnt lgkmcnt(0)" ::: "memory");
      if (lane == 0) lds_st(L_FL + 4 * (F_VDONE + cw), 1u);
      if (c16 < 2) {
#pragma unroll
        for (int e = 0; e < 8; ++e) *(f32x4*)(lf + L_QSUM / 4 + cw * 256 + c16 * 128 + 16 * e + 4 * g4) = d[e];
      }
    }
    cbar(7, lane);
    f32x4 acc;
    {
      f32x4 sv[8];
#pragma unroll
      for (int ww = 0; ww < 8; ++ww) sv[ww] = *(const f32x4*)(lf + L_QSUM / 4 + ww * 256 + (lane & 1) * 128 + 16 * cw + 4 * g4);
      acc = ((sv[0] + sv[1]) + (sv[2] + sv[3])) + ((sv[4] + sv[5]) + (sv[6] + sv[7]));
    }
    u32x4 oth = {0u, 0u, 0u, 0u};
    bool got = cw != 1;
    if (cw == 1) oth = ld16c(rs, WS_SC + (wg ^ 8) * 128);
    auto try_stats = [&](bool last) {
      if (got) return;
      unsigned spins = 0;
#pragma clang loop unroll(disable)
      for (;;) {
        if (__all(((((oth.x ^ T16) | (oth.y ^ T16)) & 0xFFFFu) | (((oth.z ^ T8) | (oth.w ^ T8)) & 0xFFu)) == 0u) || ++spins > SPIN_CAP) { got = true; break; }
        oth = ld16c(rs, WS_SC + (wg ^ 8) * 128);
        if (!last) return;
      }
      constexpr float L2E = 1.4426950408889634f;
      const float M0 = bfhi(mine.x), M1 = bfhi(mine.y), om0 = bfhi(oth.x), om1 = bfhi(oth.y);
      const float G0 = fmax_(M0, om0), G1 = fmax_(M1, om1);
      const float e0 = __builtin_amdgcn_exp2f((M0 - G0) * L2E), e1 = __builtin_amdgcn_exp2f((M1 - G1) * L2E);
      const float S0 = get_tag<0xFFu>(mine.z) * e0 + get_tag<0xFFu>(oth.z) * __builtin_amdgcn_exp2f((om0 - G0) * L2E);
      const float S1 = get_tag<0xFFu>(mine.w) * e1 + get_tag<0xFFu>(oth.w) * __builtin_amdgcn_exp2f((om1 - G1) * L2E);
      if (lane == 0) { *(f32x2*)(lf + L_ST / 4 + 48) = f32x2{e0 / S0, e1 / S1}; asm volatile("s_waitcnt lgkmcnt(0)" ::: "memory"); lds_st(L_FL + 4 * F_FAC, 1u); }
      STAMP(5);
    };
#pragma unroll
    for (int ks = 8; ks < 16; ++ks) {
      const u32x4 pf = pfrag(ks);
      const int tl = 2 * ks + 1;
      wait_word(F_LANDED, PV + 4 * (tl + 1) + ((role == ROLE_G && tl > 32 - GEARLY) ? 4 * (tl - (32 - GEARLY)) : 0));
      acc = mfma(vfrag((ks - 2) * 8192, cw), pf, acc);
      if (ks >= 9 && ks <= 13) try_stats(false);
      if (ks == 14) { try_stats(true); wait_word(F_FAC, 1u); }
    }
    const float fac = lf[L_ST / 4 + 48 + (lane & 1)];
    asm volatile("s_waitcnt lgkmcnt(0)" ::: "memory");
    if (lane == 0) lds_st(L_FL + 4 * (F_VDONE + cw), 2u);
    if (c16 < 2)
      st16c(rs, WS_OS + (((j * 8 + b) * 2 + half) * 256 + c16 * 128 + 16 * cw + 4 * g4) * 4,
            u32x4{put_tag<0xFFu>(acc.x * fac, T8), put_tag<0xFFu>(acc.y * fac, T8), put_tag<0xFFu>(acc.z * fac, T8), put_tag<0xFFu>(acc.w * fac, T8)});
  }
  auto gather4 = [&](u32 v) -> u32x4 {
    return u32x4{v, (u32)__builtin_amdgcn_mov_dpp((int)v, 0x55, 0xf, 0xf, true), (u32)__builtin_amdgcn_mov_dpp((int)v, 0xAA, 0xf, 0xf, true),
                 (u32)__builtin_amdgcn_mov_dpp((int)v, 0xFF, 0xf, 0xf, true)};
  };
  const unsigned long long to = now();
  STAMP(7);
  PMARK(7);

  auto acc_to_lds = [&](int base, const f32x4 (&d)[4]) {
    if (c16 < 8) {
#pragma unroll
      for (int e = 0; e < 4; ++e) *(f32x4*)(lf + base / 4 + (cw * 8 + c16) * RS + 16 * e + 4 * g4) = d[e];
    }
  };
  auto publish_partial = [&](int base, int jw, int cg) {
    float sv[8];
#pragma unroll
    for (int ww = 0; ww < 8; ++ww) sv[ww] = lf[L_RED_W / 4 + (ww * 8 + cw) * RS + lane];
    const float sum = ((sv[0] + sv[1]) + (sv[2] + sv[3])) + ((sv[4] + sv[5]) + (sv[6] + sv[7]));
    const u32x4 o = gather4(put_tag<0xFFu>(sum, T8));
    if ((lane & 3) == 0) st16c(rs, base + (((cw * 9 + cg) * 16 + jw) * 64 + lane) * 4, o);
  };

  if (role == ROLE_W) {
    const int jw = (rank - 72) / 9, cg = (rank - 72) % 9;
    u32x4 f[4];
    wait_word(F_LANDED, PW + 32);
    tile_frags_lds(lds, cw * 4096, lane, f);
    {
      const int off = WS_OS + ((jw * 8 + (lane & 7)) * 512 + 32 * cw + 8 * g4) * 4;
      u32x4 a0 = {0u, 0u, 0u, 0u}, a1 = a0, b0 = a0, b1 = a0;
      if (c16 < 8)
        POLL((a0 = ld16c(rs, off), a1 = ld16c(rs, off + 16), b0 = ld16c(rs, off + 1024), b1 = ld16c(rs, off + 1040),
              tag_ok<0xFFu>(a0, T8) & tag_ok<0xFFu>(a1, T8) & tag_ok<0xFFu>(b0, T8) & tag_ok<0xFFu>(b1, T8)));
      STAMP(8);
      RSTAMP(0);
#define S2(x, y) (get_tag<0xFFu>(x) + get_tag<0xFFu>(y))
      const u32x4 a = {pack2(S2(a0.x, b0.x), S2(a0.y, b0.y)), pack2(S2(a0.z, b0.z), S2(a0.w, b0.w)), pack2(S2(a1.x, b1.x), S2(a1.y, b1.y)), pack2(S2(a1.z, b1.z), S2(a1.w, b1.w))};
#undef S2
      f32x4 d[4];
#pragma unroll
      for (int e = 0; e < 4; ++e) d[e] = mfma(f[e], a, f32x4{0.f, 0.f, 0.f, 0.f});
      acc_to_lds(L_RED_W, d);
    }
    wg_barrier();
    publish_partial(WS_PW, jw, cg);
    const unsigned long long tw = now();
    STAMP(9);
    u32x4 f2[4];
    PollRegs pr = {};
    {
      const unsigned char* a = (const unsigned char*)(p.wd + (size_t)(288 * jw + 32 * cw) * DM + 64 * cg);
      const u32 o0 = rowoff(0, DM * 2, lane), o1 = rowoff(1, DM * 2, lane);
      tile_dma<-1>(a, DM * 2, o0, o1, (8 + cw) * 4096);
      if (cw == 0) tile_dma<-1>(a + 256 * DM * 2, DM * 2, o0, o1, 16 * 4096);
      waitvm<0>();
      tile_frags_lds(lds, (8 + cw) * 4096, lane, f);
      if (cw == 0) tile_frags_lds(lds, 16 * 4096, lane, f2);
    }
    {
      const int off = WS_AS + ((lane & 7) * FF + 288 * jw + 32 * cw + 8 * g4) * 4;
      u32x4 R[4] = {};
      not_before(tw, GATE_ACT);
      if (c16 < 8) {
        if (cw == 0) poll4(pr, rs, off, off + 16, off + 1024, off + 1040, T16, 0xFFFFu, R);
        else poll2(pr, rs, off, T16, 0xFFFFu, R[0], R[1]);
      }
      STAMP(12);
      const u32x4 a = afrag(R[0], R[1]);
      f32x4 d[4];
#pragma unroll
      for (int e = 0; e < 4; ++e) d[e] = mfma(f[e], a, f32x4{0.f, 0.f, 0.f, 0.f});
      if (cw == 0) {
        const u32x4 ab = afrag(R[2], R[3]);
#pragma unroll
        for (int e = 0; e < 4; ++e) d[e] = mfma(f2[e], ab, d[e]);
      }
      acc_to_lds(L_RED_W, d);
    }
    wg_barrier();
    publish_partial(WS_PD, jw, cg);
    STAMP(13);
    poll_done(pr);
  } else if (role == ROLE_G) {
    const int gx = rank;
    u32x4 f[5][4];
#pragma unroll
    for (int tl = 0; tl < 5; ++tl) {
      if (tl < 4 || cw < 4) {
        const int tau = cw + 8 * tl;
        const int n = tau < 16 ? tau : (tau >= 32 ? tau - 16 : tau + 4);
        wait_word(F_LANDED, tau < GEARLY ? PV + 4 * (32 - GEARLY + tau + 1) + 4 * (tau + 1) : PW + 4 * (n + 1));
        tile_frags_lds(lds, tau < 4 ? L_EXT + tau * 4096 : (tau - 4) * 4096, lane, f[tl]);
      }
    }
    wait_word(F_LANDED, PW + 144);
    STAMP(8);
    PollRegs pr;
    {
      const int off = WS_HS + (xcd * 8 + cw) * DM * 4;
      u32x4 h0, h1; u32 h2;
      not_before(to, GATE_H);
      poll3(pr, rs, off + lane * 32, off + 2048 + lane * 4, T16, 0xFFFFu, h0, h1, h2);
      STAMP(10);
      float v[9]; bool valid[9];
#pragma unroll
      for (int e = 0; e < 4; ++e) { v[e] = bfhi(h0[e]); v[4 + e] = bfhi(h1[e]); }
      v[8] = bfhi(h2);
#pragma unroll
      for (int e = 0; e < 9; ++e) valid[e] = true;
      const float pivot = __uint_as_float(__builtin_amdgcn_readfirstlane(h0.x) & 0xFFFF0000u);
      const RowStats st = row_stats<9>(v, valid, pivot);
      u32* hn = lu + L_HN / 4 + cw * HN_STRIDE;
      *(u32x4*)(hn + 4 * lane) = u32x4{pack2(ln_apply(v[0], st), ln_apply(v[1], st)), pack2(ln_apply(v[2], st), ln_apply(v[3], st)),
                                       pack2(ln_apply(v[4], st), ln_apply(v[5], st)), pack2(ln_apply(v[6], st), ln_apply(v[7], st))};
      ((__bf16*)(hn + 256))[lane] = (__bf16)ln_apply(v[8], st);
    }
    STAMP(9);
    wg_barrier();
    STAMP(12);
    {
      f32x4 d[4];
      const u32* hn = lu + L_HN / 4 + (lane & 7) * HN_STRIDE + 16 * (cw >> 1) + 4 * g4;
      u32x4 a[5];
#pragma unroll
      for (int tl = 0; tl < 5; ++tl) a[tl] = *(const u32x4*)(hn + (tl < 4 || cw < 4 ? 64 * tl : 0));
      SB();
#pragma unroll
      for (int e = 0; e < 4; ++e) d[e] = mfma(f[0][e], a[0], f32x4{0.f, 0.f, 0.f, 0.f});
#pragma unroll
      for (int tl = 1; tl < 5; ++tl) {
        if (tl < 4 || cw < 4) {
#pragma unroll
          for (int e = 0; e < 4; ++e) d[e] = mfma(f[tl][e], a[tl], d[e]);
        }
      }
      STAMP(13);
      acc_to_lds(0, d);
    }
    wg_barrier();
    STAMP(3);
    {
      float gv[4], uv[4];
#pragma unroll
      for (int ww = 0; ww < 4; ++ww) { gv[ww] = lf[(2 * ww * 8 + cw) * RS + lane]; uv[ww] = lf[((2 * ww + 1) * 8 + cw) * RS + lane]; }
      const float gg = rbf((gv[0] + gv[1]) + (gv[2] + gv[3])), uu = rbf((uv[0] + uv[1]) + (uv[2] + uv[3]));
      const float sl = rbf(gg * __builtin_amdgcn_rcpf(1.0f + __builtin_amdgcn_exp2f(gg * -1.4426950408889634f)));
      const u32x4 o = gather4(bfbits_hi(sl * uu) | T16);
      if ((lane & 3) == 0) st16c(rs, WS_AS + (cw * FF + 64 * gx + lane) * 4, o);
    }
    STAMP(11);
    poll_done(pr);
  } else {
    const int f = rank - 216;
    const int job = 2 * f + (cw >> 2);
    if ((cw & 3) == 0 && job < 72) {
      const int jb = job / 9, cg = job % 9;
      const u32x2 xq = *(const GAS u32x2*)(p.x + jb * DM + 64 * cg + 4 * c16);
      const u32x4 wr0 = *(const GAS u32x4*)(p.wr + 8 * lane);
      const u16 wr1 = ((const GAS u16*)p.wr)[512 + lane];
      PollRegs pr;
      auto gather16 = [&](int base) -> f32x4 {
        const int off = base + (((jb * 9 + cg) * 16 + g4) * 64 + 4 * c16) * 4;
        u32x4 R[4];
        poll4(pr, rs, off, off + 1024, off + 2048, off + 3072, T8, 0xFFu, R);
        const u32x4 q0 = R[0], q1 = R[1], q2 = R[2], q3 = R[3];
        f32x4 sum;
#pragma unroll
        for (int e = 0; e < 4; ++e) {
          float t = (get_tag<0xFFu>(q0[e]) + get_tag<0xFFu>(q1[e])) + (get_tag<0xFFu>(q2[e]) + get_tag<0xFFu>(q3[e]));
          t = swap16_add(t, t); t = swap32_add(t, t);
          sum[e] = t;
        }
        return sum;
      };
      STAMP(3);
      not_before(to, GATE_PW);
      const f32x4 at = gather16(WS_PW);
      STAMP(8);
      PMARK(8);
      RSTAMP(0);
      f32x4 hv;
      hv[0] = rbf(bflo(xq.x) + rbf(at[0])); hv[1] = rbf(bfhi(xq.x) + rbf(at[1]));
      hv[2] = rbf(bflo(xq.y) + rbf(at[2])); hv[3] = rbf(bfhi(xq.y) + rbf(at[3]));
      {
        const u32x4 hq = {__float_as_uint(hv[0]) | T16, __float_as_uint(hv[1]) | T16, __float_as_uint(hv[2]) | T16, __float_as_uint(hv[3]) | T16};
        const int o = WS_HS + ((g4 * 8 + jb) * DM + 64 * cg + 4 * c16) * 4;
        st16c(rs, o, hq); st16c(rs, o + 4 * 8 * DM * 4, hq);
      }
      const unsigned long long th = now();
      poll_done(pr);
      STAMP(9);
      RowStats st; float sig;
      {
        const int off = WS_HS + (xcd * 8 + jb) * DM * 4;
        u32x4 h0, h1; u32 h2;
        {
          unsigned spins = 0;
#pragma clang loop unroll(disable)
          for (;;) {
            h0 = ld16c(rs, off + lane * 32); h1 = ld16c(rs, off + lane * 32 + 16); h2 = ld4c(rs, off + 2048 + lane * 4);
            if (__all(tag_ok<0xFFFFu>(h0, T16) & tag_ok<0xFFFFu>(h1, T16) & ((h2 & 0xFFFFu) == T16)) || ++spins > SPIN_CAP) break;
            __builtin_amdgcn_s_sleep(2);
          }
        }
        float v[9]; bool valid[9];
#pragma unroll
        for (int e = 0; e < 4; ++e) { v[e] = bfhi(h0[e]); v[4 + e] = bfhi(h1[e]); }
        v[8] = bfhi(h2);
#pragma unroll
        for (int e = 0; e < 9; ++e) valid[e] = true;
        const float pivot = __uint_as_float(__builtin_amdgcn_readfirstlane(h0.x) & 0xFFFF0000u);
        st = row_stats<9>(v, valid, pivot);
        float r = rbf(ln_apply(v[8], st)) * bflo((u32)wr1);
#pragma unroll
        for (int e = 0; e < 8; ++e) r = fmaf(rbf(ln_apply(v[e], st)), (e & 1) ? bfhi(wr0[e >> 1]) : bflo(wr0[e >> 1]), r);
        r = rbf(wave_sum(r));
        sig = rbf(__builtin_amdgcn_rcpf(1.0f + __builtin_amdgcn_exp2f(r * -1.4426950408889634f)));
      }
      float hn[4];
#pragma unroll
      for (int e = 0; e < 4; ++e) hn[e] = rbf(ln_apply(hv[e], st));
      STAMP(10);
      not_before(th, GATE_PD);
      const f32x4 y = gather16(WS_PD);
      STAMP(12);
      RSTAMP(1);
      float o[4];
#pragma unroll
      for (int e = 0; e < 4; ++e) o[e] = hv[e] + rbf(hn[e] + rbf(rbf(y[e]) * sig));
      if (lane < 16) *(GAS u32x2*)(p.out + jb * DM + 64 * cg + 4 * lane) = u32x2{pack2(o[0], o[1]), pack2(o[2], o[3])};
      poll_done(pr);
      PMARK(9);
      if (job == 0 && lane == 0) __hip_atomic_store((GAS u32*)(p.ws + WS_EPOCH), T, __ATOMIC_RELAXED, __HIP_MEMORY_SCOPE_AGENT);
      STAMP(13);
    }
  }
  STAMP(14);
  STAMP_FLUSH();
}

}

extern "C" void kernel_launch(void* const* d_in, const int* in_sizes, int n_in, void* d_out, int out_size, void* d_ws, size_t ws_size,
                              hipStream_t stream) {
  Params p;
  p.x = (const u16*)d_in[0];
  p.kc = (const u16*)d_in[1];
  p.vc = (const u16*)d_in[2];
  p.wq = (const u16*)d_in[3];
  p.wo = (const u16*)d_in[4];
  p.wr = (const u16*)d_in[6];
  p.wg = (const u16*)d_in[7];
  p.wu = (const u16*)d_in[8];
  p.wd = (const u16*)d_in[9];
  p.out = (u16*)d_out;
  p.ws = (unsigned char*)d_ws;
  (void)hipFuncSetAttribute((const void*)fused_kernel, hipFuncAttributeMaxDynamicSharedMemorySize, LDS_BYTES);
  hipLaunchKernelGGL(fused_kernel, dim3(NWG), dim3(NTHR), LDS_BYTES, stream, p);
}
```

```cpp
#include <hip/hip_runtime.h>
#include <stdint.h>

typedef unsigned u32;
typedef unsigned short u16;
typedef u32 u32x4 __attribute__((ext_vector_type(4)));
typedef u32 u32x2 __attribute__((ext_vector_type(2)));
typedef float f32x4 __attribute__((ext_vector_type(4)));
typedef float f32x2 __attribute__((ext_vector_type(2)));
typedef short s16x4 __attribute__((ext_vector_type(4)));
typedef __bf16 bf16x8 __attribute__((ext_vector_type(8)));
typedef __bf16 bf16x2 __attribute__((ext_vector_type(2)));
#define GAS __attribute__((address_space(1)))
#define LAS __attribute__((address_space(3)))

namespace {

constexpr int DM = 576, HKV = 16, SK = 4096, DH = 128, WIN = 1024, NQ = 4096, FF = 4608;
constexpr int NWG = 256, NTHR = 576;

constexpr int LWIN = 24;
constexpr int GWIN = 40;
constexpr int GEARLY = 4;
constexpr int QDELAY = 32;
constexpr int PSTAG = 6;
constexpr int PSLEEP = 1;
constexpr unsigned GATE_PW = 90, GATE_H = 150, GATE_ACT = 220, GATE_PD = 270;

constexpr int WS_EPOCH = 0;
constexpr int WS_RANK = 2048;
constexpr int WS_QS = 4096;
constexpr int WS_SC = WS_QS + 16 * 8 * 4 * 256 * 4;
constexpr int WS_OS = WS_SC + 256 * 128;
constexpr int WS_PW = WS_OS + 16 * 8 * 2 * 256 * 4;
constexpr int HREP = 8;
constexpr int WS_HS = WS_PW + 8 * 9 * 16 * 64 * 4;
constexpr int WS_AS = WS_HS + HREP * 8 * 576 * 4;
constexpr int WS_PD = WS_AS + 8 * 4608 * 4;
constexpr int WS_END = WS_PD + 8 * 9 * 16 * 64 * 4;
constexpr int AUX_SC1 = 16;
constexpr unsigned SPIN_CAP = 1u << 16, LSPIN_CAP = 1u << 22;

constexpr int L_EXT = 128 * 1024;
constexpr int L_FL = 144 * 1024;
constexpr int L_ST = L_FL + 128;
constexpr int L_P = L_ST + 256;
constexpr int L_QSUM = L_P + 2048;
constexpr int L_Q = L_QSUM + 8192;
constexpr int L_HN = L_P;
constexpr int HN_STRIDE = 292;
constexpr int RS = 68;
constexpr int L_RED_W = 112 * 1024;
constexpr int LDS_BYTES = 160 * 1024;
static_assert(L_Q + 512 <= LDS_BYTES && L_HN + 8 * HN_STRIDE * 4 <= LDS_BYTES, "LDS");
enum { F_LANDED = 0, F_QPUB = 1, F_BAR = 2, F_QRDY = 3, F_RANK = 4, F_FAC = 5, F_KDONE = 8, F_VDONE = 16 };

struct Params {
  const u16 *x, *kc, *vc, *wq, *wo, *wr, *wg, *wu, *wd;
  u16* out;
  unsigned char* ws;
};

__device__ __forceinline__ float bflo(u32 d) { return __uint_as_float(d << 16); }
__device__ __forceinline__ float bfhi(u32 d) { return __uint_as_float(d & 0xFFFF0000u); }
__device__ __forceinline__ float rbf(float f) { return (float)(__bf16)f; }
__device__ __forceinline__ u32 bfbits_hi(float f) { return __float_as_uint(rbf(f)); }
__device__ __forceinline__ u32 pack2(float lo, float hi) {
  bf16x2 v = {(__bf16)lo, (__bf16)hi};
  return __builtin_bit_cast(u32, v);
}
template <int CTRL> __device__ __forceinline__ float dpp(float x) {
  return __uint_as_float((u32)__builtin_amdgcn_mov_dpp((int)__float_as_uint(x), CTRL, 0xf, 0xf, true));
}
constexpr int XOR1 = 0xB1, XOR2 = 0x4E, XOR7 = 0x141, XOR8 = 0x128;
__device__ __forceinline__ float swap32_add(float a, float b) {
  auto r = __builtin_amdgcn_permlane32_swap(__float_as_uint(a), __float_as_uint(b), false, false);
  return __uint_as_float(r[0]) + __uint_as_float(r[1]);
}
__device__ __forceinline__ float swap16_add(float a, float b) {
  auto r = __builtin_amdgcn_permlane16_swap(__float_as_uint(a), __float_as_uint(b), false, false);
  return __uint_as_float(r[0]) + __uint_as_float(r[1]);
}
__device__ __forceinline__ float wave_sum(float x) {
  x += dpp<XOR1>(x); x += dpp<XOR2>(x); x += dpp<XOR7>(x); x += dpp<XOR8>(x);
  x = swap16_add(x, x); x = swap32_add(x, x);
  return x;
}
__device__ __forceinline__ float fmax_(float a, float b) { return __builtin_amdgcn_fmed3f(a, b, INFINITY); }
__device__ __forceinline__ float wave_max(float x) {
  x = fmax_(x, dpp<XOR1>(x)); x = fmax_(x, dpp<XOR2>(x)); x = fmax_(x, dpp<XOR7>(x)); x = fmax_(x, dpp<XOR8>(x));
  auto r = __builtin_amdgcn_permlane16_swap(__float_as_uint(x), __float_as_uint(x), false, false);
  x = fmax_(__uint_as_float(r[0]), __uint_as_float(r[1]));
  auto t = __builtin_amdgcn_permlane32_swap(__float_as_uint(x), __float_as_uint(x), false, false);
  return fmax_(__uint_as_float(t[0]), __uint_as_float(t[1]));
}
__device__ __forceinline__ f32x4 mfma(u32x4 a, u32x4 b, f32x4 c) {
  return __builtin_amdgcn_mfma_f32_16x16x32_bf16(__builtin_bit_cast(bf16x8, a), __builtin_bit_cast(bf16x8, b), c, 0, 0, 0);
}
#define SB() __builtin_amdgcn_sched_barrier(0)
template <int N> __device__ __forceinline__ void waitvm() { asm volatile("s_waitcnt vmcnt(%0)" ::"n"(N) : "memory"); }
typedef __amdgpu_buffer_rsrc_t rsrc_t;
__device__ __forceinline__ u32x4 ld16c(rsrc_t rs, int off) { return __builtin_amdgcn_raw_buffer_load_b128(rs, off, 0, AUX_SC1); }
__device__ __forceinline__ u32 ld4c(rsrc_t rs, int off) { return __builtin_amdgcn_raw_buffer_load_b32(rs, off, 0, AUX_SC1); }
__device__ __forceinline__ void st16c(rsrc_t rs, int off, u32x4 v) { __builtin_amdgcn_raw_buffer_store_b128(v, rs, off, 0, AUX_SC1); }
template <u32 M> __device__ __forceinline__ bool tag_ok(u32x4 v, u32 t) { return ((((v.x ^ t) | (v.y ^ t)) | ((v.z ^ t) | (v.w ^ t))) & M) == 0u; }
template <u32 M> __device__ __forceinline__ u32 put_tag(float f, u32 t) { return ((__float_as_uint(f) + (M + 1) / 2) & ~M) | t; }
template <u32 M> __device__ __forceinline__ float get_tag(u32 d) { return __uint_as_float(d & ~M); }
#define POLL(cond_load) do { unsigned spins_ = 0; _Pragma("clang loop unroll(disable)") while (!(cond_load) && ++spins_ < SPIN_CAP) __builtin_amdgcn_s_sleep(PSLEEP); } while (0)

struct PollRegs { u32x4 a0, a1, a2, a3, b0, b1, b2, b3; };
#define PV_OUT(p) "=&{v[128:131]}"(p.a0), "=&{v[132:135]}"(p.a1), "=&{v[136:139]}"(p.a2), "=&{v[140:143]}"(p.a3), \
                  "=&{v[144:147]}"(p.b0), "=&{v[148:151]}"(p.b1), "=&{v[152:155]}"(p.b2), "=&{v[156:159]}"(p.b3)
#define PV_CLOB "v160", "v161", "v162", "v163", "vcc", "scc", "memory"
__device__ __forceinline__ void poll_done(const PollRegs& p) {
  asm volatile("s_waitcnt vmcnt(0)" ::"{v[128:131]}"(p.a0), "{v[132:135]}"(p.a1), "{v[136:139]}"(p.a2), "{v[140:143]}"(p.a3),
               "{v[144:147]}"(p.b0), "{v[148:151]}"(p.b1), "{v[152:155]}"(p.b2), "{v[156:159]}"(p.b3) : "memory");
}
#define PV_LD4(r0, r1, r2, r3, r4, r5, r6, r7) \
  "buffer_load_dwordx4 v[" r0 ":" r1 "], %[o0], %[rs], 0 offen sc1\n\t" "buffer_load_dwordx4 v[" r2 ":" r3 "], %[o1], %[rs], 0 offen sc1\n\t" \
  "buffer_load_dwordx4 v[" r4 ":" r5 "], %[o2], %[rs], 0 offen sc1\n\t" "buffer_load_dwordx4 v[" r6 ":" r7 "], %[o3], %[rs], 0 offen sc1\n\t"
#define PV_X4(t, a, b, c, d) "v_xor_b32 " t ", %[T], v" a "\n\t" "v_bitop3_b32 " t ", " t ", v" b ", %[T] bitop3:0xf6\n\t" "v_bitop3_b32 " t ", " t ", v" c ", %[T] bitop3:0xf6\n\t" "v_bitop3_b32 " t ", " t ", v" d ", %[T] bitop3:0xf6\n\t"
#define PV_TEST "v_and_b32 v160, %[M], v160\n\tv_cmp_ne_u32 vcc, 0, v160\n\ts_cmp_eq_u64 vcc, 0\n\t"
#define PV_MOV2(o, a, b) "v_mov_b64 " o ", v[" a ":" b "]\n\t"
__device__ __forceinline__ void poll4(PollRegs& p, rsrc_t rs, int o0, int o1, int o2, int o3, u32 T, u32 M, u32x4 (&R)[4]) {
  u32x2 r0, r1, r2, r3, r4, r5, r6, r7; u32 cnt = SPIN_CAP;
  asm volatile(
      PV_LD4("128", "131", "132", "135", "136", "139", "140", "143")
      "s_sleep %[stag]\n"
      "1:\n\t"
      PV_LD4("144", "147", "148", "151", "152", "155", "156", "159")
      "s_waitcnt vmcnt(4)\n\t"
      PV_X4("v160", "128", "129", "130", "131") PV_X4("v161", "132", "133", "134", "135") PV_X4("v162", "136", "137", "138", "139") PV_X4("v163", "140", "141", "142", "143")
      "v_or3_b32 v160, v160, v161, v162\n\tv_or_b32 v160, v160, v163\n\t" PV_TEST
      "s_cbranch_scc1 7f\n\t"
      PV_LD4("128", "131", "132", "135", "136", "139", "140", "143")
      "s_waitcnt vmcnt(4)\n\t"
      PV_X4("v160", "144", "145", "146", "147") PV_X4("v161", "148", "149", "150", "151") PV_X4("v162", "152", "153", "154", "155") PV_X4("v163", "156", "157", "158", "159")
      "v_or3_b32 v160, v160, v161, v162\n\tv_or_b32 v160, v160, v163\n\t" PV_TEST
      "s_cbranch_scc1 8f\n\t"
      "s_sub_u32 %[cnt], %[cnt], 1\n\ts_cbranch_scc0 1b\n\t"
      "s_waitcnt vmcnt(0)\n"
      "7:\n\t"
      PV_MOV2("%[r0]", "128", "129") PV_MOV2("%[r1]", "130", "131") PV_MOV2("%[r2]", "132", "133") PV_MOV2("%[r3]", "134", "135")
      PV_MOV2("%[r4]", "136", "137") PV_MOV2("%[r5]", "138", "139") PV_MOV2("%[r6]", "140", "141") PV_MOV2("%[r7]", "142", "143")
      "s_branch 9f\n"
      "8:\n\t"
      PV_MOV2("%[r0]", "144", "145") PV_MOV2("%[r1]", "146", "147") PV_MOV2("%[r2]", "148", "149") PV_MOV2("%[r3]", "150", "151")
      PV_MOV2("%[r4]", "152", "153") PV_MOV2("%[r5]", "154", "155") PV_MOV2("%[r6]", "156", "157") PV_MOV2("%[r7]", "158", "159")
      "9:\n\t"
      : [r0] "=&v"(r0), [r1] "=&v"(r1), [r2] "=&v"(r2), [r3] "=&v"(r3), [r4] "=&v"(r4), [r5] "=&v"(r5), [r6] "=&v"(r6), [r7] "=&v"(r7), [cnt] "+s"(cnt), PV_OUT(p)
      : [o0] "v"(o0), [o1] "v"(o1), [o2] "v"(o2), [o3] "v"(o3), [rs] "s"(rs), [T] "s"(T), [M] "s"(M), [stag] "n"(PSTAG)
      : PV_CLOB);
  R[0] = u32x4{r0.x, r0.y, r1.x, r1.y}; R[1] = u32x4{r2.x, r2.y, r3.x, r3.y}; R[2] = u32x4{r4.x, r4.y, r5.x, r5.y}; R[3] = u32x4{r6.x, r6.y, r7.x, r7.y};
}
#define PV_LD2(r0, r1, r2, r3) \
  "buffer_load_dwordx4 v[" r0 ":" r1 "], %[o0], %[rs], 0 offen sc1\n\t" "buffer_load_dwordx4 v[" r2 ":" r3 "], %[o0], %[rs], 0 offen offset:16 sc1\n\t"
__device__ __forceinline__ void poll2(PollRegs& p, rsrc_t rs, int o0, u32 T, u32 M, u32x4& a, u32x4& b) {
  u32x2 r0, r1, r2, r3; u32 cnt = SPIN_CAP;
  asm volatile(
      PV_LD2("128", "131", "132", "135")
      "s_sleep %[stag]\n"
      "1:\n\t"
      PV_LD2("144", "147", "148", "151")
      "s_waitcnt vmcnt(2)\n\t"
      PV_X4("v160", "128", "129", "130", "131") PV_X4("v161", "132", "133", "134", "135") "v_or_b32 v160, v160, v161\n\t" PV_TEST
      "s_cbranch_scc1 7f\n\t"
      PV_LD2("128", "131", "132", "135")
      "s_waitcnt vmcnt(2)\n\t"
      PV_X4("v160", "144", "145", "146", "147") PV_X4("v161", "148", "149", "150", "151") "v_or_b32 v160, v160, v161\n\t" PV_TEST
      "s_cbranch_scc1 8f\n\t"
      "s_sub_u32 %[cnt], %[cnt], 1\n\ts_cbranch_scc0 1b\n\t"
      "s_waitcnt vmcnt(0)\n"
      "7:\n\t"
      PV_MOV2("%[r0]", "128", "129") PV_MOV2("%[r1]", "130", "131") PV_MOV2("%[r2]", "132", "133") PV_MOV2("%[r3]", "134", "135")
      "s_branch 9f\n"
      "8:\n\t"
      PV_MOV2("%[r0]", "144", "145") PV_MOV2("%[r1]", "146", "147") PV_MOV2("%[r2]", "148", "149") PV_MOV2("%[r3]", "150", "151")
      "9:\n\t"
      : [r0] "=&v"(r0), [r1] "=&v"(r1), [r2] "=&v"(r2), [r3] "=&v"(r3), [cnt] "+s"(cnt), PV_OUT(p)
      : [o0] "v"(o0), [rs] "s"(rs), [T] "s"(T), [M] "s"(M), [stag] "n"(PSTAG)
      : PV_CLOB);
  a = u32x4{r0.x, r0.y, r1.x, r1.y}; b = u32x4{r2.x, r2.y, r3.x, r3.y};
}
#define PV_LD3(r0, r1, r2, r3, r4) \
  "buffer_load_dwordx4 v[" r0 ":" r1 "], %[o0], %[rs], 0 offen sc1\n\t" "buffer_load_dwordx4 v[" r2 ":" r3 "], %[o0], %[rs], 0 offen offset:16 sc1\n\t" \
  "buffer_load_dword v" r4 ", %[o1], %[rs], 0 offen sc1\n\t"
__device__ __forceinline__ void poll3(PollRegs& p, rsrc_t rs, int o0, int o1, u32 T, u32 M, u32x4& a, u32x4& b, u32& c) {
  u32x2 r0, r1, r2, r3; u32 r4, cnt = SPIN_CAP;
  asm volatile(
      PV_LD3("128", "131", "132", "135", "136")
      "s_sleep %[stag]\n"
      "1:\n\t"
      PV_LD3("144", "147", "148", "151", "152")
      "s_waitcnt vmcnt(3)\n\t"
      PV_X4("v160", "128", "129", "130", "131") PV_X4("v161", "132", "133", "134", "135") "v_xor_b32 v162, %[T], v136\n\t"
      "v_or3_b32 v160, v160, v161, v162\n\t" PV_TEST
      "s_cbranch_scc1 7f\n\t"
      PV_LD3("128", "131", "132", "135", "136")
      "s_waitcnt vmcnt(3)\n\t"
      PV_X4("v160", "144", "145", "146", "147") PV_X4("v161", "148", "149", "150", "151") "v_xor_b32 v162, %[T], v152\n\t"
      "v_or3_b32 v160, v160, v161, v162\n\t" PV_TEST
      "s_cbranch_scc1 8f\n\t"
      "s_sub_u32 %[cnt], %[cnt], 1\n\ts_cbranch_scc0 1b\n\t"
      "s_waitcnt vmcnt(0)\n"
      "7:\n\t"
      PV_MOV2("%[r0]", "128", "129") PV_MOV2("%[r1]", "130", "131") PV_MOV2("%[r2]", "132", "133") PV_MOV2("%[r3]", "134", "135") "v_mov_b32 %[r4], v136\n\t"
      "s_branch 9f\n"
      "8:\n\t"
      PV_MOV2("%[r0]", "144", "145") PV_MOV2("%[r1]", "146", "147") PV_MOV2("%[r2]", "148", "149") PV_MOV2("%[r3]", "150", "151") "v_mov_b32 %[r4], v152\n\t"
      "9:\n\t"
      : [r0] "=&v"(r0), [r1] "=&v"(r1), [r2] "=&v"(r2), [r3] "=&v"(r3), [r4] "=&v"(r4), [cnt] "+s"(cnt), PV_OUT(p)
      : [o0] "v"(o0), [o1] "v"(o1), [rs] "s"(rs), [T] "s"(T), [M] "s"(M), [stag] "n"(PSTAG)
      : PV_CLOB);
  a = u32x4{r0.x, r0.y, r1.x, r1.y}; b = u32x4{r2.x, r2.y, r3.x, r3.y}; c = r4;
}

__device__ __forceinline__ unsigned long long now() { return __builtin_amdgcn_s_memrealtime(); }
__device__ __forceinline__ void not_before(unsigned long long t0, unsigned ticks) {
  while (now() - t0 < ticks) __builtin_amdgcn_s_sleep(2);
}
__device__ __forceinline__ u32 lds_ld(u32 addr) {
  u32 v;
  asm volatile("ds_read_b32 %0, %1\n\ts_waitcnt lgkmcnt(0)" : "=v"(v) : "v"(addr) : "memory");
  return v;
}
__device__ __forceinline__ void lds_st(u32 addr, u32 v) { asm volatile("ds_write_b32 %0, %1" ::"v"(addr), "v"(v) : "memory"); }
__device__ __forceinline__ void wait_word(int word, u32 want) {
  unsigned spins = 0;
#pragma clang loop unroll(disable)
  while ((u32)__builtin_amdgcn_readfirstlane(lds_ld(L_FL + 4 * word)) < want)
    if (++spins > LSPIN_CAP) break;
}
__device__ __forceinline__ void wait_word_sleep(int word, u32 want) {
  unsigned spins = 0;
#pragma clang loop unroll(disable)
  while ((u32)__builtin_amdgcn_readfirstlane(lds_ld(L_FL + 4 * word)) < want) {
    __builtin_amdgcn_s_sleep(2);
    if (++spins > LSPIN_CAP) break;
  }
}
__device__ __forceinline__ void wait_all8(int word0, u32 want, int lane) {
  unsigned spins = 0;
#pragma clang loop unroll(disable)
  while (!__all(lds_ld(L_FL + 4 * (word0 + (lane & 7))) >= want)) {
    __builtin_amdgcn_s_sleep(1);
    if (++spins > LSPIN_CAP) break;
  }
}
__device__ __forceinline__ void cbar(u32 n, int lane) {
  asm volatile("s_waitcnt lgkmcnt(0)" ::: "memory");
  if (lane == 0) asm volatile("ds_add_u32 %0, %1" ::"v"((u32)(L_FL + 4 * F_BAR)), "v"(1u) : "memory");
  wait_word(F_BAR, 8u * n);
}
__device__ __forceinline__ void hw_barrier() { __builtin_amdgcn_sched_barrier(0); __builtin_amdgcn_s_barrier(); __builtin_amdgcn_sched_barrier(0); }
__device__ __forceinline__ void wg_barrier() {
  asm volatile("s_waitcnt lgkmcnt(0)" ::: "memory");
  hw_barrier();
  asm volatile("" ::: "memory");
}
__device__ __forceinline__ void glds16(const void* sbase, u32 voff, u32 lds_dst) {
  asm volatile("s_mov_b32 m0, %2\n\ts_nop 0\n\tglobal_load_lds_dwordx4 %0, %1 nt" ::"v"(voff), "s"(sbase), "s"(lds_dst) : "memory");
}
__device__ __forceinline__ int vswz(int row) { return ((row & 3) << 2) | ((row >> 2) & 3); }
__device__ __forceinline__ int swz(int row) { return ((row >> 1) & 1) | ((row >> 2) & 2); }
__device__ __forceinline__ u32 rowoff(int q, int stride, int lane) { const int row = 8 * q + (lane >> 3); return row * stride + (((lane & 7) ^ (swz(row) << 1)) & 7) * 16; }
template <int W> __device__ __forceinline__ void tile_dma(const unsigned char* a, int stride, u32 o0, u32 o1, u32 dst) {
  glds16(a, o0, dst); if (W >= 0) waitvm<W>();
  glds16(a, o1, dst + 1024); if (W >= 0) waitvm<W>();
  glds16(a + (size_t)16 * stride, o0, dst + 2048); if (W >= 0) waitvm<W>();
  glds16(a + (size_t)16 * stride, o1, dst + 3072); if (W >= 0) waitvm<W>();
}
__device__ __forceinline__ void tile_frags_lds(const unsigned char* lds, u32 base, int lane, u32x4 (&f)[4]) {
  const int g = lane >> 4, q = (lane & 15) >> 2, p = lane & 3;
#pragma unroll
  for (int e = 0; e < 4; ++e) {
    u32x2 h[2];
#pragma unroll
    for (int t = 0; t < 2; ++t) {
      const int row = 8 * g + 4 * t + q;
      const u32 a = base + row * 128 + (((2 * e + (p >> 1)) ^ (swz(row) << 1)) << 4) + 8 * (p & 1);
      h[t] = __builtin_bit_cast(u32x2, __builtin_amdgcn_ds_read_tr16_b64_v4i16((LAS s16x4*)(lds + a)));
    }
    f[e] = u32x4{h[0].x, h[0].y, h[1].x, h[1].y};
  }
}

__device__ __forceinline__ u32x4 afrag(u32x4 a, u32x4 b) {
  u32x4 f;
  f.x = __builtin_amdgcn_perm(a.y, a.x, 0x07060302u);
  f.y = __builtin_amdgcn_perm(a.w, a.z, 0x07060302u);
  f.z = __builtin_amdgcn_perm(b.y, b.x, 0x07060302u);
  f.w = __builtin_amdgcn_perm(b.w, b.z, 0x07060302u);
  return f;
}

struct RowStats { float m, rs; };
template <int N> __device__ __forceinline__ RowStats row_stats(const float (&v)[N], const bool (&valid)[N], float pivot) {
  float s1[2] = {0.f, 0.f}, s2[2] = {0.f, 0.f};
#pragma unroll
  for (int e = 0; e < N; ++e) { const float d = valid[e] ? v[e] - pivot : 0.f; s1[e & 1] += d; s2[e & 1] = fmaf(d, d, s2[e & 1]); }
  const float m1 = wave_sum(s1[0] + s1[1]) * (1.0f / DM), m2 = wave_sum(s2[0] + s2[1]) * (1.0f / DM);
  const float var = rbf(fmax_(fmaf(-m1, m1, m2), 0.f));
  RowStats r;
  r.m = rbf(pivot + m1);
  r.rs = __builtin_amdgcn_rcpf(rbf(__builtin_amdgcn_sqrtf(rbf(var + 1e-5f))));
  return r;
}
__device__ __forceinline__ float ln_apply(float v, RowStats s) { return rbf(v - s.m) * s.rs; }

enum { ROLE_W = 0, ROLE_G = 1, ROLE_F = 2 };

__global__ void __launch_bounds__(NTHR, 3) fused_kernel(Params p) {
  extern __shared__ __attribute__((aligned(16))) unsigned char lds[];
  const int tid = threadIdx.x, lane = tid & 63, w = __builtin_amdgcn_readfirstlane(tid >> 6);
  const int wg = blockIdx.x, xcd = wg & 7, s = wg >> 3, j = xcd + 8 * (s >> 4), i = s & 15, b = i >> 1, half = i & 1;
  const size_t ubase = ((size_t)(b * HKV + j) * SK + (SK - WIN)) * DH;
  const int cq = i & 3, kp = i >> 2;
  const int ks0 = kp < 3 ? 4 * kp : 13, nt = kp < 2 ? 4 : 5;
  const u32 PK = 4 * nt, PV = PK + 128, PW = PK + 256;
  const int g4 = lane >> 4, c16 = lane & 15;
  const int cw = (w + 7) & 7;
  u32 xr[5];
  u32x4 xa;
  auto load_x = [&]() {
    const GAS u32* xrow = (const GAS u32*)(p.x + cw * DM);
#pragma unroll
    for (int m = 0; m < 4; ++m) xr[m] = __builtin_nontemporal_load(xrow + lane + 64 * m);
    xr[4] = __builtin_nontemporal_load(xrow + (lane & 31) + 256);
    xa = __builtin_nontemporal_load((const GAS u32x4*)(p.x + (lane & 7) * DM + 32 * (ks0 + (cw < nt ? cw : 0)) + 8 * g4));
  };
  if (w == 0) {
    if (lane < 32) lds_st(L_FL + 4 * lane, 0u);
    asm volatile("s_waitcnt lgkmcnt(0)" ::: "memory");
  }
  hw_barrier();

  if (w == 0) {
    __builtin_amdgcn_s_setprio(3);
    u32 pub = 0;
#define PUBLISH(W) lds_st(L_FL + 4 * F_LANDED, pub - (W))
    {
      const unsigned char* a = (const unsigned char*)(p.wq + (size_t)32 * ks0 * NQ + 256 * j + 64 * cq);
      const u32 o0 = rowoff(0, NQ * 2, lane), o1 = rowoff(1, NQ * 2, lane);
#pragma unroll
      for (int t = 0; t < 4; ++t) tile_dma<-1>(a + (size_t)t * 32 * NQ * 2, NQ * 2, o0, o1, L_EXT + t * 4096);
      if (nt == 5) tile_dma<-1>(a + (size_t)4 * 32 * NQ * 2, NQ * 2, o0, o1, L_QSUM);
      pub = PK;
    }
    {
      const unsigned char* kb = (const unsigned char*)(p.kc + ubase + (size_t)half * 512 * DH);
      u32 koff[4];
#pragma unroll
      for (int q = 0; q < 4; ++q) { const int key = 4 * q + g4; koff[q] = key * 256 + ((c16 ^ key) & 15) * 16; }
      auto kseg = [&](int t0, int t1) {
#pragma clang loop unroll(disable)
        for (int t = t0; t < t1; ++t) {
#pragma unroll
          for (int q = 0; q < 4; ++q) { glds16(kb + t * 4096, koff[q], (t * 4 + q) * 1024); waitvm<LWIN>(); }
          pub += 4;
          if (pub > LWIN) PUBLISH(LWIN);
        }
      };
      kseg(0, 32);
    }
    wait_word_sleep(F_RANK, 1u);
    const int rank = (int)__builtin_amdgcn_readfirstlane(lds_ld(L_FL + 4 * F_RANK)) - 1;
    const int role = rank < 72 ? ROLE_G : (rank < 216 ? ROLE_W : ROLE_F);
    const u32 go0 = rowoff(0, FF * 2, lane), go1 = rowoff(1, FF * 2, lane);
    auto gtile = [&](int t) -> const unsigned char* { return (const unsigned char*)(((t & 1) ? p.wu : p.wg) + (size_t)(t >> 1) * 32 * FF + 64 * rank); };
    {
      const unsigned char* vb = (const unsigned char*)(p.vc + ubase + (size_t)half * 512 * DH);
      u32 voff[4];
#pragma unroll
      for (int q = 0; q < 4; ++q) { const int key = 4 * q + g4; voff[q] = key * 256 + ((c16 ^ vswz(key)) & 15) * 16; }
      wait_word(F_QPUB, 1u);
#pragma clang loop unroll(disable)
      for (int t = 0; t < 32; ++t) {
        if ((t & 7) == 4) wait_all8(F_KDONE, (t >> 3) + 1, lane);
        const u32 dst = t < 4 ? L_EXT + t * 4096 : (t - 4) * 4096;
#pragma unroll
        for (int q = 0; q < 4; ++q) { glds16(vb + t * 4096, voff[q], dst + q * 1024); waitvm<LWIN>(); }
        pub += 4; PUBLISH(LWIN);
        if (role == ROLE_G && t >= 32 - GEARLY) {
          const int tau = t - (32 - GEARLY);
          if (tau == 0) wait_all8(F_VDONE, 1, lane);
          tile_dma<LWIN>(gtile(tau), FF * 2, go0, go1, tau < 4 ? L_EXT + tau * 4096 : (tau - 4) * 4096);
          pub += 4; PUBLISH(LWIN);
        }
      }
    }
    if (role == ROLE_W) {
      const int jw = (rank - 72) / 9, cg = (rank - 72) % 9;
      const unsigned char* a = (const unsigned char*)(p.wo + (size_t)256 * jw * DM + 64 * cg);
      const u32 o0 = rowoff(0, DM * 2, lane), o1 = rowoff(1, DM * 2, lane);
      wait_all8(F_VDONE, 1, lane);
#pragma clang loop unroll(disable)
      for (int t = 0; t < 8; ++t) { tile_dma<-1>(a + (size_t)t * 32 * DM * 2, DM * 2, o0, o1, t * 4096); pub += 4; }
    } else if (role == ROLE_G) {
      static_assert(GEARLY >= 1 && 80 - GWIN >= 4 * GEARLY - 4, "the last tile of V is published before the loader waits for its consumption");
#pragma clang loop unroll(disable)
      for (int n = GEARLY; n < 36; ++n) {
        if (n == 20) wait_all8(F_VDONE, 2, lane);
        const int t = n < 16 ? n : (n < 20 ? n + 16 : n - 4);
        tile_dma<GWIN>(gtile(t), FF * 2, go0, go1, t < 4 ? L_EXT + t * 4096 : (t - 4) * 4096);
        pub += 4;
        if (4 * (n - GEARLY) + 4 > GWIN - LWIN) PUBLISH(GWIN);
      }
    }
#define DR(N) if ((role == ROLE_G ? GWIN : (role == ROLE_W ? LWIN + 32 : LWIN)) > N) { waitvm<N>(); PUBLISH(N); }
    static_assert(LWIN + 32 <= 60 && GWIN <= 60, "vmcnt");
    DR(56) DR(52) DR(48) DR(44) DR(40) DR(36) DR(32) DR(28) DR(24) DR(20) DR(16) DR(12) DR(8) DR(4) DR(0)
#undef DR
#undef PUBLISH
    return;
  }

  load_x();
  const int ct = tid - 64;
  const rsrc_t rs = __builtin_amdgcn_make_buffer_rsrc((void*)p.ws, 0, WS_END, 0x00020000);
  const u32 T = *(const __attribute__((address_space(4))) u32*)(p.ws + WS_EPOCH) + 1u;
  const u32 T16 = T & 0xFFFFu, T8 = T & 0xFFu, T4 = T & 0xFu;
  const u32 tstart = (u32)now();
  if (ct == 448) __builtin_amdgcn_raw_buffer_store_b64(u32x2{tstart, T}, rs, WS_RANK + wg * 8, 0, AUX_SC1);
  float* const lf = (float*)lds;
  u32* const lu = (u32*)lds;
  {
    float v[10]; bool valid[10];
#pragma unroll
    for (int m = 0; m < 5; ++m) { valid[2 * m] = valid[2 * m + 1] = (m < 4) || (lane < 32); v[2 * m] = bflo(xr[m]); v[2 * m + 1] = bfhi(xr[m]); }
    const float pivot = __uint_as_float(__builtin_amdgcn_readfirstlane(xr[0]) << 16);
    const RowStats st = row_stats<10>(v, valid, pivot);
    if (lane == 0) *(f32x2*)(lf + L_ST / 4 + 2 * cw) = f32x2{st.m, st.rs};
    cbar(1, lane);
  }
  if (cw < nt) {
    const f32x2 sv = *(const f32x2*)(lf + L_ST / 4 + 2 * (lane & 7));
    const RowStats st = {sv.x, sv.y};
    u32x4 a;
#pragma unroll
    for (int q = 0; q < 4; ++q) a[q] = pack2(ln_apply(bflo(xa[q]), st), ln_apply(bfhi(xa[q]), st));
    const u32 base = cw < 4 ? L_EXT + cw * 4096 : L_QSUM;
    wait_word(F_LANDED, 4 * (cw + 1));
    u32x4 f[4]; tile_frags_lds(lds, base, lane, f);
    f32x4 d[4];
#pragma unroll
    for (int e = 0; e < 4; ++e) d[e] = mfma(f[e], a, f32x4{0.f, 0.f, 0.f, 0.f});
    if (c16 < 8) {
#pragma unroll
      for (int e = 0; e < 4; ++e) *(f32x4*)(lf + base / 4 + c16 * RS + 16 * e + 4 * g4) = d[e];
    }
  }
  cbar(2, lane);
  {
    float sum = (lf[L_EXT / 4 + cw * RS + lane] + lf[L_EXT / 4 + 1024 + cw * RS + lane]) + (lf[L_EXT / 4 + 2048 + cw * RS + lane] + lf[L_EXT / 4 + 3072 + cw * RS + lane]);
    if (nt == 5) sum += lf[L_QSUM / 4 + cw * RS + lane];
    const u32 v = put_tag<0xFu>(sum, T4);
    const u32x4 o = {v, (u32)__builtin_amdgcn_mov_dpp((int)v, 0x55, 0xf, 0xf, true), (u32)__builtin_amdgcn_mov_dpp((int)v, 0xAA, 0xf, 0xf, true),
                     (u32)__builtin_amdgcn_mov_dpp((int)v, 0xFF, 0xf, 0xf, true)};
    if ((lane & 3) == 0) st16c(rs, WS_QS + ((((j * 8 + cw) * 4 + kp) * 256) + 64 * cq + lane) * 4, o);
  }
  cbar(3, lane);
  if (ct == 0) lds_st(L_FL + 4 * F_QPUB, 1u);
  if (cw >= 2 && cw < 6) {
    u32x4 qg;
    __builtin_amdgcn_s_sleep(QDELAY);
    {
      unsigned spins = 0;
#pragma clang loop unroll(disable)
      for (;;) {
        qg = ld16c(rs, WS_QS + (((j * 8 + b) * 4 + (cw - 2)) * 256 + 4 * lane) * 4);
        if (__all(tag_ok<0xFu>(qg, T4)) || ++spins > SPIN_CAP) break;
        __builtin_amdgcn_s_sleep(4);
      }
    }
    *(f32x4*)(lf + L_QSUM / 4 + (cw - 2) * 256 + 4 * lane) = f32x4{get_tag<0xFu>(qg.x), get_tag<0xFu>(qg.y), get_tag<0xFu>(qg.z), get_tag<0xFu>(qg.w)};
  }
  if (cw == 7) {
    u32x4 e0, e1; unsigned spins = 0;
#pragma clang loop unroll(disable)
    for (;;) {
      e0 = ld16c(rs, WS_RANK + lane * 32); e1 = ld16c(rs, WS_RANK + lane * 32 + 16);
      if (__all((e0.y == T) & (e0.w == T) & (e1.y == T) & (e1.w == T)) || ++spins > SPIN_CAP) break;
      __builtin_amdgcn_s_sleep(4);
    }
    auto before = [&](u32 t, int g) -> float { const int d = (int)(t - tstart); return (d < 0 || (d == 0 && g < wg)) ? 1.f : 0.f; };
    const float n = wave_sum((before(e0.x, 4 * lane) + before(e0.z, 4 * lane + 1)) + (before(e1.x, 4 * lane + 2) + before(e1.z, 4 * lane + 3)));
    if (lane == 0) lds_st(L_FL + 4 * F_RANK, (u32)n + 1u);
  }
  cbar(4, lane);
  if (cw == 0) {
    f32x4 sv[4];
#pragma unroll
    for (int q = 0; q < 4; ++q) sv[q] = *(const f32x4*)(lf + L_QSUM / 4 + q * 256 + 4 * lane);
    const f32x4 sum = (sv[0] + sv[1]) + (sv[2] + sv[3]);
    *(u32x2*)(lu + L_Q / 4 + 2 * lane) = u32x2{pack2(sum.x, sum.y), pack2(sum.z, sum.w)};
    asm volatile("s_waitcnt lgkmcnt(0)" ::: "memory");
    if (lane == 0) lds_st(L_FL + 4 * F_QRDY, 1u);
  } else {
    wait_word(F_QRDY, 1u);
  }
  float s0, s1;
  {
    u32x4 qa[4];
#pragma unroll
    for (int ks = 0; ks < 4; ++ks) qa[ks] = *(const u32x4*)(lu + L_Q / 4 + (lane & 1) * 64 + 16 * ks + 4 * g4);
    f32x4 d[4];
#pragma unroll
    for (int m = 0; m < 4; ++m) {
      const int tile = cw + 8 * m;
      wait_word_sleep(F_LANDED, PK + 4 * (tile + 1));
      u32x4 kf[4];
#pragma unroll
      for (int ks = 0; ks < 4; ++ks) kf[ks] = *(const u32x4*)(lds + tile * 4096 + c16 * 256 + (((4 * ks + g4) ^ c16) << 4));
      f32x4 acc = mfma(qa[0], kf[0], f32x4{0.f, 0.f, 0.f, 0.f});
      acc = mfma(qa[1], kf[1], acc); acc = mfma(qa[2], kf[2], acc); acc = mfma(qa[3], kf[3], acc);
      d[m] = acc;
      asm volatile("s_waitcnt lgkmcnt(0)" ::: "memory");
      if (lane == 0) lds_st(L_FL + 4 * (F_KDONE + cw), m + 1);
    }
    const float x0 = g4 == 0 ? d[0][0] : g4 == 1 ? d[1][0] : g4 == 2 ? d[2][0] : d[3][0];
    const float x1 = g4 == 0 ? d[0][1] : g4 == 1 ? d[1][1] : g4 == 2 ? d[2][1] : d[3][1];
    constexpr float SCALE = 0.08838834764831845f;
    s0 = rbf(rbf(x0) * SCALE); s1 = rbf(rbf(x1) * SCALE);
  }
  u32x4 mine;
  {
    constexpr float L2E = 1.4426950408889634f;
    const float m0 = wave_max(s0), m1 = wave_max(s1);
    const float l0 = wave_sum(__builtin_amdgcn_exp2f((s0 - m0) * L2E)), l1 = wave_sum(__builtin_amdgcn_exp2f((s1 - m1) * L2E));
    if (lane == 0) *(f32x4*)(lf + L_ST / 4 + 16 + 4 * cw) = f32x4{m0, m1, l0, l1};
    cbar(5, lane);
    float M0 = -INFINITY, M1 = -INFINITY;
    f32x4 st[8];
#pragma unroll
    for (int ww = 0; ww < 8; ++ww) { st[ww] = *(const f32x4*)(lf + L_ST / 4 + 16 + 4 * ww); M0 = fmax_(M0, st[ww].x); M1 = fmax_(M1, st[ww].y); }
    float L0 = 0.f, L1 = 0.f;
#pragma unroll
    for (int ww = 0; ww < 8; ++ww) {
      L0 = fmaf(st[ww].z, __builtin_amdgcn_exp2f((st[ww].x - M0) * L2E), L0);
      L1 = fmaf(st[ww].w, __builtin_amdgcn_exp2f((st[ww].y - M1) * L2E), L1);
    }
    const int kl = 16 * (cw + 8 * g4) + c16;
    __bf16* P = (__bf16*)(lds + L_P);
    P[kl] = (__bf16)__builtin_amdgcn_exp2f((s0 - M0) * L2E);
    P[512 + kl] = (__bf16)__builtin_amdgcn_exp2f((s1 - M1) * L2E);
    mine = u32x4{__float_as_uint(M0) | T16, __float_as_uint(M1) | T16, put_tag<0xFFu>(L0, T8), put_tag<0xFFu>(L1, T8)};
    if (ct == 64) st16c(rs, WS_SC + wg * 128, mine);
  }
  cbar(6, lane);
  wait_word(F_RANK, 1u);
  const int rank = (int)__builtin_amdgcn_readfirstlane(lds_ld(L_FL + 4 * F_RANK)) - 1;
  const int role = rank < 72 ? ROLE_G : (rank < 216 ? ROLE_W : ROLE_F);
  {
    const int q4 = (lane & 15) >> 2, p4 = lane & 3;
    auto vfrag = [&](u32 base, int e) -> u32x4 {
      u32x2 h[2];
#pragma unroll
      for (int t = 0; t < 2; ++t) {
        const int row = 8 * g4 + 4 * t + q4;
        const u32 a = base + row * 256 + (((2 * e + (p4 >> 1)) ^ vswz(row)) << 4) + 8 * (p4 & 1);
        h[t] = __builtin_bit_cast(u32x2, __builtin_amdgcn_ds_read_tr16_b64_v4i16((LAS s16x4*)(lds + a)));
      }
      return u32x4{h[0].x, h[0].y, h[1].x, h[1].y};
    };
    auto pfrag = [&](int ks) -> u32x4 { return *(const u32x4*)(lds + L_P + (lane & 1) * 1024 + (32 * ks + 8 * g4) * 2); };
    {
      const u32x4 pf = pfrag(cw);
      const u32 base = cw < 2 ? L_EXT + cw * 8192 : (cw - 2) * 8192;
      wait_word(F_LANDED, PV + 8 * (cw + 1));
      u32x4 f[8];
#pragma unroll
      for (int e = 0; e < 8; ++e) f[e] = vfrag(base, e);
      SB();
      f32x4 d[8];
#pragma unroll
      for (int e = 0; e < 8; ++e) d[e] = mfma(f[e], pf, f32x4{0.f, 0.f, 0.f, 0.f});
      asm volatile("s_waitcnt lgkmcnt(0)" ::: "memory");
      if (lane == 0) lds_st(L_FL + 4 * (F_VDONE + cw), 1u);
      if (c16 < 2) {
#pragma unroll
        for (int e = 0; e < 8; ++e) *(f32x4*)(lf + L_QSUM / 4 + cw * 256 + c16 * 128 + 16 * e + 4 * g4) = d[e];
      }
    }
    cbar(7, lane);
    f32x4 acc;
    {
      f32x4 sv[8];
#pragma unroll
      for (int ww = 0; ww < 8; ++ww) sv[ww] = *(const f32x4*)(lf + L_QSUM / 4 + ww * 256 + (lane & 1) * 128 + 16 * cw + 4 * g4);
      acc = ((sv[0] + sv[1]) + (sv[2] + sv[3])) + ((sv[4] + sv[5]) + (sv[6] + sv[7]));
    }
    u32x4 oth = {0u, 0u, 0u, 0u};
    bool got = cw != 1;
    if (cw == 1) oth = ld16c(rs, WS_SC + (wg ^ 8) * 128);
    auto try_stats = [&](bool last) {
      if (got) return;
      unsigned spins = 0;
#pragma clang loop unroll(disable)
      for (;;) {
        if (__all(((((oth.x ^ T16) | (oth.y ^ T16)) & 0xFFFFu) | (((oth.z ^ T8) | (oth.w ^ T8)) & 0xFFu)) == 0u) || ++spins > SPIN_CAP) { got = true; break; }
        oth = ld16c(rs, WS_SC + (wg ^ 8) * 128);
        if (!last) return;
      }
      constexpr float L2E = 1.4426950408889634f;
      const float M0 = bfhi(mine.x), M1 = bfhi(mine.y), om0 = bfhi(oth.x), om1 = bfhi(oth.y);
      const float G0 = fmax_(M0, om0), G1 = fmax_(M1, om1);
      const float e0 = __builtin_amdgcn_exp2f((M0 - G0) * L2E), e1 = __builtin_amdgcn_exp2f((M1 - G1) * L2E);
      const float S0 = get_tag<0xFFu>(mine.z) * e0 + get_tag<0xFFu>(oth.z) * __builtin_amdgcn_exp2f((om0 - G0) * L2E);
      const float S1 = get_tag<0xFFu>(mine.w) * e1 + get_tag<0xFFu>(oth.w) * __builtin_amdgcn_exp2f((om1 - G1) * L2E);
      if (lane == 0) { *(f32x2*)(lf + L_ST / 4 + 48) = f32x2{e0 / S0, e1 / S1}; asm volatile("s_waitcnt lgkmcnt(0)" ::: "memory"); lds_st(L_FL + 4 * F_FAC, 1u); }
    };
#pragma unroll
    for (int ks = 8; ks < 16; ++ks) {
      const u32x4 pf = pfrag(ks);
      const int tl = 2 * ks + 1;
      wait_word(F_LANDED, PV + 4 * (tl + 1) + ((role == ROLE_G && tl > 32 - GEARLY) ? 4 * (tl - (32 - GEARLY)) : 0));
      acc = mfma(vfrag((ks - 2) * 8192, cw), pf, acc);
      if (ks >= 9 && ks <= 13) try_stats(false);
      if (ks == 14) { try_stats(true); wait_word(F_FAC, 1u); }
    }
    const float fac = lf[L_ST / 4 + 48 + (lane & 1)];
    asm volatile("s_waitcnt lgkmcnt(0)" ::: "memory");
    if (lane == 0) lds_st(L_FL + 4 * (F_VDONE + cw), 2u);
    if (c16 < 2)
      st16c(rs, WS_OS + (((j * 8 + b) * 2 + half) * 256 + c16 * 128 + 16 * cw + 4 * g4) * 4,
            u32x4{put_tag<0xFFu>(acc.x * fac, T8), put_tag<0xFFu>(acc.y * fac, T8), put_tag<0xFFu>(acc.z * fac, T8), put_tag<0xFFu>(acc.w * fac, T8)});
  }
  auto gather4 = [&](u32 v) -> u32x4 {
    return u32x4{v, (u32)__builtin_amdgcn_mov_dpp((int)v, 0x55, 0xf, 0xf, true), (u32)__builtin_amdgcn_mov_dpp((int)v, 0xAA, 0xf, 0xf, true),
                 (u32)__builtin_amdgcn_mov_dpp((int)v, 0xFF, 0xf, 0xf, true)};
  };
  const unsigned long long to = now();

  auto acc_to_lds = [&](int base, const f32x4 (&d)[4]) {
    if (c16 < 8) {
#pragma unroll
      for (int e = 0; e < 4; ++e) *(f32x4*)(lf + base / 4 + (cw * 8 + c16) * RS + 16 * e + 4 * g4) = d[e];
    }
  };
  auto publish_partial = [&](int base, int jw, int cg) {
    float sv[8];
#pragma unroll
    for (int ww = 0; ww < 8; ++ww) sv[ww] = lf[L_RED_W / 4 + (ww * 8 + cw) * RS + lane];
    const float sum = ((sv[0] + sv[1]) + (sv[2] + sv[3])) + ((sv[4] + sv[5]) + (sv[6] + sv[7]));
    const u32x4 o = gather4(put_tag<0xFFu>(sum, T8));
    if ((lane & 3) == 0) st16c(rs, base + (((cw * 9 + cg) * 16 + jw) * 64 + lane) * 4, o);
  };

  if (role == ROLE_W) {
    const int jw = (rank - 72) / 9, cg = (rank - 72) % 9;
    u32x4 f[4];
    wait_word(F_LANDED, PW + 32);
    tile_frags_lds(lds, cw * 4096, lane, f);
    {
      const int off = WS_OS + ((jw * 8 + (lane & 7)) * 512 + 32 * cw + 8 * g4) * 4;
      u32x4 a0 = {0u, 0u, 0u, 0u}, a1 = a0, b0 = a0, b1 = a0;
      if (c16 < 8)
        POLL((a0 = ld16c(rs, off), a1 = ld16c(rs, off + 16), b0 = ld16c(rs, off + 1024), b1 = ld16c(rs, off + 1040),
              tag_ok<0xFFu>(a0, T8) & tag_ok<0xFFu>(a1, T8) & tag_ok<0xFFu>(b0, T8) & tag_ok<0xFFu>(b1, T8)));
#define S2(x, y) (get_tag<0xFFu>(x) + get_tag<0xFFu>(y))
      const u32x4 a = {pack2(S2(a0.x, b0.x), S2(a0.y, b0.y)), pack2(S2(a0.z, b0.z), S2(a0.w, b0.w)), pack2(S2(a1.x, b1.x), S2(a1.y, b1.y)), pack2(S2(a1.z, b1.z), S2(a1.w, b1.w))};
#undef S2
      f32x4 d[4];
#pragma unroll
      for (int e = 0; e < 4; ++e) d[e] = mfma(f[e], a, f32x4{0.f, 0.f, 0.f, 0.f});
      acc_to_lds(L_RED_W, d);
    }
    wg_barrier();
    publish_partial(WS_PW, jw, cg);
    const unsigned long long tw = now();
    u32x4 f2[4];
    PollRegs pr = {};
    {
      const unsigned char* a = (const unsigned char*)(p.wd + (size_t)(288 * jw + 32 * cw) * DM + 64 * cg);
      const u32 o0 = rowoff(0, DM * 2, lane), o1 = rowoff(1, DM * 2, lane);
      tile_dma<-1>(a, DM * 2, o0, o1, (8 + cw) * 4096);
      if (cw == 0) tile_dma<-1>(a + 256 * DM * 2, DM * 2, o0, o1, 16 * 4096);
      waitvm<0>();
      tile_frags_lds(lds, (8 + cw) * 4096, lane, f);
      if (cw == 0) tile_frags_lds(lds, 16 * 4096, lane, f2);
    }
    {
      const int off = WS_AS + ((lane & 7) * FF + 288 * jw + 32 * cw + 8 * g4) * 4;
      u32x4 R[4] = {};
      not_before(tw, GATE_ACT);
      if (c16 < 8) {
        if (cw == 0) poll4(pr, rs, off, off + 16, off + 1024, off + 1040, T16, 0xFFFFu, R);
        else poll2(pr, rs, off, T16, 0xFFFFu, R[0], R[1]);
      }
      const u32x4 a = afrag(R[0], R[1]);
      f32x4 d[4];
#pragma unroll
      for (int e = 0; e < 4; ++e) d[e] = mfma(f[e], a, f32x4{0.f, 0.f, 0.f, 0.f});
      if (cw == 0) {
        const u32x4 ab = afrag(R[2], R[3]);
#pragma unroll
        for (int e = 0; e < 4; ++e) d[e] = mfma(f2[e], ab, d[e]);
      }
      acc_to_lds(L_RED_W, d);
    }
    wg_barrier();
    publish_partial(WS_PD, jw, cg);
    poll_done(pr);
  } else if (role == ROLE_G) {
    const int gx = rank;
    u32x4 f[5][4];
#pragma unroll
    for (int tl = 0; tl < 5; ++tl) {
      if (tl < 4 || cw < 4) {
        const int tau = cw + 8 * tl;
        const int n = tau < 16 ? tau : (tau >= 32 ? tau - 16 : tau + 4);
        wait_word(F_LANDED, tau < GEARLY ? PV + 4 * (32 - GEARLY + tau + 1) + 4 * (tau + 1) : PW + 4 * (n + 1));
        tile_frags_lds(lds, tau < 4 ? L_EXT + tau * 4096 : (tau - 4) * 4096, lane, f[tl]);
      }
    }
    wait_word(F_LANDED, PW + 144);
    PollRegs pr;
    {
      const int off = WS_HS + (xcd * 8 + cw) * DM * 4;
      u32x4 h0, h1; u32 h2;
      not_before(to, GATE_H);
      poll3(pr, rs, off + lane * 32, off + 2048 + lane * 4, T16, 0xFFFFu, h0, h1, h2);
      float v[9]; bool valid[9];
#pragma unroll
      for (int e = 0; e < 4; ++e) { v[e] = bfhi(h0[e]); v[4 + e] = bfhi(h1[e]); }
      v[8] = bfhi(h2);
#pragma unroll
      for (int e = 0; e < 9; ++e) valid[e] = true;
      const float pivot = __uint_as_float(__builtin_amdgcn_readfirstlane(h0.x) & 0xFFFF0000u);
      const RowStats st = row_stats<9>(v, valid, pivot);
      u32* hn = lu + L_HN / 4 + cw * HN_STRIDE;
      *(u32x4*)(hn + 4 * lane) = u32x4{pack2(ln_apply(v[0], st), ln_apply(v[1], st)), pack2(ln_apply(v[2], st), ln_apply(v[3], st)),
                                       pack2(ln_apply(v[4], st), ln_apply(v[5], st)), pack2(ln_apply(v[6], st), ln_apply(v[7], st))};
      ((__bf16*)(hn + 256))[lane] = (__bf16)ln_apply(v[8], st);
    }
    wg_barrier();
    {
      f32x4 d[4];
      const u32* hn = lu + L_HN / 4 + (lane & 7) * HN_STRIDE + 16 * (cw >> 1) + 4 * g4;
      u32x4 a[5];
#pragma unroll
      for (int tl = 0; tl < 5; ++tl) a[tl] = *(const u32x4*)(hn + (tl < 4 || cw < 4 ? 64 * tl : 0));
      SB();
#pragma unroll
      for (int e = 0; e < 4; ++e) d[e] = mfma(f[0][e], a[0], f32x4{0.f, 0.f, 0.f, 0.f});
#pragma unroll
      for (int tl = 1; tl < 5; ++tl) {
        if (tl < 4 || cw < 4) {
#pragma unroll
          for (int e = 0; e < 4; ++e) d[e] = mfma(f[tl][e], a[tl], d[e]);
        }
      }
      acc_to_lds(0, d);
    }
    wg_barrier();
    {
      float gv[4], uv[4];
#pragma unroll
      for (int ww = 0; ww < 4; ++ww) { gv[ww] = lf[(2 * ww * 8 + cw) * RS + lane]; uv[ww] = lf[((2 * ww + 1) * 8 + cw) * RS + lane]; }
      const float gg = rbf((gv[0] + gv[1]) + (gv[2] + gv[3])), uu = rbf((uv[0] + uv[1]) + (uv[2] + uv[3]));
      const float sl = rbf(gg * __builtin_amdgcn_rcpf(1.0f + __builtin_amdgcn_exp2f(gg * -1.4426950408889634f)));
      const u32x4 o = gather4(bfbits_hi(sl * uu) | T16);
      if ((lane & 3) == 0) st16c(rs, WS_AS + (cw * FF + 64 * gx + lane) * 4, o);
    }
    poll_done(pr);
  } else {
    const int f = rank - 216;
    const int job = 2 * f + (cw >> 2);
    if ((cw & 3) == 0 && job < 72) {
      const int jb = job / 9, cg = job % 9;
      const u32x2 xq = *(const GAS u32x2*)(p.x + jb * DM + 64 * cg + 4 * c16);
      const u32x4 wr0 = *(const GAS u32x4*)(p.wr + 8 * lane);
      const u16 wr1 = ((const GAS u16*)p.wr)[512 + lane];
      PollRegs pr;
      auto gather16 = [&](int base) -> f32x4 {
        const int off = base + (((jb * 9 + cg) * 16 + g4) * 64 + 4 * c16) * 4;
        u32x4 R[4];
        poll4(pr, rs, off, off + 1024, off + 2048, off + 3072, T8, 0xFFu, R);
        const u32x4 q0 = R[0], q1 = R[1], q2 = R[2], q3 = R[3];
        f32x4 sum;
#pragma unroll
        for (int e = 0; e < 4; ++e) {
          float t = (get_tag<0xFFu>(q0[e]) + get_tag<0xFFu>(q1[e])) + (get_tag<0xFFu>(q2[e]) + get_tag<0xFFu>(q3[e]));
          t = swap16_add(t, t); t = swap32_add(t, t);
          sum[e] = t;
        }
        return sum;
      };
      not_before(to, GATE_PW);
      const f32x4 at = gather16(WS_PW);
      f32x4 hv;
      hv[0] = rbf(bflo(xq.x) + rbf(at[0])); hv[1] = rbf(bfhi(xq.x) + rbf(at[1]));
      hv[2] = rbf(bflo(xq.y) + rbf(at[2])); hv[3] = rbf(bfhi(xq.y) + rbf(at[3]));
      {
        const u32x4 hq = {__float_as_uint(hv[0]) | T16, __float_as_uint(hv[1]) | T16, __float_as_uint(hv[2]) | T16, __float_as_uint(hv[3]) | T16};
        const int o = WS_HS + ((g4 * 8 + jb) * DM + 64 * cg + 4 * c16) * 4;
        st16c(rs, o, hq); st16c(rs, o + 4 * 8 * DM * 4, hq);
      }
      const unsigned long long th = now();
      poll_done(pr);
      RowStats st; float sig;
      {
        const int off = WS_HS + (xcd * 8 + jb) * DM * 4;
        u32x4 h0, h1; u32 h2;
        {
          unsigned spins = 0;
#pragma clang loop unroll(disable)
          for (;;) {
            h0 = ld16c(rs, off + lane * 32); h1 = ld16c(rs, off + lane * 32 + 16); h2 = ld4c(rs, off + 2048 + lane * 4);
            if (__all(tag_ok<0xFFFFu>(h0, T16) & tag_ok<0xFFFFu>(h1, T16) & ((h2 & 0xFFFFu) == T16)) || ++spins > SPIN_CAP) break;
            __builtin_amdgcn_s_sleep(2);
          }
        }
        float v[9]; bool valid[9];
#pragma unroll
        for (int e = 0; e < 4; ++e) { v[e] = bfhi(h0[e]); v[4 + e] = bfhi(h1[e]); }
        v[8] = bfhi(h2);
#pragma unroll
        for (int e = 0; e < 9; ++e) valid[e] = true;
        const float pivot = __uint_as_float(__builtin_amdgcn_readfirstlane(h0.x) & 0xFFFF0000u);
        st = row_stats<9>(v, valid, pivot);
        float r = rbf(ln_apply(v[8], st)) * bflo((u32)wr1);
#pragma unroll
        for (int e = 0; e < 8; ++e) r = fmaf(rbf(ln_apply(v[e], st)), (e & 1) ? bfhi(wr0[e >> 1]) : bflo(wr0[e >> 1]), r);
        r = rbf(wave_sum(r));
        sig = rbf(__builtin_amdgcn_rcpf(1.0f + __builtin_amdgcn_exp2f(r * -1.4426950408889634f)));
      }
      float hn[4];
#pragma unroll
      for (int e = 0; e < 4; ++e) hn[e] = rbf(ln_apply(hv[e], st));
      not_before(th, GATE_PD);
      const f32x4 y = gather16(WS_PD);
      float o[4];
#pragma unroll
      for (int e = 0; e < 4; ++e) o[e] = hv[e] + rbf(hn[e] + rbf(rbf(y[e]) * sig));
      if (lane < 16) *(GAS u32x2*)(p.out + jb * DM + 64 * cg + 4 * lane) = u32x2{pack2(o[0], o[1]), pack2(o[2], o[3])};
      poll_done(pr);
      if (job == 0 && lane == 0) __hip_atomic_store((GAS u32*)(p.ws + WS_EPOCH), T, __ATOMIC_RELAXED, __HIP_MEMORY_SCOPE_AGENT);
    }
  }
}

}

static inline size_t ws_offset(size_t ws_size) { return (ws_size - (8u << 20)) & ~(size_t)4095; }
extern "C" void kernel_launch(void* const* d_in, const int* in_sizes, int n_in, void* d_out, int out_size, void* d_ws, size_t ws_size,
                              hipStream_t stream) {
  Params p;
  p.x = (const u16*)d_in[0];
  p.kc = (const u16*)d_in[1];
  p.vc = (const u16*)d_in[2];
  p.wq = (const u16*)d_in[3];
  p.wo = (const u16*)d_in[4];
  p.wr = (const u16*)d_in[6];
  p.wg = (const u16*)d_in[7];
  p.wu = (const u16*)d_in[8];
  p.wd = (const u16*)d_in[9];
  p.out = (u16*)d_out;
  p.ws = (unsigned char*)d_ws + ws_offset(ws_size);
  (void)hipFuncSetAttribute((const void*)fused_kernel, hipFuncAttributeMaxDynamicSharedMemorySize, LDS_BYTES);
  hipLaunchKernelGGL(fused_kernel, dim3(NWG), dim3(NTHR), LDS_BYTES, stream, p);
}
```

```cpp
#include <hip/hip_runtime.h>
#include <stdint.h>

typedef unsigned u32;
typedef unsigned short u16;
typedef u32 u32x4 __attribute__((ext_vector_type(4)));
typedef u32 u32x2 __attribute__((ext_vector_type(2)));
typedef float f32x4 __attribute__((ext_vector_type(4)));
typedef float f32x2 __attribute__((ext_vector_type(2)));
typedef short s16x4 __attribute__((ext_vector_type(4)));
typedef __bf16 bf16x8 __attribute__((ext_vector_type(8)));
typedef __bf16 bf16x2 __attribute__((ext_vector_type(2)));
#define GAS __attribute__((address_space(1)))
#define LAS __attribute__((address_space(3)))

namespace {

constexpr int DM = 576, HKV = 16, SK = 4096, DH = 128, WIN = 1024, NQ = 4096, FF = 4608;
constexpr int NWG = 256, NTHR = 576;

constexpr int LWIN = 24;
constexpr int GWIN = 40;
constexpr int GEARLY = 4;
constexpr int QDELAY = 32;
constexpr int PSTAG = 6;
constexpr int PSLEEP = 1;
constexpr unsigned GATE_PW = 90, GATE_H = 150, GATE_ACT = 220, GATE_PD = 270;

constexpr int WS_EPOCH = 0;
constexpr int WS_RANK = 2048;
constexpr int WS_QS = 4096;
constexpr int WS_SC = WS_QS + 16 * 8 * 4 * 256 * 4;
constexpr int WS_OS = WS_SC + 256 * 128;
constexpr int WS_PW = WS_OS + 16 * 8 * 2 * 256 * 4;
constexpr int HREP = 8;
constexpr int WS_HS = WS_PW + 8 * 9 * 16 * 64 * 4;
constexpr int WS_AS = WS_HS + HREP * 8 * 576 * 4;
constexpr int WS_PD = WS_AS + 8 * 4608 * 4;
constexpr int WS_END = WS_PD + 8 * 9 * 16 * 64 * 4;
constexpr int AUX_SC1 = 16;
constexpr unsigned SPIN_CAP = 1u << 16, LSPIN_CAP = 1u << 22;

constexpr int L_EXT = 128 * 1024;
constexpr int L_FL = 144 * 1024;
constexpr int L_ST = L_FL + 128;
constexpr int L_P = L_ST + 256;
constexpr int L_QSUM = L_P + 2048;
constexpr int L_Q = L_QSUM + 8192;
constexpr int L_HN = L_P;
constexpr int HN_STRIDE = 292;
constexpr int RS = 68;
constexpr int L_RED_W = 112 * 1024;
constexpr int LDS_BYTES = 160 * 1024;
static_assert(L_Q + 512 <= LDS_BYTES && L_HN + 8 * HN_STRIDE * 4 <= LDS_BYTES, "LDS");
enum { F_LANDED = 0, F_QPUB = 1, F_BAR = 2, F_QRDY = 3, F_RANK = 4, F_FAC = 5, F_KDONE = 8, F_VDONE = 16 };

struct Params {
  const u16 *x, *kc, *vc, *wq, *wo, *wr, *wg, *wu, *wd;
  u16* out;
  unsigned char* ws;
};

__device__ __forceinline__ float bflo(u32 d) { return __uint_as_float(d << 16); }
__device__ __forceinline__ float bfhi(u32 d) { return __uint_as_float(d & 0xFFFF0000u); }
__device__ __forceinline__ float rbf(float f) { return (float)(__bf16)f; }
__device__ __forceinline__ u32 bfbits_hi(float f) { return __float_as_uint(rbf(f)); }
__device__ __forceinline__ u32 pack2(float lo, float hi) {
  bf16x2 v = {(__bf16)lo, (__bf16)hi};
  return __builtin_bit_cast(u32, v);
}
template <int CTRL> __device__ __forceinline__ float dpp(float x) {
  return __uint_as_float((u32)__builtin_amdgcn_mov_dpp((int)__float_as_uint(x), CTRL, 0xf, 0xf, true));
}
constexpr int XOR1 = 0xB1, XOR2 = 0x4E, XOR7 = 0x141, XOR8 = 0x128;
__device__ __forceinline__ float swap32_add(float a, float b) {
  auto r = __builtin_amdgcn_permlane32_swap(__float_as_uint(a), __float_as_uint(b), false, false);
  return __uint_as_float(r[0]) + __uint_as_float(r[1]);
}
__device__ __forceinline__ float swap16_add(float a, float b) {
  auto r = __builtin_amdgcn_permlane16_swap(__float_as_uint(a), __float_as_uint(b), false, false);
  return __uint_as_float(r[0]) + __uint_as_float(r[1]);
}
__device__ __forceinline__ float wave_sum(float x) {
  x += dpp<XOR1>(x); x += dpp<XOR2>(x); x += dpp<XOR7>(x); x += dpp<XOR8>(x);
  x = swap16_add(x, x); x = swap32_add(x, x);
  return x;
}
__device__ __forceinline__ float fmax_(float a, float b) { return __builtin_amdgcn_fmed3f(a, b, INFINITY); }
__device__ __forceinline__ float wave_max(float x) {
  x = fmax_(x, dpp<XOR1>(x)); x = fmax_(x, dpp<XOR2>(x)); x = fmax_(x, dpp<XOR7>(x)); x = fmax_(x, dpp<XOR8>(x));
  auto r = __builtin_amdgcn_permlane16_swap(__float_as_uint(x), __float_as_uint(x), false, false);
  x = fmax_(__uint_as_float(r[0]), __uint_as_float(r[1]));
  auto t = __builtin_amdgcn_permlane32_swap(__float_as_uint(x), __float_as_uint(x), false, false);
  return fmax_(__uint_as_float(t[0]), __uint_as_float(t[1]));
}
__device__ __forceinline__ f32x4 mfma(u32x4 a, u32x4 b, f32x4 c) {
  return __builtin_amdgcn_mfma_f32_16x16x32_bf16(__builtin_bit_cast(bf16x8, a), __builtin_bit_cast(bf16x8, b), c, 0, 0, 0);
}
#define SB() __builtin_amdgcn_sched_barrier(0)
template <int N> __device__ __forceinline__ void waitvm() { asm volatile("s_waitcnt vmcnt(%0)" ::"n"(N) : "memory"); }
typedef __amdgpu_buffer_rsrc_t rsrc_t;
__device__ __forceinline__ u32x4 ld16c(rsrc_t rs, int off) { return __builtin_amdgcn_raw_buffer_load_b128(rs, off, 0, AUX_SC1); }
__device__ __forceinline__ u32 ld4c(rsrc_t rs, int off) { return __builtin_amdgcn_raw_buffer_load_b32(rs, off, 0, AUX_SC1); }
__device__ __forceinline__ void st16c(rsrc_t rs, int off, u32x4 v) { __builtin_amdgcn_raw_buffer_store_b128(v, rs, off, 0, AUX_SC1); }
template <u32 M> __device__ __forceinline__ bool tag_ok(u32x4 v, u32 t) { return ((((v.x ^ t) | (v.y ^ t)) | ((v.z ^ t) | (v.w ^ t))) & M) == 0u; }
template <u32 M> __device__ __forceinline__ u32 put_tag(float f, u32 t) { return ((__float_as_uint(f) + (M + 1) / 2) & ~M) | t; }
template <u32 M> __device__ __forceinline__ float get_tag(u32 d) { return __uint_as_float(d & ~M); }
#define POLL(cond_load) do { unsigned spins_ = 0; _Pragma("clang loop unroll(disable)") while (!(cond_load) && ++spins_ < SPIN_CAP) __builtin_amdgcn_s_sleep(PSLEEP); } while (0)

struct PollRegs { u32x4 a0, a1, a2, a3, b0, b1, b2, b3; };
#define PV_OUT(p) "=&{v[128:131]}"(p.a0), "=&{v[132:135]}"(p.a1), "=&{v[136:139]}"(p.a2), "=&{v[140:143]}"(p.a3), \
                  "=&{v[144:147]}"(p.b0), "=&{v[148:151]}"(p.b1), "=&{v[152:155]}"(p.b2), "=&{v[156:159]}"(p.b3)
#define PV_CLOB "v160", "v161", "v162", "v163", "vcc", "scc", "memory"
__device__ __forceinline__ void poll_done(const PollRegs& p) {
  asm volatile("s_waitcnt vmcnt(0)" ::"{v[128:131]}"(p.a0), "{v[132:135]}"(p.a1), "{v[136:139]}"(p.a2), "{v[140:143]}"(p.a3),
               "{v[144:147]}"(p.b0), "{v[148:151]}"(p.b1), "{v[152:155]}"(p.b2), "{v[156:159]}"(p.b3) : "memory");
}
#define PV_LD4(r0, r1, r2, r3, r4, r5, r6, r7) \
  "buffer_load_dwordx4 v[" r0 ":" r1 "], %[o0], %[rs], 0 offen sc1\n\t" "buffer_load_dwordx4 v[" r2 ":" r3 "], %[o1], %[rs], 0 offen sc1\n\t" \
  "buffer_load_dwordx4 v[" r4 ":" r5 "], %[o2], %[rs], 0 offen sc1\n\t" "buffer_load_dwordx4 v[" r6 ":" r7 "], %[o3], %[rs], 0 offen sc1\n\t"
#define PV_X4(t, a, b, c, d) "v_xor_b32 " t ", %[T], v" a "\n\t" "v_bitop3_b32 " t ", " t ", v" b ", %[T] bitop3:0xf6\n\t" "v_bitop3_b32 " t ", " t ", v" c ", %[T] bitop3:0xf6\n\t" "v_bitop3_b32 " t ", " t ", v" d ", %[T] bitop3:0xf6\n\t"
#define PV_TEST "v_and_b32 v160, %[M], v160\n\tv_cmp_ne_u32 vcc, 0, v160\n\ts_cmp_eq_u64 vcc, 0\n\t"
#define PV_MOV2(o, a, b) "v_mov_b64 " o ", v[" a ":" b "]\n\t"
__device__ __forceinline__ void poll4(PollRegs& p, rsrc_t rs, int o0, int o1, int o2, int o3, u32 T, u32 M, u32x4 (&R)[4]) {
  u32x2 r0, r1, r2, r3, r4, r5, r6, r7; u32 cnt = SPIN_CAP;
  asm volatile(
      PV_LD4("128", "131", "132", "135", "136", "139", "140", "143")
      "s_sleep %[stag]\n"
      "1:\n\t"
      PV_LD4("144", "147", "148", "151", "152", "155", "156", "159")
      "s_waitcnt vmcnt(4)\n\t"
      PV_X4("v160", "128", "129", "130", "131") PV_X4("v161", "132", "133", "134", "135") PV_X4("v162", "136", "137", "138", "139") PV_X4("v163", "140", "141", "142", "143")
      "v_or3_b32 v160, v160, v161, v162\n\tv_or_b32 v160, v160, v163\n\t" PV_TEST
      "s_cbranch_scc1 7f\n\t"
      PV_LD4("128", "131", "132", "135", "136", "139", "140", "143")
      "s_waitcnt vmcnt(4)\n\t"
      PV_X4("v160", "144", "145", "146", "147") PV_X4("v161", "148", "149", "150", "151") PV_X4("v162", "152", "153", "154", "155") PV_X4("v163", "156", "157", "158", "159")
      "v_or3_b32 v160, v160, v161, v162\n\tv_or_b32 v160, v160, v163\n\t" PV_TEST
      "s_cbranch_scc1 8f\n\t"
      "s_sub_u32 %[cnt], %[cnt], 1\n\ts_cbranch_scc0 1b\n\t"
      "s_waitcnt vmcnt(0)\n"
      "7:\n\t"
      PV_MOV2("%[r0]", "128", "129") PV_MOV2("%[r1]", "130", "131") PV_MOV2("%[r2]", "132", "133") PV_MOV2("%[r3]", "134", "135")
      PV_MOV2("%[r4]", "136", "137") PV_MOV2("%[r5]", "138", "139") PV_MOV2("%[r6]", "140", "141") PV_MOV2("%[r7]", "142", "143")
      "s_branch 9f\n"
      "8:\n\t"
      PV_MOV2("%[r0]", "144", "145") PV_MOV2("%[r1]", "146", "147") PV_MOV2("%[r2]", "148", "149") PV_MOV2("%[r3]", "150", "151")
      PV_MOV2("%[r4]", "152", "153") PV_MOV2("%[r5]", "154", "155") PV_MOV2("%[r6]", "156", "157") PV_MOV2("%[r7]", "158", "159")
      "9:\n\t"
      : [r0] "=&v"(r0), [r1] "=&v"(r1), [r2] "=&v"(r2), [r3] "=&v"(r3), [r4] "=&v"(r4), [r5] "=&v"(r5), [r6] "=&v"(r6), [r7] "=&v"(r7), [cnt] "+s"(cnt), PV_OUT(p)
      : [o0] "v"(o0), [o1] "v"(o1), [o2] "v"(o2), [o3] "v"(o3), [rs] "s"(rs), [T] "s"(T), [M] "s"(M), [stag] "n"(PSTAG)
      : PV_CLOB);
  R[0] = u32x4{r0.x, r0.y, r1.x, r1.y}; R[1] = u32x4{r2.x, r2.y, r3.x, r3.y}; R[2] = u32x4{r4.x, r4.y, r5.x, r5.y}; R[3] = u32x4{r6.x, r6.y, r7.x, r7.y};
}
#define PV_LD2(r0, r1, r2, r3) \
  "buffer_load_dwordx4 v[" r0 ":" r1 "], %[o0], %[rs], 0 offen sc1\n\t" "buffer_load_dwordx4 v[" r2 ":" r3 "], %[o0], %[rs], 0 offen offset:16 sc1\n\t"
__device__ __forceinline__ void poll2(PollRegs& p, rsrc_t rs, int o0, u32 T, u32 M, u32x4& a, u32x4& b) {
  u32x2 r0, r1, r2, r3; u32 cnt = SPIN_CAP;
  asm volatile(
      PV_LD2("128", "131", "132", "135")
      "s_sleep %[stag]\n"
      "1:\n\t"
      PV_LD2("144", "147", "148", "151")
      "s_waitcnt vmcnt(2)\n\t"
      PV_X4("v160", "128", "129", "130", "131") PV_X4("v161", "132", "133", "134", "135") "v_or_b32 v160, v160, v161\n\t" PV_TEST
      "s_cbranch_scc1 7f\n\t"
      PV_LD2("128", "131", "132", "135")
      "s_waitcnt vmcnt(2)\n\t"
      PV_X4("v160", "144", "145", "146", "147") PV_X4("v161", "148", "149", "150", "151") "v_or_b32 v160, v160, v161\n\t" PV_TEST
      "s_cbranch_scc1 8f\n\t"
      "s_sub_u32 %[cnt], %[cnt], 1\n\ts_cbranch_scc0 1b\n\t"
      "s_waitcnt vmcnt(0)\n"
      "7:\n\t"
      PV_MOV2("%[r0]", "128", "129") PV_MOV2("%[r1]", "130", "131") PV_MOV2("%[r2]", "132", "133") PV_MOV2("%[r3]", "134", "135")
      "s_branch 9f\n"
      "8:\n\t"
      PV_MOV2("%[r0]", "144", "145") PV_MOV2("%[r1]", "146", "147") PV_MOV2("%[r2]", "148", "149") PV_MOV2("%[r3]", "150", "151")
      "9:\n\t"
      : [r0] "=&v"(r0), [r1] "=&v"(r1), [r2] "=&v"(r2), [r3] "=&v"(r3), [cnt] "+s"(cnt), PV_OUT(p)
      : [o0] "v"(o0), [rs] "s"(rs), [T] "s"(T), [M] "s"(M), [stag] "n"(PSTAG)
      : PV_CLOB);
  a = u32x4{r0.x, r0.y, r1.x, r1.y}; b = u32x4{r2.x, r2.y, r3.x, r3.y};
}
#define PV_LD3(r0, r1, r2, r3, r4) \
  "buffer_load_dwordx4 v[" r0 ":" r1 "], %[o0], %[rs], 0 offen sc1\n\t" "buffer_load_dwordx4 v[" r2 ":" r3 "], %[o0], %[rs], 0 offen offset:16 sc1\n\t" \
  "buffer_load_dword v" r4 ", %[o1], %[rs], 0 offen sc1\n\t"
__device__ __forceinline__ void poll3(PollRegs& p, rsrc_t rs, int o0, int o1, u32 T, u32 M, u32x4& a, u32x4& b, u32& c) {
  u32x2 r0, r1, r2, r3; u32 r4, cnt = SPIN_CAP;
  asm volatile(
      PV_LD3("128", "131", "132", "135", "136")
      "s_sleep %[stag]\n"
      "1:\n\t"
      PV_LD3("144", "147", "148", "151", "152")
      "s_waitcnt vmcnt(3)\n\t"
      PV_X4("v160", "128", "129", "130", "131") PV_X4("v161", "132", "133", "134", "135") "v_xor_b32 v162, %[T], v136\n\t"
      "v_or3_b32 v160, v160, v161, v162\n\t" PV_TEST
      "s_cbranch_scc1 7f\n\t"
      PV_LD3("128", "131", "132", "135", "136")
      "s_waitcnt vmcnt(3)\n\t"
      PV_X4("v160", "144", "145", "146", "147") PV_X4("v161", "148", "149", "150", "151") "v_xor_b32 v162, %[T], v152\n\t"
      "v_or3_b32 v160, v160, v161, v162\n\t" PV_TEST
      "s_cbranch_scc1 8f\n\t"
      "s_sub_u32 %[cnt], %[cnt], 1\n\ts_cbranch_scc0 1b\n\t"
      "s_waitcnt vmcnt(0)\n"
      "7:\n\t"
      PV_MOV2("%[r0]", "128", "129") PV_MOV2("%[r1]", "130", "131") PV_MOV2("%[r2]", "132", "133") PV_MOV2("%[r3]", "134", "135") "v_mov_b32 %[r4], v136\n\t"
      "s_branch 9f\n"
      "8:\n\t"
      PV_MOV2("%[r0]", "144", "145") PV_MOV2("%[r1]", "146", "147") PV_MOV2("%[r2]", "148", "149") PV_MOV2("%[r3]", "150", "151") "v_mov_b32 %[r4], v152\n\t"
      "9:\n\t"
      : [r0] "=&v"(r0), [r1] "=&v"(r1), [r2] "=&v"(r2), [r3] "=&v"(r3), [r4] "=&v"(r4), [cnt] "+s"(cnt), PV_OUT(p)
      : [o0] "v"(o0), [o1] "v"(o1), [rs] "s"(rs), [T] "s"(T), [M] "s"(M), [stag] "n"(PSTAG)
      : PV_CLOB);
  a = u32x4{r0.x, r0.y, r1.x, r1.y}; b = u32x4{r2.x, r2.y, r3.x, r3.y}; c = r4;
}

__device__ __forceinline__ unsigned long long now() { return __builtin_amdgcn_s_memrealtime(); }
__device__ __forceinline__ void not_before(unsigned long long t0, unsigned ticks) {
  while (now() - t0 < ticks) __builtin_amdgcn_s_sleep(2);
}
__device__ __forceinline__ u32 lds_ld(u32 addr) {
  u32 v;
  asm volatile("ds_read_b32 %0, %1\n\ts_waitcnt lgkmcnt(0)" : "=v"(v) : "v"(addr) : "memory");
  return v;
}
__device__ __forceinline__ void lds_st(u32 addr, u32 v) { asm volatile("ds_write_b32 %0, %1" ::"v"(addr), "v"(v) : "memory"); }
__device__ __forceinline__ void wait_word(int word, u32 want) {
  unsigned spins = 0;
#pragma clang loop unroll(disable)
  while ((u32)__builtin_amdgcn_readfirstlane(lds_ld(L_FL + 4 * word)) < want)
    if (++spins > LSPIN_CAP) break;
}
__device__ __forceinline__ void wait_word_sleep(int word, u32 want) {
  unsigned spins = 0;
#pragma clang loop unroll(disable)
  while ((u32)__builtin_amdgcn_readfirstlane(lds_ld(L_FL + 4 * word)) < want) {
    __builtin_amdgcn_s_sleep(2);
    if (++spins > LSPIN_CAP) break;
  }
}
__device__ __forceinline__ void wait_all8(int word0, u32 want, int lane) {
  unsigned spins = 0;
#pragma clang loop unroll(disable)
  while (!__all(lds_ld(L_FL + 4 * (word0 + (lane & 7))) >= want)) {
    __builtin_amdgcn_s_sleep(1);
    if (++spins > LSPIN_CAP) break;
  }
}
__device__ __forceinline__ void cbar(u32 n, int lane) {
  asm volatile("s_waitcnt lgkmcnt(0)" ::: "memory");
  if (lane == 0) asm volatile("ds_add_u32 %0, %1" ::"v"((u32)(L_FL + 4 * F_BAR)), "v"(1u) : "memory");
  wait_word(F_BAR, 8u * n);
}
__device__ __forceinline__ void hw_barrier() { __builtin_amdgcn_sched_barrier(0); __builtin_amdgcn_s_barrier(); __builtin_amdgcn_sched_barrier(0); }
__device__ __forceinline__ void wg_barrier() {
  asm volatile("s_waitcnt lgkmcnt(0)" ::: "memory");
  hw_barrier();
  asm volatile("" ::: "memory");
}
__device__ __forceinline__ void glds16(const void* sbase, u32 voff, u32 lds_dst) {
  asm volatile("s_mov_b32 m0, %2\n\ts_nop 0\n\tglobal_load_lds_dwordx4 %0, %1 nt" ::"v"(voff), "s"(sbase), "s"(lds_dst) : "memory");
}
__device__ __forceinline__ int vswz(int row) { return ((row & 3) << 2) | ((row >> 2) & 3); }
__device__ __forceinline__ int swz(int row) { return ((row >> 1) & 1) | ((row >> 2) & 2); }
__device__ __forceinline__ u32 rowoff(int q, int stride, int lane) { const int row = 8 * q + (lane >> 3); return row * stride + (((lane & 7) ^ (swz(row) << 1)) & 7) * 16; }
template <int W> __device__ __forceinline__ void tile_dma(const unsigned char* a, int stride, u32 o0, u32 o1, u32 dst) {
  glds16(a, o0, dst); if (W >= 0) waitvm<W>();
  glds16(a, o1, dst + 1024); if (W >= 0) waitvm<W>();
  glds16(a + (size_t)16 * stride, o0, dst + 2048); if (W >= 0) waitvm<W>();
  glds16(a + (size_t)16 * stride, o1, dst + 3072); if (W >= 0) waitvm<W>();
}
__device__ __forceinline__ void tile_frags_lds(const unsigned char* lds, u32 base, int lane, u32x4 (&f)[4]) {
  const int g = lane >> 4, q = (lane & 15) >> 2, p = lane & 3;
#pragma unroll
  for (int e = 0; e < 4; ++e) {
    u32x2 h[2];
#pragma unroll
    for (int t = 0; t < 2; ++t) {
      const int row = 8 * g + 4 * t + q;
      const u32 a = base + row * 128 + (((2 * e + (p >> 1)) ^ (swz(row) << 1)) << 4) + 8 * (p & 1);
      h[t] = __builtin_bit_cast(u32x2, __builtin_amdgcn_ds_read_tr16_b64_v4i16((LAS s16x4*)(lds + a)));
    }
    f[e] = u32x4{h[0].x, h[0].y, h[1].x, h[1].y};
  }
}

__device__ __forceinline__ u32x4 afrag(u32x4 a, u32x4 b) {
  u32x4 f;
  f.x = __builtin_amdgcn_perm(a.y, a.x, 0x07060302u);
  f.y = __builtin_amdgcn_perm(a.w, a.z, 0x07060302u);
  f.z = __builtin_amdgcn_perm(b.y, b.x, 0x07060302u);
  f.w = __builtin_amdgcn_perm(b.w, b.z, 0x07060302u);
  return f;
}

struct RowStats { float m, rs; };
template <int N> __device__ __forceinline__ RowStats row_stats(const float (&v)[N], const bool (&valid)[N], float pivot) {
  float s1[2] = {0.f, 0.f}, s2[2] = {0.f, 0.f};
#pragma unroll
  for (int e = 0; e < N; ++e) { const float d = valid[e] ? v[e] - pivot : 0.f; s1[e & 1] += d; s2[e & 1] = fmaf(d, d, s2[e & 1]); }
  const float m1 = wave_sum(s1[0] + s1[1]) * (1.0f / DM), m2 = wave_sum(s2[0] + s2[1]) * (1.0f / DM);
  const float var = rbf(fmax_(fmaf(-m1, m1, m2), 0.f));
  RowStats r;
  r.m = rbf(pivot + m1);
  r.rs = __builtin_amdgcn_rcpf(rbf(__builtin_amdgcn_sqrtf(rbf(var + 1e-5f))));
  return r;
}
__device__ __forceinline__ float ln_apply(float v, RowStats s) { return rbf(v - s.m) * s.rs; }

enum { ROLE_W = 0, ROLE_G = 1, ROLE_F = 2 };

__global__ void __launch_bounds__(NTHR, 3) fused_kernel(Params p) {
  extern __shared__ __attribute__((aligned(16))) unsigned char lds[];
  const int tid = threadIdx.x, lane = tid & 63, w = __builtin_amdgcn_readfirstlane(tid >> 6);
  const int wg = blockIdx.x, xcd = wg & 7, s = wg >> 3, j = xcd + 8 * (s >> 4), i = s & 15, b = i >> 1, half = i & 1;
  const size_t ubase = ((size_t)(b * HKV + j) * SK + (SK - WIN)) * DH;
  const int cq = i & 3, kp = i >> 2;
  const int ks0 = kp < 3 ? 4 * kp : 13, nt = kp < 2 ? 4 : 5;
  const u32 PK = 4 * nt, PV = PK + 128, PW = PK + 256;
  const int g4 = lane >> 4, c16 = lane & 15;
  const int cw = (w + 7) & 7;
  u32 xr[5];
  u32x4 xa;
  auto load_x = [&]() {
    const GAS u32* xrow = (const GAS u32*)(p.x + cw * DM);
#pragma unroll
    for (int m = 0; m < 4; ++m) xr[m] = __builtin_nontemporal_load(xrow + lane + 64 * m);
    xr[4] = __builtin_nontemporal_load(xrow + (lane & 31) + 256);
    xa = __builtin_nontemporal_load((const GAS u32x4*)(p.x + (lane & 7) * DM + 32 * (ks0 + (cw < nt ? cw : 0)) + 8 * g4));
  };
  if (w == 0) {
    if (lane < 32) lds_st(L_FL + 4 * lane, 0u);
    asm volatile("s_waitcnt lgkmcnt(0)" ::: "memory");
  }
  hw_barrier();

  if (w == 0) {
    __builtin_amdgcn_s_setprio(3);
    u32 pub = 0;
#define PUBLISH(W) lds_st(L_FL + 4 * F_LANDED, pub - (W))
    {
      const unsigned char* a = (const unsigned char*)(p.wq + (size_t)32 * ks0 * NQ + 256 * j + 64 * cq);
      const u32 o0 = rowoff(0, NQ * 2, lane), o1 = rowoff(1, NQ * 2, lane);
#pragma unroll
      for (int t = 0; t < 4; ++t) tile_dma<-1>(a + (size_t)t * 32 * NQ * 2, NQ * 2, o0, o1, L_EXT + t * 4096);
      if (nt == 5) tile_dma<-1>(a + (size_t)4 * 32 * NQ * 2, NQ * 2, o0, o1, L_QSUM);
      pub = PK;
    }
    {
      const unsigned char* kb = (const unsigned char*)(p.kc + ubase + (size_t)half * 512 * DH);
      u32 koff[4];
#pragma unroll
      for (int q = 0; q < 4; ++q) { const int key = 4 * q + g4; koff[q] = key * 256 + ((c16 ^ key) & 15) * 16; }
      auto kseg = [&](int t0, int t1) {
#pragma clang loop unroll(disable)
        for (int t = t0; t < t1; ++t) {
#pragma unroll
          for (int q = 0; q < 4; ++q) { glds16(kb + t * 4096, koff[q], (t * 4 + q) * 1024); waitvm<LWIN>(); }
          pub += 4;
          if (pub > LWIN) PUBLISH(LWIN);
        }
      };
      kseg(0, 32);
    }
    wait_word_sleep(F_RANK, 1u);
    const int rank = (int)__builtin_amdgcn_readfirstlane(lds_ld(L_FL + 4 * F_RANK)) - 1;
    const int role = rank < 72 ? ROLE_G : (rank < 216 ? ROLE_W : ROLE_F);
    const u32 go0 = rowoff(0, FF * 2, lane), go1 = rowoff(1, FF * 2, lane);
    auto gtile = [&](int t) -> const unsigned char* { return (const unsigned char*)(((t & 1) ? p.wu : p.wg) + (size_t)(t >> 1) * 32 * FF + 64 * rank); };
    {
      const unsigned char* vb = (const unsigned char*)(p.vc + ubase + (size_t)half * 512 * DH);
      u32 voff[4];
#pragma unroll
      for (int q = 0; q < 4; ++q) { const int key = 4 * q + g4; voff[q] = key * 256 + ((c16 ^ vswz(key)) & 15) * 16; }
      wait_word(F_QPUB, 1u);
#pragma clang loop unroll(disable)
      for (int t = 0; t < 32; ++t) {
        if ((t & 7) == 4) wait_all8(F_KDONE, (t >> 3) + 1, lane);
        const u32 dst = t < 4 ? L_EXT + t * 4096 : (t - 4) * 4096;
#pragma unroll
        for (int q = 0; q < 4; ++q) { glds16(vb + t * 4096, voff[q], dst + q * 1024); waitvm<LWIN>(); }
        pub += 4; PUBLISH(LWIN);
        if (role == ROLE_G && t >= 32 - GEARLY) {
          const int tau = t - (32 - GEARLY);
          if (tau == 0) wait_all8(F_VDONE, 1, lane);
          tile_dma<LWIN>(gtile(tau), FF * 2, go0, go1, tau < 4 ? L_EXT + tau * 4096 : (tau - 4) * 4096);
          pub += 4; PUBLISH(LWIN);
        }
      }
    }
    if (role == ROLE_W) {
      const int jw = (rank - 72) / 9, cg = (rank - 72) % 9;
      const unsigned char* a = (const unsigned char*)(p.wo + (size_t)256 * jw * DM + 64 * cg);
      const u32 o0 = rowoff(0, DM * 2, lane), o1 = rowoff(1, DM * 2, lane);
      wait_all8(F_VDONE, 1, lane);
#pragma clang loop unroll(disable)
      for (int t = 0; t < 8; ++t) { tile_dma<-1>(a + (size_t)t * 32 * DM * 2, DM * 2, o0, o1, t * 4096); pub += 4; }
    } else if (role == ROLE_G) {
      static_assert(GEARLY >= 1 && 80 - GWIN >= 4 * GEARLY - 4, "the last tile of V is published before the loader waits for its consumption");
#pragma clang loop unroll(disable)
      for (int n = GEARLY; n < 36; ++n) {
        if (n == 20) wait_all8(F_VDONE, 2, lane);
        const int t = n < 16 ? n : (n < 20 ? n + 16 : n - 4);
        tile_dma<GWIN>(gtile(t), FF * 2, go0, go1, t < 4 ? L_EXT + t * 4096 : (t - 4) * 4096);
        pub += 4;
        if (4 * (n - GEARLY) + 4 > GWIN - LWIN) PUBLISH(GWIN);
      }
    }
#define DR(N) if ((role == ROLE_G ? GWIN : (role == ROLE_W ? LWIN + 32 : LWIN)) > N) { waitvm<N>(); PUBLISH(N); }
    static_assert(LWIN + 32 <= 60 && GWIN <= 60, "vmcnt");
    DR(56) DR(52) DR(48) DR(44) DR(40) DR(36) DR(32) DR(28) DR(24) DR(20) DR(16) DR(12) DR(8) DR(4) DR(0)
#undef DR
#undef PUBLISH
    return;
  }

  load_x();
  const int ct = tid - 64;
  const rsrc_t rs = __builtin_amdgcn_make_buffer_rsrc((void*)p.ws, 0, WS_END, 0x00020000);
  const u32 T = *(const __attribute__((address_space(4))) u32*)(p.ws + WS_EPOCH) + 1u;
  const u32 T16 = T & 0xFFFFu, T8 = T & 0xFFu, T4 = T & 0xFu;
  const u32 tstart = (u32)now();
  if (ct == 448) __builtin_amdgcn_raw_buffer_store_b64(u32x2{tstart, T}, rs, WS_RANK + wg * 8, 0, AUX_SC1);
  float* const lf = (float*)lds;
  u32* const lu = (u32*)lds;
  {
    float v[10]; bool valid[10];
#pragma unroll
    for (int m = 0; m < 5; ++m) { valid[2 * m] = valid[2 * m + 1] = (m < 4) || (lane < 32); v[2 * m] = bflo(xr[m]); v[2 * m + 1] = bfhi(xr[m]); }
    const float pivot = __uint_as_float(__builtin_amdgcn_readfirstlane(xr[0]) << 16);
    const RowStats st = row_stats<10>(v, valid, pivot);
    if (lane == 0) *(f32x2*)(lf + L_ST / 4 + 2 * cw) = f32x2{st.m, st.rs};
    cbar(1, lane);
  }
  if (cw < nt) {
    const f32x2 sv = *(const f32x2*)(lf + L_ST / 4 + 2 * (lane & 7));
    const RowStats st = {sv.x, sv.y};
    u32x4 a;
#pragma unroll
    for (int q = 0; q < 4; ++q) a[q] = pack2(ln_apply(bflo(xa[q]), st), ln_apply(bfhi(xa[q]), st));
    const u32 base = cw < 4 ? L_EXT + cw * 4096 : L_QSUM;
    wait_word(F_LANDED, 4 * (cw + 1));
    u32x4 f[4]; tile_frags_lds(lds, base, lane, f);
    f32x4 d[4];
#pragma unroll
    for (int e = 0; e < 4; ++e) d[e] = mfma(f[e], a, f32x4{0.f, 0.f, 0.f, 0.f});
    if (c16 < 8) {
#pragma unroll
      for (int e = 0; e < 4; ++e) *(f32x4*)(lf + base / 4 + c16 * RS + 16 * e + 4 * g4) = d[e];
    }
  }
  cbar(2, lane);
  {
    float sum = (lf[L_EXT / 4 + cw * RS + lane] + lf[L_EXT / 4 + 1024 + cw * RS + lane]) + (lf[L_EXT / 4 + 2048 + cw * RS + lane] + lf[L_EXT / 4 + 3072 + cw * RS + lane]);
    if (nt == 5) sum += lf[L_QSUM / 4 + cw * RS + lane];
    const u32 v = put_tag<0xFu>(sum, T4);
    const u32x4 o = {v, (u32)__builtin_amdgcn_mov_dpp((int)v, 0x55, 0xf, 0xf, true), (u32)__builtin_amdgcn_mov_dpp((int)v, 0xAA, 0xf, 0xf, true),
                     (u32)__builtin_amdgcn_mov_dpp((int)v, 0xFF, 0xf, 0xf, true)};
    if ((lane & 3) == 0) st16c(rs, WS_QS + ((((j * 8 + cw) * 4 + kp) * 256) + 64 * cq + lane) * 4, o);
  }
  cbar(3, lane);
  if (ct == 0) lds_st(L_FL + 4 * F_QPUB, 1u);
  if (cw >= 2 && cw < 6) {
    u32x4 qg;
    __builtin_amdgcn_s_sleep(QDELAY);
    {
      unsigned spins = 0;
#pragma clang loop unroll(disable)
      for (;;) {
        qg = ld16c(rs, WS_QS + (((j * 8 + b) * 4 + (cw - 2)) * 256 + 4 * lane) * 4);
        if (__all(tag_ok<0xFu>(qg, T4)) || ++spins > SPIN_CAP) break;
        __builtin_amdgcn_s_sleep(4);
      }
    }
    *(f32x4*)(lf + L_QSUM / 4 + (cw - 2) * 256 + 4 * lane) = f32x4{get_tag<0xFu>(qg.x), get_tag<0xFu>(qg.y), get_tag<0xFu>(qg.z), get_tag<0xFu>(qg.w)};
  }
  if (cw == 7) {
    u32x4 e0, e1; unsigned spins = 0;
#pragma clang loop unroll(disable)
    for (;;) {
      e0 = ld16c(rs, WS_RANK + lane * 32); e1 = ld16c(rs, WS_RANK + lane * 32 + 16);
      if (__all((e0.y == T) & (e0.w == T) & (e1.y == T) & (e1.w == T)) || ++spins > SPIN_CAP) break;
      __builtin_amdgcn_s_sleep(4);
    }
    auto before = [&](u32 t, int g) -> float { const int d = (int)(t - tstart); return (d < 0 || (d == 0 && g < wg)) ? 1.f : 0.f; };
    const float n = wave_sum((before(e0.x, 4 * lane) + before(e0.z, 4 * lane + 1)) + (before(e1.x, 4 * lane + 2) + before(e1.z, 4 * lane + 3)));
    if (lane == 0) lds_st(L_FL + 4 * F_RANK, (u32)n + 1u);
  }
  cbar(4, lane);
  if (cw == 0) {
    f32x4 sv[4];
#pragma unroll
    for (int q = 0; q < 4; ++q) sv[q] = *(const f32x4*)(lf + L_QSUM / 4 + q * 256 + 4 * lane);
    const f32x4 sum = (sv[0] + sv[1]) + (sv[2] + sv[3]);
    *(u32x2*)(lu + L_Q / 4 + 2 * lane) = u32x2{pack2(sum.x, sum.y), pack2(sum.z, sum.w)};
    asm volatile("s_waitcnt lgkmcnt(0)" ::: "memory");
    if (lane == 0) lds_st(L_FL + 4 * F_QRDY, 1u);
  } else {
    wait_word(F_QRDY, 1u);
  }
  float s0, s1;
  {
    u32x4 qa[4];
#pragma unroll
    for (int ks = 0; ks < 4; ++ks) qa[ks] = *(const u32x4*)(lu + L_Q / 4 + (lane & 1) * 64 + 16 * ks + 4 * g4);
    f32x4 d[4];
#pragma unroll
    for (int m = 0; m < 4; ++m) {
      const int tile = cw + 8 * m;
      wait_word_sleep(F_LANDED, PK + 4 * (tile + 1));
      u32x4 kf[4];
#pragma unroll
      for (int ks = 0; ks < 4; ++ks) kf[ks] = *(const u32x4*)(lds + tile * 4096 + c16 * 256 + (((4 * ks + g4) ^ c16) << 4));
      f32x4 acc = mfma(qa[0], kf[0], f32x4{0.f, 0.f, 0.f, 0.f});
      acc = mfma(qa[1], kf[1], acc); acc = mfma(qa[2], kf[2], acc); acc = mfma(qa[3], kf[3], acc);
      d[m] = acc;
      asm volatile("s_waitcnt lgkmcnt(0)" ::: "memory");
      if (lane == 0) lds_st(L_FL + 4 * (F_KDONE + cw), m + 1);
    }
    const float x0 = g4 == 0 ? d[0][0] : g4 == 1 ? d[1][0] : g4 == 2 ? d[2][0] : d[3][0];
    const float x1 = g4 == 0 ? d[0][1] : g4 == 1 ? d[1][1] : g4 == 2 ? d[2][1] : d[3][1];
    constexpr float SCALE = 0.08838834764831845f;
    s0 = rbf(rbf(x0) * SCALE); s1 = rbf(rbf(x1) * SCALE);
  }
  u32x4 mine;
  {
    constexpr float L2E = 1.4426950408889634f;
    const float m0 = wave_max(s0), m1 = wave_max(s1);
    const float l0 = wave_sum(__builtin_amdgcn_exp2f((s0 - m0) * L2E)), l1 = wave_sum(__builtin_amdgcn_exp2f((s1 - m1) * L2E));
    if (lane == 0) *(f32x4*)(lf + L_ST / 4 + 16 + 4 * cw) = f32x4{m0, m1, l0, l1};
    cbar(5, lane);
    float M0 = -INFINITY, M1 = -INFINITY;
    f32x4 st[8];
#pragma unroll
    for (int ww = 0; ww < 8; ++ww) { st[ww] = *(const f32x4*)(lf + L_ST / 4 + 16 + 4 * ww); M0 = fmax_(M0, st[ww].x); M1 = fmax_(M1, st[ww].y); }
    float L0 = 0.f, L1 = 0.f;
#pragma unroll
    for (int ww = 0; ww < 8; ++ww) {
      L0 = fmaf(st[ww].z, __builtin_amdgcn_exp2f((st[ww].x - M0) * L2E), L0);
      L1 = fmaf(st[ww].w, __builtin_amdgcn_exp2f((st[ww].y - M1) * L2E), L1);
    }
    const int kl = 16 * (cw + 8 * g4) + c16;
    __bf16* P = (__bf16*)(lds + L_P);
    P[kl] = (__bf16)__builtin_amdgcn_exp2f((s0 - M0) * L2E);
    P[512 + kl] = (__bf16)__builtin_amdgcn_exp2f((s1 - M1) * L2E);
    mine = u32x4{__float_as_uint(M0) | T16, __float_as_uint(M1) | T16, put_tag<0xFFu>(L0, T8), put_tag<0xFFu>(L1, T8)};
    if (ct == 64) st16c(rs, WS_SC + wg * 128, mine);
  }
  cbar(6, lane);
  wait_word(F_RANK, 1u);
  const int rank = (int)__builtin_amdgcn_readfirstlane(lds_ld(L_FL + 4 * F_RANK)) - 1;
  const int role = rank < 72 ? ROLE_G : (rank < 216 ? ROLE_W : ROLE_F);
  {
    const int q4 = (lane & 15) >> 2, p4 = lane & 3;
    auto vfrag = [&](u32 base, int e) -> u32x4 {
      u32x2 h[2];
#pragma unroll
      for (int t = 0; t < 2; ++t) {
        const int row = 8 * g4 + 4 * t + q4;
        const u32 a = base + row * 256 + (((2 * e + (p4 >> 1)) ^ vswz(row)) << 4) + 8 * (p4 & 1);
        h[t] = __builtin_bit_cast(u32x2, __builtin_amdgcn_ds_read_tr16_b64_v4i16((LAS s16x4*)(lds + a)));
      }
      return u32x4{h[0].x, h[0].y, h[1].x, h[1].y};
    };
    auto pfrag = [&](int ks) -> u32x4 { return *(const u32x4*)(lds + L_P + (lane & 1) * 1024 + (32 * ks + 8 * g4) * 2); };
    {
      const u32x4 pf = pfrag(cw);
      const u32 base = cw < 2 ? L_EXT + cw * 8192 : (cw - 2) * 8192;
      wait_word(F_LANDED, PV + 8 * (cw + 1));
      u32x4 f[8];
#pragma unroll
      for (int e = 0; e < 8; ++e) f[e] = vfrag(base, e);
      SB();
      f32x4 d[8];
#pragma unroll
      for (int e = 0; e < 8; ++e) d[e] = mfma(f[e], pf, f32x4{0.f, 0.f, 0.f, 0.f});
      asm volatile("s_waitcnt lgkmcnt(0)" ::: "memory");
      if (lane == 0) lds_st(L_FL + 4 * (F_VDONE + cw), 1u);
      if (c16 < 2) {
#pragma unroll
        for (int e = 0; e < 8; ++e) *(f32x4*)(lf + L_QSUM / 4 + cw * 256 + c16 * 128 + 16 * e + 4 * g4) = d[e];
      }
    }
    cbar(7, lane);
    f32x4 acc;
    {
      f32x4 sv[8];
#pragma unroll
      for (int ww = 0; ww < 8; ++ww) sv[ww] = *(const f32x4*)(lf + L_QSUM / 4 + ww * 256 + (lane & 1) * 128 + 16 * cw + 4 * g4);
      acc = ((sv[0] + sv[1]) + (sv[2] + sv[3])) + ((sv[4] + sv[5]) + (sv[6] + sv[7]));
    }
    u32x4 oth = {0u, 0u, 0u, 0u};
    bool got = cw != 1;
    if (cw == 1) oth = ld16c(rs, WS_SC + (wg ^ 8) * 128);
    auto try_stats = [&](bool last) {
      if (got) return;
      unsigned spins = 0;
#pragma clang loop unroll(disable)
      for (;;) {
        if (__all(((((oth.x ^ T16) | (oth.y ^ T16)) & 0xFFFFu) | (((oth.z ^ T8) | (oth.w ^ T8)) & 0xFFu)) == 0u) || ++spins > SPIN_CAP) { got = true; break; }
        oth = ld16c(rs, WS_SC + (wg ^ 8) * 128);
        if (!last) return;
      }
      constexpr float L2E = 1.4426950408889634f;
      const float M0 = bfhi(mine.x), M1 = bfhi(mine.y), om0 = bfhi(oth.x), om1 = bfhi(oth.y);
      const float G0 = fmax_(M0, om0), G1 = fmax_(M1, om1);
      const float e0 = __builtin_amdgcn_exp2f((M0 - G0) * L2E), e1 = __builtin_amdgcn_exp2f((M1 - G1) * L2E);
      const float S0 = get_tag<0xFFu>(mine.z) * e0 + get_tag<0xFFu>(oth.z) * __builtin_amdgcn_exp2f((om0 - G0) * L2E);
      const float S1 = get_tag<0xFFu>(mine.w) * e1 + get_tag<0xFFu>(oth.w) * __builtin_amdgcn_exp2f((om1 - G1) * L2E);
      if (lane == 0) { *(f32x2*)(lf + L_ST / 4 + 48) = f32x2{e0 / S0, e1 / S1}; asm volatile("s_waitcnt lgkmcnt(0)" ::: "memory"); lds_st(L_FL + 4 * F_FAC, 1u); }
    };
#pragma unroll
    for (int ks = 8; ks < 16; ++ks) {
      const u32x4 pf = pfrag(ks);
      const int tl = 2 * ks + 1;
      wait_word(F_LANDED, PV + 4 * (tl + 1) + ((role == ROLE_G && tl > 32 - GEARLY) ? 4 * (tl - (32 - GEARLY)) : 0));
      acc = mfma(vfrag((ks - 2) * 8192, cw), pf, acc);
      if (ks >= 9 && ks <= 13) try_stats(false);
      if (ks == 14) { try_stats(true); wait_word(F_FAC, 1u); }
    }
    const float fac = lf[L_ST / 4 + 48 + (lane & 1)];
    asm volatile("s_waitcnt lgkmcnt(0)" ::: "memory");
    if (lane == 0) lds_st(L_FL + 4 * (F_VDONE + cw), 2u);
    if (c16 < 2)
      st16c(rs, WS_OS + (((j * 8 + b) * 2 + half) * 256 + c16 * 128 + 16 * cw + 4 * g4) * 4,
            u32x4{put_tag<0xFFu>(acc.x * fac, T8), put_tag<0xFFu>(acc.y * fac, T8), put_tag<0xFFu>(acc.z * fac, T8), put_tag<0xFFu>(acc.w * fac, T8)});
  }
  auto gather4 = [&](u32 v) -> u32x4 {
    return u32x4{v, (u32)__builtin_amdgcn_mov_dpp((int)v, 0x55, 0xf, 0xf, true), (u32)__builtin_amdgcn_mov_dpp((int)v, 0xAA, 0xf, 0xf, true),
                 (u32)__builtin_amdgcn_mov_dpp((int)v, 0xFF, 0xf, 0xf, true)};
  };
  const unsigned long long to = now();

  auto acc_to_lds = [&](int base, const f32x4 (&d)[4]) {
    if (c16 < 8) {
#pragma unroll
      for (int e = 0; e < 4; ++e) *(f32x4*)(lf + base / 4 + (cw * 8 + c16) * RS + 16 * e + 4 * g4) = d[e];
    }
  };
  auto publish_partial = [&](int base, int jw, int cg) {
    float sv[8];
#pragma unroll
    for (int ww = 0; ww < 8; ++ww) sv[ww] = lf[L_RED_W / 4 + (ww * 8 + cw) * RS + lane];
    const float sum = ((sv[0] + sv[1]) + (sv[2] + sv[3])) + ((sv[4] + sv[5]) + (sv[6] + sv[7]));
    const u32x4 o = gather4(put_tag<0xFFu>(sum, T8));
    if ((lane & 3) == 0) st16c(rs, base + (((cw * 9 + cg) * 16 + jw) * 64 + lane) * 4, o);
  };

  if (role == ROLE_W) {
    const int jw = (rank - 72) / 9, cg = (rank - 72) % 9;
    u32x4 f[4];
    wait_word(F_LANDED, PW + 32);
    tile_frags_lds(lds, cw * 4096, lane, f);
    {
      const int off = WS_OS + ((jw * 8 + (lane & 7)) * 512 + 32 * cw + 8 * g4) * 4;
      u32x4 a0 = {0u, 0u, 0u, 0u}, a1 = a0, b0 = a0, b1 = a0;
      if (c16 < 8)
        POLL((a0 = ld16c(rs, off), a1 = ld16c(rs, off + 16), b0 = ld16c(rs, off + 1024), b1 = ld16c(rs, off + 1040),
              tag_ok<0xFFu>(a0, T8) & tag_ok<0xFFu>(a1, T8) & tag_ok<0xFFu>(b0, T8) & tag_ok<0xFFu>(b1, T8)));
#define S2(x, y) (get_tag<0xFFu>(x) + get_tag<0xFFu>(y))
      const u32x4 a = {pack2(S2(a0.x, b0.x), S2(a0.y, b0.y)), pack2(S2(a0.z, b0.z), S2(a0.w, b0.w)), pack2(S2(a1.x, b1.x), S2(a1.y, b1.y)), pack2(S2(a1.z, b1.z), S2(a1.w, b1.w))};
#undef S2
      f32x4 d[4];
#pragma unroll
      for (int e = 0; e < 4; ++e) d[e] = mfma(f[e], a, f32x4{0.f, 0.f, 0.f, 0.f});
      acc_to_lds(L_RED_W, d);
    }
    wg_barrier();
    publish_partial(WS_PW, jw, cg);
    const unsigned long long tw = now();
    u32x4 f2[4];
    PollRegs pr = {};
    {
      const unsigned char* a = (const unsigned char*)(p.wd + (size_t)(288 * jw + 32 * cw) * DM + 64 * cg);
      const u32 o0 = rowoff(0, DM * 2, lane), o1 = rowoff(1, DM * 2, lane);
      tile_dma<-1>(a, DM * 2, o0, o1, (8 + cw) * 4096);
      if (cw == 0) tile_dma<-1>(a + 256 * DM * 2, DM * 2, o0, o1, 16 * 4096);
      waitvm<0>();
      tile_frags_lds(lds, (8 + cw) * 4096, lane, f);
      if (cw == 0) tile_frags_lds(lds, 16 * 4096, lane, f2);
    }
    {
      const int off = WS_AS + ((lane & 7) * FF + 288 * jw + 32 * cw + 8 * g4) * 4;
      u32x4 R[4] = {};
      not_before(tw, GATE_ACT);
      if (c16 < 8) {
        if (cw == 0) poll4(pr, rs, off, off + 16, off + 1024, off + 1040, T16, 0xFFFFu, R);
        else poll2(pr, rs, off, T16, 0xFFFFu, R[0], R[1]);
      }
      const u32x4 a = afrag(R[0], R[1]);
      f32x4 d[4];
#pragma unroll
      for (int e = 0; e < 4; ++e) d[e] = mfma(f[e], a, f32x4{0.f, 0.f, 0.f, 0.f});
      if (cw == 0) {
        const u32x4 ab = afrag(R[2], R[3]);
#pragma unroll
        for (int e = 0; e < 4; ++e) d[e] = mfma(f2[e], ab, d[e]);
      }
      acc_to_lds(L_RED_W, d);
    }
    wg_barrier();
    publish_partial(WS_PD, jw, cg);
    poll_done(pr);
  } else if (role == ROLE_G) {
    const int gx = rank;
    u32x4 f[5][4];
#pragma unroll
    for (int tl = 0; tl < 5; ++tl) {
      if (tl < 4 || cw < 4) {
        const int tau = cw + 8 * tl;
        const int n = tau < 16 ? tau : (tau >= 32 ? tau - 16 : tau + 4);
        wait_word(F_LANDED, tau < GEARLY ? PV + 4 * (32 - GEARLY + tau + 1) + 4 * (tau + 1) : PW + 4 * (n + 1));
        tile_frags_lds(lds, tau < 4 ? L_EXT + tau * 4096 : (tau - 4) * 4096, lane, f[tl]);
      }
    }
    wait_word(F_LANDED, PW + 144);
    PollRegs pr;
    {
      const int off = WS_HS + (xcd * 8 + cw) * DM * 4;
      u32x4 h0, h1; u32 h2;
      not_before(to, GATE_H);
      poll3(pr, rs, off + lane * 32, off + 2048 + lane * 4, T16, 0xFFFFu, h0, h1, h2);
      float v[9]; bool valid[9];
#pragma unroll
      for (int e = 0; e < 4; ++e) { v[e] = bfhi(h0[e]); v[4 + e] = bfhi(h1[e]); }
      v[8] = bfhi(h2);
#pragma unroll
      for (int e = 0; e < 9; ++e) valid[e] = true;
      const float pivot = __uint_as_float(__builtin_amdgcn_readfirstlane(h0.x) & 0xFFFF0000u);
      const RowStats st = row_stats<9>(v, valid, pivot);
      u32* hn = lu + L_HN / 4 + cw * HN_STRIDE;
      *(u32x4*)(hn + 4 * lane) = u32x4{pack2(ln_apply(v[0], st), ln_apply(v[1], st)), pack2(ln_apply(v[2], st), ln_apply(v[3], st)),
                                       pack2(ln_apply(v[4], st), ln_apply(v[5], st)), pack2(ln_apply(v[6], st), ln_apply(v[7], st))};
      ((__bf16*)(hn + 256))[lane] = (__bf16)ln_apply(v[8], st);
    }
    wg_barrier();
    {
      f32x4 d[4];
      const u32* hn = lu + L_HN / 4 + (lane & 7) * HN_STRIDE + 16 * (cw >> 1) + 4 * g4;
      u32x4 a[5];
#pragma unroll
      for (int tl = 0; tl < 5; ++tl) a[tl] = *(const u32x4*)(hn + (tl < 4 || cw < 4 ? 64 * tl : 0));
      SB();
#pragma unroll
      for (int e = 0; e < 4; ++e) d[e] = mfma(f[0][e], a[0], f32x4{0.f, 0.f, 0.f, 0.f});
#pragma unroll
      for (int tl = 1; tl < 5; ++tl) {
        if (tl < 4 || cw < 4) {
#pragma unroll
          for (int e = 0; e < 4; ++e) d[e] = mfma(f[tl][e], a[tl], d[e]);
        }
      }
      acc_to_lds(0, d);
    }
    wg_barrier();
    {
      float gv[4], uv[4];
#pragma unroll
      for (int ww = 0; ww < 4; ++ww) { gv[ww] = lf[(2 * ww * 8 + cw) * RS + lane]; uv[ww] = lf[((2 * ww + 1) * 8 + cw) * RS + lane]; }
      const float gg = rbf((gv[0] + gv[1]) + (gv[2] + gv[3])), uu = rbf((uv[0] + uv[1]) + (uv[2] + uv[3]));
      const float sl = rbf(gg * __builtin_amdgcn_rcpf(1.0f + __builtin_amdgcn_exp2f(gg * -1.4426950408889634f)));
      const u32x4 o = gather4(bfbits_hi(sl * uu) | T16);
      if ((lane & 3) == 0) st16c(rs, WS_AS + (cw * FF + 64 * gx + lane) * 4, o);
    }
    poll_done(pr);
  } else {
    const int f = rank - 216;
    const int job = 2 * f + cw;
    if (cw < 2 && job < 72) {
      const int jb = job / 9, cg = job % 9;
      const u32x2 xq = *(const GAS u32x2*)(p.x + jb * DM + 64 * cg + 4 * c16);
      const u32x4 wr0 = *(const GAS u32x4*)(p.wr + 8 * lane);
      const u16 wr1 = ((const GAS u16*)p.wr)[512 + lane];
      PollRegs pr;
      auto gather16 = [&](int base) -> f32x4 {
        const int off = base + (((jb * 9 + cg) * 16 + g4) * 64 + 4 * c16) * 4;
        u32x4 R[4];
        poll4(pr, rs, off, off + 1024, off + 2048, off + 3072, T8, 0xFFu, R);
        const u32x4 q0 = R[0], q1 = R[1], q2 = R[2], q3 = R[3];
        f32x4 sum;
#pragma unroll
        for (int e = 0; e < 4; ++e) {
          float t = (get_tag<0xFFu>(q0[e]) + get_tag<0xFFu>(q1[e])) + (get_tag<0xFFu>(q2[e]) + get_tag<0xFFu>(q3[e]));
          t = swap16_add(t, t); t = swap32_add(t, t);
          sum[e] = t;
        }
        return sum;
      };
      not_before(to, GATE_PW);
      const f32x4 at = gather16(WS_PW);
      f32x4 hv;
      hv[0] = rbf(bflo(xq.x) + rbf(at[0])); hv[1] = rbf(bfhi(xq.x) + rbf(at[1]));
      hv[2] = rbf(bflo(xq.y) + rbf(at[2])); hv[3] = rbf(bfhi(xq.y) + rbf(at[3]));
      {
        const u32x4 hq = {__float_as_uint(hv[0]) | T16, __float_as_uint(hv[1]) | T16, __float_as_uint(hv[2]) | T16, __float_as_uint(hv[3]) | T16};
        const int o = WS_HS + ((g4 * 8 + jb) * DM + 64 * cg + 4 * c16) * 4;
        st16c(rs, o, hq); st16c(rs, o + 4 * 8 * DM * 4, hq);
      }
      const unsigned long long th = now();
      poll_done(pr);
      RowStats st; float sig;
      {
        const int off = WS_HS + (xcd * 8 + jb) * DM * 4;
        u32x4 h0, h1; u32 h2;
        {
          unsigned spins = 0;
#pragma clang loop unroll(disable)
          for (;;) {
            h0 = ld16c(rs, off + lane * 32); h1 = ld16c(rs, off + lane * 32 + 16); h2 = ld4c(rs, off + 2048 + lane * 4);
            if (__all(tag_ok<0xFFFFu>(h0, T16) & tag_ok<0xFFFFu>(h1, T16) & ((h2 & 0xFFFFu) == T16)) || ++spins > SPIN_CAP) break;
            __builtin_amdgcn_s_sleep(2);
          }
        }
        float v[9]; bool valid[9];
#pragma unroll
        for (int e = 0; e < 4; ++e) { v[e] = bfhi(h0[e]); v[4 + e] = bfhi(h1[e]); }
        v[8] = bfhi(h2);
#pragma unroll
        for (int e = 0; e < 9; ++e) valid[e] = true;
        const float pivot = __uint_as_float(__builtin_amdgcn_readfirstlane(h0.x) & 0xFFFF0000u);
        st = row_stats<9>(v, valid, pivot);
        float r = rbf(ln_apply(v[8], st)) * bflo((u32)wr1);
#pragma unroll
        for (int e = 0; e < 8; ++e) r = fmaf(rbf(ln_apply(v[e], st)), (e & 1) ? bfhi(wr0[e >> 1]) : bflo(wr0[e >> 1]), r);
        r = rbf(wave_sum(r));
        sig = rbf(__builtin_amdgcn_rcpf(1.0f + __builtin_amdgcn_exp2f(r * -1.4426950408889634f)));
      }
      const float hsel = g4 == 0 ? hv[0] : g4 == 1 ? hv[2] : g4 == 2 ? hv[1] : hv[3];
      const float hnsel = rbf(ln_apply(hsel, st));
      u16* const op = p.out + jb * DM + 64 * cg + 4 * c16 + (((g4 & 1) << 1) | (g4 >> 1));
      not_before(th, GATE_PD);
      {
        const int off = WS_PD + (((jb * 9 + cg) * 16 + g4) * 64 + 4 * c16) * 4;
        u32x4 R[4];
        poll4(pr, rs, off, off + 1024, off + 2048, off + 3072, T8, 0xFFu, R);
        float t[4];
#pragma unroll
        for (int e = 0; e < 4; ++e) t[e] = (get_tag<0xFFu>(R[0][e]) + get_tag<0xFFu>(R[1][e])) + (get_tag<0xFFu>(R[2][e]) + get_tag<0xFFu>(R[3][e]));
        const float y = swap16_add(swap32_add(t[0], t[1]), swap32_add(t[2], t[3]));
        *(GAS u16*)op = (u16)(bfbits_hi(hsel + rbf(hnsel + rbf(rbf(y) * sig))) >> 16);
      }
      poll_done(pr);
      if (job == 0 && lane == 0) __hip_atomic_store((GAS u32*)(p.ws + WS_EPOCH), T, __ATOMIC_RELAXED, __HIP_MEMORY_SCOPE_AGENT);
    }
  }
}

}

static inline size_t ws_offset(size_t ws_size) { return (ws_size - (8u << 20)) & ~(size_t)4095; }
extern "C" void kernel_launch(void* const* d_in, const int* in_sizes, int n_in, void* d_out, int out_size, void* d_ws, size_t ws_size,
                              hipStream_t stream) {
  Params p;
  p.x = (const u16*)d_in[0];
  p.kc = (const u16*)d_in[1];
  p.vc = (const u16*)d_in[2];
  p.wq = (const u16*)d_in[3];
  p.wo = (const u16*)d_in[4];
  p.wr = (const u16*)d_in[6];
  p.wg = (const u16*)d_in[7];
  p.wu = (const u16*)d_in[8];
  p.wd = (const u16*)d_in[9];
  p.out = (u16*)d_out;
  p.ws = (unsigned char*)d_ws + ws_offset(ws_size);
  (void)hipFuncSetAttribute((const void*)fused_kernel, hipFuncAttributeMaxDynamicSharedMemorySize, LDS_BYTES);
  hipLaunchKernelGGL(fused_kernel, dim3(NWG), dim3(NTHR), LDS_BYTES, stream, p);
}
```
